```python
import torch

def _softmax(x, axis=-1):
    return torch.softmax(x, dim=axis)

def _gelu(x):
    return torch.nn.functional.gelu(x, approximate='tanh')

def _silu(x):
    return torch.nn.functional.silu(x)

def _sigmoid(x):
    return torch.sigmoid(x)

def _layernorm(x, axis=-1, eps=1e-05):
    m = x.mean(dim=axis, keepdim=True)
    v = x.var(dim=axis, keepdim=True, unbiased=False)
    return (x - m) / torch.sqrt(v + eps)

def _scatter_add(tgt, idx, src, axis=0):
    return tgt.clone().index_add_(axis, idx, src)

def _segment_sum(src, seg, n):
    out = torch.zeros((n,) + src.shape[1:], dtype=src.dtype)
    return out.index_add_(0, seg, src)

def _pad(x, pw):
    flat = tuple((v for pair in reversed(pw) for v in pair))
    return torch.nn.functional.pad(x, flat)
kernel_name = 'adaLN_cond__spectral_fnet__dense_activation_relu__prompt'

def setup_inputs():

    _w = lambda a: (lambda t: t.to(torch.bfloat16) if t.is_floating_point() else t)(torch.as_tensor(a))

    def _A_setup():
        B, S, D = (1, 2048, 640)
        ns = torch.arange(S)[:, None] * torch.arange(S)[None, :]
        nd = torch.arange(D)[:, None] * torch.arange(D)[None, :]
        Ws_re = torch.cos(-2.0 * torch.pi * ns / S).to(float)
        Ws_im = torch.sin(-2.0 * torch.pi * ns / S).to(float)
        Wd_re = torch.cos(-2.0 * torch.pi * nd / D).to(float)
        Wd_im = torch.sin(-2.0 * torch.pi * nd / D).to(float)
        return {'x': _w((lambda *s: torch.randn(*s, dtype=torch.bfloat16) * 0.1)(B, S, D)), 'dft_seq_re': _w(Ws_re), 'dft_seq_im': _w(Ws_im), 'dft_hid_re': _w(Wd_re), 'dft_hid_im': _w(Wd_im)}
    _A_in = _A_setup()

    def _B_setup():
        B, S, D, Dff = (1, 2048, 640, 1536)
        return {'x': _w((lambda *s: torch.randn(*s, dtype=torch.bfloat16) * 0.1)(B, S, D)), 'w1': _w((lambda *s: torch.randn(*s, dtype=torch.bfloat16) * 3.2)(D, Dff)), 'w2': _w((lambda *s: torch.randn(*s, dtype=torch.bfloat16) * 3.2)(Dff, D))}
    _B_in = _B_setup()
    return {'A_x': _A_in['x'], 'A_dft_seq_re': _A_in['dft_seq_re'], 'A_dft_seq_im': _A_in['dft_seq_im'], 'A_dft_hid_re': _A_in['dft_hid_re'], 'A_dft_hid_im': _A_in['dft_hid_im'], 'B_x': _B_in['x'], 'B_w1': _B_in['w1'], 'B_w2': _B_in['w2'], 'c': _w((lambda *s: torch.randn(*s, dtype=torch.bfloat16) * 0.4)(1, 640) * 8.0), 'w_mod': _w((lambda *s: torch.randn(*s, dtype=torch.bfloat16) * 0.4)(640, 6 * 640) * 640 ** (-0.5) * 8.0)}

def reference(A_x, A_dft_seq_re, A_dft_seq_im, A_dft_hid_re, A_dft_hid_im, B_x, B_w1, B_w2, c, w_mod):

    def _A_ref(x, dft_seq_re, dft_seq_im, dft_hid_re, dft_hid_im):
        a = torch.einsum('bsd,ed->bse', x, dft_hid_re)
        b = torch.einsum('bsd,ed->bse', x, dft_hid_im)
        y = torch.einsum('ts,bse->bte', dft_seq_re, a) - torch.einsum('ts,bse->bte', dft_seq_im, b)
        return y

    def _B_ref(x, w1, w2):
        h = torch.einsum('bsd,df->bsf', x, w1)
        zero = torch.zeros_like(h)
        h = torch.maximum(h, zero)
        return torch.einsum('bsf,fd->bsd', h, w2)
    x = A_x
    mod = (_silu(c) @ w_mod).reshape(c.shape[0], 6, 1, -1)
    sa, ca, ga, sb, cb, gb = (mod[:, 0].clone(), mod[:, 1].clone(), mod[:, 2].clone(), mod[:, 3].clone(), mod[:, 4].clone(), mod[:, 5].clone())
    h = _layernorm(x, axis=-1) * (1 + ca) + sa
    x = x + ga * _A_ref(h, A_dft_seq_re, A_dft_seq_im, A_dft_hid_re, A_dft_hid_im)
    h = _layernorm(x, axis=-1) * (1 + cb) + sb
    x = x + gb * _B_ref(h, B_w1, B_w2)
    return x
```

```cpp
#include <hip/hip_runtime.h>
#include <hip/hip_bf16.h>
#include <stdint.h>

typedef __attribute__((ext_vector_type(8))) __bf16 bf16x8;
typedef __attribute__((ext_vector_type(4))) __bf16 bf16x4;
typedef __attribute__((ext_vector_type(2))) __bf16 bf16x2;
typedef __attribute__((ext_vector_type(4))) float f32x4;
typedef __attribute__((ext_vector_type(2))) float f32x2;
typedef __attribute__((ext_vector_type(4))) short s16x4;
typedef __attribute__((ext_vector_type(8))) short s16x8;
typedef __attribute__((ext_vector_type(4))) unsigned u32x4;
typedef __attribute__((ext_vector_type(2))) unsigned u32x2;
typedef __attribute__((address_space(3))) s16x4* lds_s16x4_ptr;
typedef __attribute__((address_space(3))) void* lds_vptr;

constexpr int S_ = 2048, D_ = 640, F_ = 1536, MODN = 3840;

constexpr size_t WS_MODP = 0;
constexpr size_t WS_HT   = 65536;
constexpr size_t WS_PQ   = WS_HT + (size_t)D_ * S_ * 2;
constexpr size_t WS_X1   = WS_PQ + (size_t)S_ * 1280 * 2;
constexpr size_t WS_H1   = WS_X1 + (size_t)S_ * D_ * 2;

template <int V> struct IC { static constexpr int value = V; };

__device__ __forceinline__ float bf2f(unsigned short u) { return __uint_as_float(((unsigned)u) << 16); }
__device__ __forceinline__ float bfr(float f) { return (float)(__bf16)f; }
__device__ __forceinline__ unsigned pk2(float a, float b) {
  bf16x2 v = __builtin_convertvector((f32x2){a, b}, bf16x2);
  return __builtin_bit_cast(unsigned, v);
}
__device__ __forceinline__ float lo16(unsigned u) { return __uint_as_float(u << 16); }
__device__ __forceinline__ float hi16(unsigned u) { return __uint_as_float(u & 0xffff0000u); }

__device__ __forceinline__ void dma16(unsigned voff, const char* sbase, unsigned lds) {
  asm volatile("s_mov_b32 m0, %2\n\ts_nop 0\n\tglobal_load_lds_dwordx4 %0, %1"
               ::"v"(voff), "s"(sbase), "s"(lds) : "memory");
}
__device__ __forceinline__ void dma16h(unsigned voff, const char* sbase, unsigned lds) {
  asm volatile("s_mov_b32 m0, %2\n\ts_mov_b32 exec_hi, 0\n\tglobal_load_lds_dwordx4 %0, %1\n\ts_mov_b32 exec_hi, -1"
               ::"v"(voff), "s"(sbase), "s"(lds) : "memory");
}
__device__ __forceinline__ unsigned lds_off(const void* p) {
  return (unsigned)(uintptr_t)(__attribute__((address_space(3))) const char*)p;
}
#define SB0() __builtin_amdgcn_sched_barrier(0)
template <int N> __device__ __forceinline__ void wait_vm_lgkm0() {
  asm volatile("s_waitcnt vmcnt(%0) lgkmcnt(0)" ::"n"(N) : "memory");
}
__device__ __forceinline__ void bar() {
  asm volatile("" ::: "memory");
  __builtin_amdgcn_s_barrier();
  asm volatile("" ::: "memory");
}
__device__ __forceinline__ bf16x8 lds_frag(const char* p) { return *(const bf16x8*)p; }
__device__ __forceinline__ s16x4 lds_tr(const char* p) {
  return __builtin_amdgcn_ds_read_tr16_b64_v4i16((lds_s16x4_ptr)p);
}
__device__ __forceinline__ bf16x8 cat_frag(s16x4 a, s16x4 b) {
  s16x8 v = __builtin_shufflevector(a, b, 0, 1, 2, 3, 4, 5, 6, 7);
  return __builtin_bit_cast(bf16x8, v);
}
#define MFMA16(a, b, c) __builtin_amdgcn_mfma_f32_16x16x32_bf16((a), (b), (c), 0, 0, 0)

template <int NS, int NK, int P, int TI, class Body>
__device__ __forceinline__ void pipe_tail(int& rd, Body& body) {
  if constexpr (TI < NS) {
    if constexpr (TI < NS - 1) {
      wait_vm_lgkm0<P*(NS - 2 - TI)>();
      bar();
      SB0();
      body(IC<TI & 1>{}, IC<0>{}, IC<1>{}, rd, 0, 0);
      rd = (rd + 1 == NS) ? 0 : rd + 1;
    } else {
      SB0();
      body(IC<TI & 1>{}, IC<0>{}, IC<0>{}, 0, 0, 0);
    }
    SB0();
    pipe_tail<NS, NK, P, TI + 1>(rd, body);
  }
}
template <int NS, int NK, int P, class Issue, class Pre, class Load0, class Body>
__device__ __forceinline__ void pipeline(Issue issue, Pre pre, Load0 load0, Body body) {
  static_assert(((NK - NS) & 1) == 0 && NK >= NS, "bad pipeline config");
#pragma unroll
  for (int s = 0; s < NS; ++s) issue(s, s);
  pre();
  wait_vm_lgkm0<P*(NS - 1)>();
  bar();
  load0();
  int rd = 1, wr = 0;
  for (int i = 0; i < NK - NS; i += 2) {
    wait_vm_lgkm0<P*(NS - 2)>();
    bar();
    SB0();
    body(IC<0>{}, IC<1>{}, IC<1>{}, rd, i + NS, wr);
    SB0();
    rd = (rd + 1 == NS) ? 0 : rd + 1;
    wr = (wr + 1 == NS) ? 0 : wr + 1;
    wait_vm_lgkm0<P*(NS - 2)>();
    bar();
    SB0();
    body(IC<1>{}, IC<1>{}, IC<1>{}, rd, i + 1 + NS, wr);
    SB0();
    rd = (rd + 1 == NS) ? 0 : rd + 1;
    wr = (wr + 1 == NS) ? 0 : wr + 1;
  }
  pipe_tail<NS, NK, P, 0>(rd, body);
}

__global__ __launch_bounds__(256) void k1_ln_mod(const unsigned short* __restrict__ x,
                                                 const unsigned short* __restrict__ c,
                                                 const unsigned short* __restrict__ wmod,
                                                 char* __restrict__ ws) {
  __shared__ __attribute__((aligned(16))) char smem[16 * 1296];
  const int tid = threadIdx.x, lane = tid & 63;
  const int bid = blockIdx.x;
  if (bid < 128) {
    const int xcd = bid & 7, i = bid >> 3;
    const int rg = xcd * 4 + (i >> 2), sub = i & 3;
    const int row0 = rg * 64 + sub * 16;
    const int r = tid >> 4, j = tid & 15;
    const u32x4* src = (const u32x4*)(x + (size_t)(row0 + r) * D_);
    u32x4 v[5];
#pragma unroll
    for (int q = 0; q < 5; ++q) v[q] = src[j + 16 * q];
    float f[40];
#pragma unroll
    for (int q = 0; q < 5; ++q)
#pragma unroll
      for (int e = 0; e < 4; ++e) { f[q * 8 + 2 * e] = lo16(v[q][e]); f[q * 8 + 2 * e + 1] = hi16(v[q][e]); }
    float s = 0.f;
#pragma unroll
    for (int e = 0; e < 40; ++e) s += f[e];
#pragma unroll
    for (int o = 1; o < 16; o <<= 1) s += __shfl_xor(s, o, 64);
    const float mean = s * (1.0f / D_);
    float s2 = 0.f;
#pragma unroll
    for (int e = 0; e < 40; ++e) { float d = f[e] - mean; s2 += d * d; }
#pragma unroll
    for (int o = 1; o < 16; o <<= 1) s2 += __shfl_xor(s2, o, 64);
    const float mb = bfr(mean);
    const float vb = bfr(s2 * (1.0f / D_));
    const float den = bfr(sqrtf(bfr(vb + 1e-5f)));
    const float rden = 1.0f / den;
    (void)rden;
#pragma unroll
    for (int q = 0; q < 5; ++q) {
      u32x4 o;
#pragma unroll
      for (int e = 0; e < 4; ++e) {
        float a = bfr(f[q * 8 + 2 * e] - mb) / den;
        float b = bfr(f[q * 8 + 2 * e + 1] - mb) / den;
        o[e] = pk2(a, b);
      }
      *(u32x4*)(smem + r * 1296 + (j + 16 * q) * 16) = o;
    }
    __syncthreads();
    const int grp = tid >> 4, li = tid & 15, qq = li >> 2, pp = li & 3;
    char* ht = ws + WS_HT;
#pragma unroll
    for (int it = 0; it < 5; ++it) {
      const int pr = it * 16 + grp;
      const int cblk = pr >> 1, half = pr & 1;
      const char* a0 = smem + (8 * half + qq) * 1296 + (16 * cblk + 4 * pp) * 2;
      s16x4 t0 = lds_tr(a0);
      s16x4 t1 = lds_tr(a0 + 4 * 1296);
      s16x8 o = __builtin_shufflevector(t0, t1, 0, 1, 2, 3, 4, 5, 6, 7);
      const int d = 16 * cblk + li;
      const int cb = d / 80, rr = d - cb * 80;
      const int chunk = 2 * sub + half;
      const int pos = chunk ^ ((rr >> 1) & 7);
      *(s16x8*)(ht + ((size_t)(cb * 32 + rg) * 80 + rr) * 128 + pos * 16) = o;
    }
  } else {
    const int g = bid - 128;
    const int cg = g % 60, kq = g / 60;
    const int l8 = tid & 7, rg = tid >> 3;
    u32x4 v[5];
    float sv[5];
#pragma unroll
    for (int q = 0; q < 5; ++q) {
      const int k = kq * 160 + q * 32 + rg;
      v[q] = *(const u32x4*)(wmod + (size_t)k * MODN + cg * 64 + l8 * 8);
      float cv = bf2f(c[k]);
      sv[q] = bfr(cv / (1.0f + __expf(-cv)));
    }
    float acc[8];
#pragma unroll
    for (int e = 0; e < 8; ++e) acc[e] = 0.f;
#pragma unroll
    for (int q = 0; q < 5; ++q)
#pragma unroll
      for (int e = 0; e < 4; ++e) {
        acc[2 * e] += sv[q] * lo16(v[q][e]);
        acc[2 * e + 1] += sv[q] * hi16(v[q][e]);
      }
#pragma unroll
    for (int o = 8; o < 64; o <<= 1)
#pragma unroll
      for (int e = 0; e < 8; ++e) acc[e] += __shfl_xor(acc[e], o, 64);
    float* red = (float*)smem;
    if (lane < 8) {
#pragma unroll
      for (int e = 0; e < 8; ++e) red[(tid >> 6) * 64 + lane * 8 + e] = acc[e];
    }
    __syncthreads();
    if (tid < 64) {
      float t = red[tid] + red[64 + tid] + red[128 + tid] + red[192 + tid];
      ((float*)(ws + WS_MODP))[kq * MODN + cg * 64 + tid] = t;
    }
  }
}

__device__ __forceinline__ float mod_val(const float* modp, int idx) {
  return bfr((modp[idx] + modp[MODN + idx]) + (modp[2 * MODN + idx] + modp[3 * MODN + idx]));
}

constexpr int K2_NS = 6, K2_STAGE = 128 * 128 + 80 * 128;
__global__ __launch_bounds__(256, 2) void k2_seq_dft(const unsigned short* __restrict__ wre,
                                                     const unsigned short* __restrict__ wim,
                                                     char* __restrict__ ws) {
  extern __shared__ __attribute__((aligned(16))) char smem[];
  const int tid = threadIdx.x, lane = tid & 63;
  const int w = __builtin_amdgcn_readfirstlane(tid >> 6);
  const int wm = w & 1, wk = w >> 1;
  const int bid = blockIdx.x;
  const int xcd = bid & 7, bi = bid >> 3;
  const int cb = bi & 7, rsel = bi >> 3;
  const int isim = rsel >> 1;
  const int t0 = (xcd * 2 + (rsel & 1)) * 128;
  const float* modp = (const float*)(ws + WS_MODP);
  float mp0 = 0.f, mp1 = 0.f, mp2 = 0.f, mp3 = 0.f;
  if (tid < 160) {
    const int idx = (tid < 80) ? (D_ + cb * 80 + tid) : (cb * 80 + tid - 80);
    mp0 = modp[idx]; mp1 = modp[MODN + idx]; mp2 = modp[2 * MODN + idx]; mp3 = modp[3 * MODN + idx];
  }
  const char* rbase = (const char*)(isim ? wim : wre) + (size_t)t0 * (S_ * 2);
  const char* cbase = ws + WS_HT + (size_t)cb * 32 * 10240;
  unsigned roff[4];
#pragma unroll
  for (int j = 0; j < 4; ++j) {
    const int row = 32 * w + 8 * j + (lane >> 3);
    roff[j] = row * (S_ * 2) + 16 * ((lane & 7) ^ ((row >> 1) & 7));
  }
  const unsigned coff = (20 * w) * 128 + lane * 16;
  const unsigned lds0 = lds_off(smem);
  auto iss = [&](auto jc, int ks, int buf) {
    constexpr int j = decltype(jc)::value;
    const unsigned lb = lds0 + buf * K2_STAGE;
    if constexpr (j < 4) dma16(roff[j], rbase + ks * 128, lb + (32 * w + 8 * j) * 128);
    else if constexpr (j < 6) dma16(coff, cbase + ks * 10240 + (j - 4) * 1024, lb + 16384 + (20 * w + 8 * (j - 4)) * 128);
    else dma16h(coff, cbase + ks * 10240 + 2048, lb + 16384 + (20 * w + 16) * 128);
  };
  auto issue = [&](int ks, int buf) {
    iss(IC<0>{}, ks, buf); iss(IC<1>{}, ks, buf); iss(IC<2>{}, ks, buf); iss(IC<3>{}, ks, buf);
    iss(IC<4>{}, ks, buf); iss(IC<5>{}, ks, buf); iss(IC<6>{}, ks, buf);
  };
  const int fsw = ((4 * wk + (lane >> 4)) ^ ((lane & 15) >> 1)) * 16;
  const int ra = (64 * wm + (lane & 15)) * 128 + fsw;
  const int ca_ = 16384 + (lane & 15) * 128 + fsw;
  bf16x8 rf[2][4], cf[2][5];
  f32x4 acc[6][4];
#pragma unroll
  for (int j = 0; j < 6; ++j)
#pragma unroll
    for (int i = 0; i < 4; ++i) acc[j][i] = (f32x4){0.f, 0.f, 0.f, 0.f};
  const s16x8 ones_s = {0x3F80, 0x3F80, 0x3F80, 0x3F80, 0x3F80, 0x3F80, 0x3F80, 0x3F80};
  const bf16x8 ones = __builtin_bit_cast(bf16x8, ones_s);
  auto loadr = [&](auto par, int buf) {
    constexpr int PP = decltype(par)::value;
    const char* lb = smem + buf * K2_STAGE;
#pragma unroll
    for (int i = 0; i < 4; ++i) rf[PP][i] = lds_frag(lb + ra + i * 2048);
  };
  auto loadc = [&](auto par, int buf) {
    constexpr int PP = decltype(par)::value;
    const char* lb = smem + buf * K2_STAGE;
#pragma unroll
    for (int j = 0; j < 5; ++j) cf[PP][j] = lds_frag(lb + ca_ + j * 2048);
  };
  auto mma = [&](auto par, auto ic) {
    constexpr int PP = decltype(par)::value, i = decltype(ic)::value;
#pragma unroll
    for (int j = 0; j < 5; ++j) acc[j][i] = MFMA16(cf[PP][j], rf[PP][i], acc[j][i]);
    acc[5][i] = MFMA16(ones, rf[PP][i], acc[5][i]);
  };
  auto body = [&](auto par, auto di, auto dl, int rd, int ks, int wr) {
    constexpr int PP = decltype(par)::value, DI = decltype(di)::value, DL = decltype(dl)::value;
    if constexpr (DI) { iss(IC<0>{}, ks, wr); iss(IC<1>{}, ks, wr); }
    if constexpr (DL) loadr(IC<PP ^ 1>{}, rd);
    SB0();
    mma(par, IC<0>{});
    SB0();
    if constexpr (DI) { iss(IC<2>{}, ks, wr); iss(IC<3>{}, ks, wr); }
    if constexpr (DL) loadc(IC<PP ^ 1>{}, rd);
    SB0();
    mma(par, IC<1>{});
    SB0();
    if constexpr (DI) { iss(IC<4>{}, ks, wr); iss(IC<5>{}, ks, wr); }
    SB0();
    mma(par, IC<2>{});
    SB0();
    if constexpr (DI) iss(IC<6>{}, ks, wr);
    SB0();
    mma(par, IC<3>{});
  };
  pipeline<K2_NS, 32, 7>(issue, [] {}, [&] { loadr(IC<0>{}, 0); loadc(IC<0>{}, 0); }, body);
  wait_vm_lgkm0<0>();
  bar();
  float* mv = (float*)(smem + 49152);
  if (tid < 160) {
    float m = bfr((mp0 + mp1) + (mp2 + mp3));
    mv[tid] = (tid < 80) ? bfr(1.0f + m) : m;
  }
  {
    char* dst = smem + w * 12288 + lane * 16;
#pragma unroll
    for (int j = 0; j < 6; ++j) {
      f32x4 a0 = wk ? acc[j][0] : acc[j][2];
      f32x4 a1 = wk ? acc[j][1] : acc[j][3];
      *(f32x4*)(dst + (2 * j) * 1024) = a0;
      *(f32x4*)(dst + (2 * j + 1) * 1024) = a1;
    }
  }
  __syncthreads();
  {
    const char* srcp = smem + (w ^ 2) * 12288 + lane * 16;
    f32x4 k0[6], k1[6];
#pragma unroll
    for (int j = 0; j < 6; ++j) {
      k0[j] = (wk ? acc[j][2] : acc[j][0]) + *(const f32x4*)(srcp + (2 * j) * 1024);
      k1[j] = (wk ? acc[j][3] : acc[j][1]) + *(const f32x4*)(srcp + (2 * j + 1) * 1024);
    }
    const float sgn = isim ? -1.0f : 1.0f;
    const int tt = t0 + 64 * wm + 32 * wk + (lane & 15);
    char* pq = ws + WS_PQ + (size_t)tt * 2560 + (isim * 640 + cb * 80 + 4 * (lane >> 4)) * 2;
#pragma unroll
    for (int j = 0; j < 5; ++j) {
      f32x4 c1 = *(const f32x4*)(mv + 16 * j + 4 * (lane >> 4));
      f32x4 sa = *(const f32x4*)(mv + 80 + 16 * j + 4 * (lane >> 4));
      f32x4 v0 = (c1 * k0[j] + sa * k0[5][0]) * sgn;
      f32x4 v1 = (c1 * k1[j] + sa * k1[5][0]) * sgn;
      u32x2 o0 = {pk2(v0[0], v0[1]), pk2(v0[2], v0[3])};
      u32x2 o1 = {pk2(v1[0], v1[1]), pk2(v1[2], v1[3])};
      *(u32x2*)(pq + j * 32) = o0;
      *(u32x2*)(pq + 16 * 2560 + j * 32) = o1;
    }
  }
}

constexpr int K3_NS = 8, K3_STAGE = 64 * 128 + 80 * 128;
__global__ __launch_bounds__(256, 2) void k3_hid_dft(const unsigned short* __restrict__ dre,
                                                     const unsigned short* __restrict__ dim,
                                                     const unsigned short* __restrict__ x,
                                                     char* __restrict__ ws) {
  extern __shared__ __attribute__((aligned(16))) char smem[];
  const int tid = threadIdx.x, lane = tid & 63;
  const int w = __builtin_amdgcn_readfirstlane(tid >> 6);
  const int wm = w & 1, wk = w >> 1;
  const int bid = blockIdx.x;
  const int xcd = bid & 7, bi = bid >> 3;
  const int cb = bi & 7, rb = xcd * 4 + (bi >> 3);
  const int t0 = rb * 64;
  const float* modp = (const float*)(ws + WS_MODP);
  float mp0 = 0.f, mp1 = 0.f, mp2 = 0.f, mp3 = 0.f;
  if (tid < 80) {
    const int idx = 2 * D_ + cb * 80 + tid;
    mp0 = modp[idx]; mp1 = modp[MODN + idx]; mp2 = modp[2 * MODN + idx]; mp3 = modp[3 * MODN + idx];
  }
  const int tt = t0 + 32 * wm + 16 * wk + (lane & 15);
  u32x2 xr[5];
#pragma unroll
  for (int j = 0; j < 5; ++j)
    xr[j] = *(const u32x2*)(x + (size_t)tt * D_ + cb * 80 + 16 * j + 4 * (lane >> 4));

  const char* rbase = ws + WS_PQ + (size_t)t0 * 2560;
  const char* cre = (const char*)dre + (size_t)cb * 80 * 1280;
  const char* cim = (const char*)dim + (size_t)cb * 80 * 1280 - 1280;
  unsigned roff[2], coff[3];
#pragma unroll
  for (int j = 0; j < 2; ++j) {
    const int row = 16 * w + 8 * j + (lane >> 3);
    roff[j] = row * 2560 + 16 * ((lane & 7) ^ ((row >> 1) & 7));
  }
#pragma unroll
  for (int j = 0; j < 3; ++j) {
    int row = 20 * w + 8 * j + (lane >> 3);
    if (row > 79) row = 79;
    coff[j] = row * 1280 + 16 * ((lane & 7) ^ ((row >> 1) & 7));
  }
  const unsigned lds0 = lds_off(smem);
  auto iss = [&](auto jc, int ks, int buf) {
    constexpr int j = decltype(jc)::value;
    const unsigned lb = lds0 + buf * K3_STAGE;
    const char* cgp = (ks < 10 ? cre : cim) + ks * 128;
    if constexpr (j < 2) dma16(roff[j], rbase + ks * 128, lb + (16 * w + 8 * j) * 128);
    else if constexpr (j < 4) dma16(coff[j - 2], cgp, lb + 8192 + (20 * w + 8 * (j - 2)) * 128);
    else dma16h(coff[2], cgp, lb + 8192 + (20 * w + 16) * 128);
  };
  auto issue = [&](int ks, int buf) {
    iss(IC<0>{}, ks, buf); iss(IC<1>{}, ks, buf); iss(IC<2>{}, ks, buf); iss(IC<3>{}, ks, buf); iss(IC<4>{}, ks, buf);
  };
  const int fsw = ((4 * wk + (lane >> 4)) ^ ((lane & 15) >> 1)) * 16;
  const int ra = (32 * wm + (lane & 15)) * 128 + fsw;
  const int ca_ = 8192 + (lane & 15) * 128 + fsw;
  bf16x8 rf[2][2], cf[2][5];
  f32x4 acc[5][2];
#pragma unroll
  for (int j = 0; j < 5; ++j)
#pragma unroll
    for (int i = 0; i < 2; ++i) acc[j][i] = (f32x4){0.f, 0.f, 0.f, 0.f};
  auto load = [&](auto par, int buf) {
    constexpr int PP = decltype(par)::value;
    const char* lb = smem + buf * K3_STAGE;
#pragma unroll
    for (int i = 0; i < 2; ++i) rf[PP][i] = lds_frag(lb + ra + i * 2048);
#pragma unroll
    for (int j = 0; j < 5; ++j) cf[PP][j] = lds_frag(lb + ca_ + j * 2048);
  };
  auto mma = [&](auto par, auto ic) {
    constexpr int PP = decltype(par)::value, i = decltype(ic)::value;
#pragma unroll
    for (int j = 0; j < 5; ++j) acc[j][i] = MFMA16(cf[PP][j], rf[PP][i], acc[j][i]);
  };
  auto body = [&](auto par, auto di, auto dl, int rd, int ks, int wr) {
    constexpr int PP = decltype(par)::value, DI = decltype(di)::value, DL = decltype(dl)::value;
    if constexpr (DI) { iss(IC<0>{}, ks, wr); iss(IC<1>{}, ks, wr); }
    if constexpr (DL) load(IC<PP ^ 1>{}, rd);
    SB0();
    mma(par, IC<0>{});
    SB0();
    if constexpr (DI) { iss(IC<2>{}, ks, wr); iss(IC<3>{}, ks, wr); }
    SB0();
    mma(par, IC<1>{});
    SB0();
    if constexpr (DI) iss(IC<4>{}, ks, wr);
  };
  pipeline<K3_NS, 20, 5>(issue, [] {}, [&] { load(IC<0>{}, 0); }, body);
  wait_vm_lgkm0<0>();
  bar();
  float* mv = (float*)(smem + 32768);
  if (tid < 80) mv[tid] = bfr((mp0 + mp1) + (mp2 + mp3));
  {
    char* dst = smem + w * 5120 + lane * 16;
#pragma unroll
    for (int j = 0; j < 5; ++j) *(f32x4*)(dst + j * 1024) = wk ? acc[j][0] : acc[j][1];
  }
  __syncthreads();
  {
    const char* srcp = smem + (w ^ 2) * 5120 + lane * 16;
    char* x1 = ws + WS_X1 + (size_t)tt * (D_ * 2) + (cb * 80 + 4 * (lane >> 4)) * 2;
#pragma unroll
    for (int j = 0; j < 5; ++j) {
      f32x4 y = (wk ? acc[j][1] : acc[j][0]) + *(const f32x4*)(srcp + j * 1024);
      f32x4 ga = *(const f32x4*)(mv + 16 * j + 4 * (lane >> 4));
      float r0 = lo16(xr[j][0]) + bfr(ga[0] * bfr(y[0]));
      float r1 = hi16(xr[j][0]) + bfr(ga[1] * bfr(y[1]));
      float r2 = lo16(xr[j][1]) + bfr(ga[2] * bfr(y[2]));
      float r3 = hi16(xr[j][1]) + bfr(ga[3] * bfr(y[3]));
      u32x2 o = {pk2(r0, r1), pk2(r2, r3)};
      *(u32x2*)(x1 + j * 32) = o;
    }
  }
}

constexpr int K4_NS = 6, K4_STAGE = 32 * 384, K4_A = 81920, K4_MOD = K4_A + K4_NS * K4_STAGE;
__global__ __launch_bounds__(256, 2) void k4_mlp1(const unsigned short* __restrict__ w1,
                                                  char* __restrict__ ws) {
  extern __shared__ __attribute__((aligned(16))) char smem[];
  const int tid = threadIdx.x, lane = tid & 63;
  const int w = __builtin_amdgcn_readfirstlane(tid >> 6);
  const int bid = blockIdx.x;
  const int xcd = bid & 7, bi = bid >> 3;
  const int cb = bi & 7, rb = xcd * 4 + (bi >> 3);
  const int t0 = rb * 64;
  const float* modp = (const float*)(ws + WS_MODP);
  const int l8 = tid & 7, r32 = tid >> 3;
  u32x4 xv[2][10];
  {
    const char* xb = ws + WS_X1 + (size_t)(t0 + r32) * (D_ * 2) + l8 * 16;
#pragma unroll
    for (int p = 0; p < 2; ++p)
#pragma unroll
      for (int i = 0; i < 10; ++i) xv[p][i] = *(const u32x4*)(xb + p * 32 * (D_ * 2) + i * 128);
  }
  float mq[5][4];
#pragma unroll
  for (int q = 0; q < 5; ++q) {
    const int idx = 3 * D_ + tid + 256 * q;
#pragma unroll
    for (int e = 0; e < 4; ++e) mq[q][e] = modp[e * MODN + idx];
  }
  const char* cbase = (const char*)w1 + cb * 384;
  unsigned coff[3];
#pragma unroll
  for (int j = 0; j < 3; ++j) {
    const int L = 192 * w + 64 * j + lane;
    const int r = L / 24, pc = L - r * 24;
    const int k = (r & ~12) | ((r & 4) << 1) | ((r & 8) >> 1);
    const int c = pc ^ (((r >> 1) & 3) << 1);
    coff[j] = k * (F_ * 2) + c * 16;
  }
  const unsigned lds0 = lds_off(smem);
  auto iss = [&](auto jc, int ks, int buf) {
    constexpr int j = decltype(jc)::value;
    dma16(coff[j], cbase + (size_t)ks * 32 * (F_ * 2), lds0 + K4_A + buf * K4_STAGE + (192 * w + 64 * j) * 16);
  };
  auto issue = [&](int ks, int buf) { iss(IC<0>{}, ks, buf); iss(IC<1>{}, ks, buf); iss(IC<2>{}, ks, buf); };
  auto pre = [&]() {
    unsigned short* mv = (unsigned short*)(smem + K4_MOD);
#pragma unroll
    for (int q = 0; q < 5; ++q) {
      const int idx = tid + 256 * q;
      float m = bfr((mq[q][0] + mq[q][1]) + (mq[q][2] + mq[q][3]));
      float o = (idx < 640) ? m : (1.0f + m);
      mv[idx] = (unsigned short)(pk2(o, 0.f) & 0xffffu);
    }
    asm volatile("s_waitcnt lgkmcnt(0)" ::: "memory");
    bar();
#pragma unroll
    for (int p = 0; p < 2; ++p) {
      SB0();
      float s = 0.f;
#pragma unroll
      for (int i = 0; i < 10; ++i)
#pragma unroll
        for (int e = 0; e < 4; ++e) s += lo16(xv[p][i][e]) + hi16(xv[p][i][e]);
#pragma unroll
      for (int o = 1; o < 8; o <<= 1) s += __shfl_xor(s, o, 64);
      const float mean = s * (1.0f / D_);
      float s2 = 0.f;
#pragma unroll
      for (int i = 0; i < 10; ++i)
#pragma unroll
        for (int e = 0; e < 4; ++e) {
          float a = lo16(xv[p][i][e]) - mean, b = hi16(xv[p][i][e]) - mean;
          s2 += a * a + b * b;
        }
#pragma unroll
      for (int o = 1; o < 8; o <<= 1) s2 += __shfl_xor(s2, o, 64);
      const float mb = bfr(mean);
      const float vb = bfr(s2 * (1.0f / D_));
      const float den = bfr(sqrtf(bfr(vb + 1e-5f)));
      const int row = p * 32 + r32;
      char* dst = smem + row * 128 + ((l8 ^ ((row >> 1) & 7)) * 16);
      SB0();
#pragma unroll
      for (int i = 0; i < 10; ++i) {
        const int d0 = (l8 + 8 * i) * 8;
        u32x4 sbp = *(const u32x4*)(mv + d0);
        u32x4 c1p = *(const u32x4*)(mv + 640 + d0);
        u32x4 o;
#pragma unroll
        for (int e = 0; e < 4; ++e) {
          float a = bfr(bfr(lo16(xv[p][i][e]) - mb) / den);
          float b = bfr(bfr(hi16(xv[p][i][e]) - mb) / den);
          a = bfr(a * lo16(c1p[e])) + lo16(sbp[e]);
          b = bfr(b * hi16(c1p[e])) + hi16(sbp[e]);
          o[e] = pk2(a, b);
        }
        *(u32x4*)(dst + i * 8192) = o;
        if (i & 1) SB0();
      }
    }
    SB0();
  };
  const int g = lane >> 4, q = (lane & 15) >> 2, p4 = lane & 3;
  const int rpos = 16 * (g >> 1) + 4 * (g & 1) + q;
  const int xs = (rpos >> 1) & 3;
  int cadr[3];
#pragma unroll
  for (int j = 0; j < 3; ++j) {
    const int jj = 3 * w + j;
    cadr[j] = K4_A + rpos * 384 + (2 * (jj ^ xs) + (p4 >> 1)) * 16 + (p4 & 1) * 8;
  }
  const int ra = (lane & 15) * 128;
  const int sw0 = ((g) ^ ((lane & 15) >> 1)) * 16, sw1 = ((4 + g) ^ ((lane & 15) >> 1)) * 16;
  bf16x8 rf[2][4], cf[2][3];
  f32x4 acc[3][4];
#pragma unroll
  for (int j = 0; j < 3; ++j)
#pragma unroll
    for (int i = 0; i < 4; ++i) acc[j][i] = (f32x4){0.f, 0.f, 0.f, 0.f};
  int kst = 0;
  auto load = [&](auto par, int buf) {
    constexpr int PP = decltype(par)::value;
    const char* ab = smem + (kst >> 1) * 8192 + ra + (PP ? sw1 : sw0);
#pragma unroll
    for (int i = 0; i < 4; ++i) rf[PP][i] = lds_frag(ab + i * 2048);
    const char* lb = smem + buf * K4_STAGE;
#pragma unroll
    for (int j = 0; j < 3; ++j) cf[PP][j] = cat_frag(lds_tr(lb + cadr[j]), lds_tr(lb + cadr[j] + 8 * 384));
    ++kst;
  };
  auto mma = [&](auto par, auto ic) {
    constexpr int PP = decltype(par)::value, i = decltype(ic)::value;
#pragma unroll
    for (int j = 0; j < 3; ++j) acc[j][i] = MFMA16(cf[PP][j], rf[PP][i], acc[j][i]);
  };
  auto body = [&](auto par, auto di, auto dl, int rd, int ks, int wr) {
    constexpr int PP = decltype(par)::value, DI = decltype(di)::value, DL = decltype(dl)::value;
    if constexpr (DI) iss(IC<0>{}, ks, wr);
    if constexpr (DL) load(IC<PP ^ 1>{}, rd);
    SB0();
    mma(par, IC<0>{});
    SB0();
    if constexpr (DI) iss(IC<1>{}, ks, wr);
    SB0();
    mma(par, IC<1>{});
    SB0();
    if constexpr (DI) iss(IC<2>{}, ks, wr);
    SB0();
    mma(par, IC<2>{});
    mma(par, IC<3>{});
  };
  pipeline<K4_NS, 20, 3>(issue, pre, [&] { load(IC<0>{}, 0); }, body);
  {
    char* h1 = ws + WS_H1 + (size_t)(t0 + (lane & 15)) * (F_ * 2) + (cb * 192 + 48 * w + 4 * g) * 2;
#pragma unroll
    for (int i = 0; i < 4; ++i)
#pragma unroll
      for (int j = 0; j < 3; ++j) {
        f32x4 v = acc[j][i];
        u32x2 o = {pk2(fmaxf(v[0], 0.f), fmaxf(v[1], 0.f)), pk2(fmaxf(v[2], 0.f), fmaxf(v[3], 0.f))};
        *(u32x2*)(h1 + (size_t)i * 16 * (F_ * 2) + j * 32) = o;
      }
  }
}

constexpr int K5_NS = 8, K5_STAGE = 64 * 128 + 64 * 160;
__global__ __launch_bounds__(256, 2) void k5_mlp2(const unsigned short* __restrict__ w2,
                                                  char* __restrict__ ws,
                                                  unsigned short* __restrict__ out) {
  extern __shared__ __attribute__((aligned(16))) char smem[];
  const int tid = threadIdx.x, lane = tid & 63;
  const int w = __builtin_amdgcn_readfirstlane(tid >> 6);
  const int wm = w & 1, wk = w >> 1;
  const int bid = blockIdx.x;
  const int xcd = bid & 7, bi = bid >> 3;
  const int cb = bi & 7, rb = xcd * 4 + (bi >> 3);
  const int t0 = rb * 64;
  const float* modp = (const float*)(ws + WS_MODP);
  float mp0 = 0.f, mp1 = 0.f, mp2 = 0.f, mp3 = 0.f;
  if (tid < 80) {
    const int idx = 5 * D_ + cb * 80 + tid;
    mp0 = modp[idx]; mp1 = modp[MODN + idx]; mp2 = modp[2 * MODN + idx]; mp3 = modp[3 * MODN + idx];
  }
  const int tt = t0 + 32 * wm + 16 * wk + (lane & 15);
  u32x2 xr[5];
#pragma unroll
  for (int j = 0; j < 5; ++j)
    xr[j] = *(const u32x2*)(ws + WS_X1 + (size_t)tt * (D_ * 2) + (cb * 80 + 16 * j + 4 * (lane >> 4)) * 2);

  const char* rbase = ws + WS_H1 + (size_t)t0 * (F_ * 2);
  const char* cbase = (const char*)w2 + cb * 160;
  unsigned roff[2], coff[3];
#pragma unroll
  for (int j = 0; j < 2; ++j) {
    const int row = 16 * w + 8 * j + (lane >> 3);
    roff[j] = row * (F_ * 2) + 16 * ((lane & 7) ^ ((row >> 1) & 7));
  }
#pragma unroll
  for (int j = 0; j < 3; ++j) {
    int L = 160 * w + 64 * j + lane;
    if (L > 639) L = 639;
    const int r = L / 10, ch = L - r * 10;
    const int k = (r & ~12) | ((r & 4) << 1) | ((r & 8) >> 1);
    coff[j] = k * (D_ * 2) + ch * 16;
  }
  const unsigned lds0 = lds_off(smem);
  auto iss = [&](auto jc, int ks, int buf) {
    constexpr int j = decltype(jc)::value;
    const unsigned lb = lds0 + buf * K5_STAGE;
    const char* cgp = cbase + (size_t)ks * 64 * (D_ * 2);
    if constexpr (j < 2) dma16(roff[j], rbase + ks * 128, lb + (16 * w + 8 * j) * 128);
    else if constexpr (j < 4) dma16(coff[j - 2], cgp, lb + 8192 + (160 * w + 64 * (j - 2)) * 16);
    else dma16h(coff[2], cgp, lb + 8192 + (160 * w + 128) * 16);
  };
  auto issue = [&](int ks, int buf) {
    iss(IC<0>{}, ks, buf); iss(IC<1>{}, ks, buf); iss(IC<2>{}, ks, buf); iss(IC<3>{}, ks, buf); iss(IC<4>{}, ks, buf);
  };
  const int g = lane >> 4, q = (lane & 15) >> 2, p4 = lane & 3;
  const int fsw = ((4 * wk + g) ^ ((lane & 15) >> 1)) * 16;
  const int ra = (32 * wm + (lane & 15)) * 128 + fsw;
  const int ca_ = 8192 + (32 * wk + 16 * (g >> 1) + 4 * (g & 1) + q) * 160 + p4 * 8;
  bf16x8 rf[2][2], cf[2][5];
  f32x4 acc[5][2];
#pragma unroll
  for (int j = 0; j < 5; ++j)
#pragma unroll
    for (int i = 0; i < 2; ++i) acc[j][i] = (f32x4){0.f, 0.f, 0.f, 0.f};
  auto load = [&](auto par, int buf) {
    constexpr int PP = decltype(par)::value;
    const char* lb = smem + buf * K5_STAGE;
#pragma unroll
    for (int i = 0; i < 2; ++i) rf[PP][i] = lds_frag(lb + ra + i * 2048);
#pragma unroll
    for (int j = 0; j < 5; ++j) cf[PP][j] = cat_frag(lds_tr(lb + ca_ + j * 32), lds_tr(lb + ca_ + j * 32 + 1280));
  };
  auto mma = [&](auto par, auto ic) {
    constexpr int PP = decltype(par)::value, i = decltype(ic)::value;
#pragma unroll
    for (int j = 0; j < 5; ++j) acc[j][i] = MFMA16(cf[PP][j], rf[PP][i], acc[j][i]);
  };
  auto body = [&](auto par, auto di, auto dl, int rd, int ks, int wr) {
    constexpr int PP = decltype(par)::value, DI = decltype(di)::value, DL = decltype(dl)::value;
    if constexpr (DI) { iss(IC<0>{}, ks, wr); iss(IC<1>{}, ks, wr); }
    if constexpr (DL) load(IC<PP ^ 1>{}, rd);
    SB0();
    mma(par, IC<0>{});
    SB0();
    if constexpr (DI) { iss(IC<2>{}, ks, wr); iss(IC<3>{}, ks, wr); }
    SB0();
    mma(par, IC<1>{});
    SB0();
    if constexpr (DI) iss(IC<4>{}, ks, wr);
  };
  pipeline<K5_NS, 24, 5>(issue, [] {}, [&] { load(IC<0>{}, 0); }, body);
  wait_vm_lgkm0<0>();
  bar();
  float* mv = (float*)(smem + 32768);
  if (tid < 80) mv[tid] = bfr((mp0 + mp1) + (mp2 + mp3));
  {
    char* dst = smem + w * 5120 + lane * 16;
#pragma unroll
    for (int j = 0; j < 5; ++j) *(f32x4*)(dst + j * 1024) = wk ? acc[j][0] : acc[j][1];
  }
  __syncthreads();
  {
    const char* srcp = smem + (w ^ 2) * 5120 + lane * 16;
    char* op = (char*)out + (size_t)tt * (D_ * 2) + (cb * 80 + 4 * (lane >> 4)) * 2;
#pragma unroll
    for (int j = 0; j < 5; ++j) {
      f32x4 y = (wk ? acc[j][1] : acc[j][0]) + *(const f32x4*)(srcp + j * 1024);
      f32x4 gb = *(const f32x4*)(mv + 16 * j + 4 * (lane >> 4));
      float r0 = lo16(xr[j][0]) + bfr(gb[0] * bfr(y[0]));
      float r1 = hi16(xr[j][0]) + bfr(gb[1] * bfr(y[1]));
      float r2 = lo16(xr[j][1]) + bfr(gb[2] * bfr(y[2]));
      float r3 = hi16(xr[j][1]) + bfr(gb[3] * bfr(y[3]));
      u32x2 o = {pk2(r0, r1), pk2(r2, r3)};
      *(u32x2*)(op + j * 32) = o;
    }
  }
}

extern "C" void kernel_launch(void* const* d_in, const int* in_sizes, int n_in,
                              void* d_out, int out_size, void* d_ws, size_t ws_size,
                              hipStream_t stream) {
  const unsigned short* x    = (const unsigned short*)d_in[0];
  const unsigned short* wsre = (const unsigned short*)d_in[1];
  const unsigned short* wsim = (const unsigned short*)d_in[2];
  const unsigned short* wdre = (const unsigned short*)d_in[3];
  const unsigned short* wdim = (const unsigned short*)d_in[4];
  const unsigned short* w1   = (const unsigned short*)d_in[6];
  const unsigned short* w2   = (const unsigned short*)d_in[7];
  const unsigned short* c    = (const unsigned short*)d_in[8];
  const unsigned short* wmod = (const unsigned short*)d_in[9];
  char* ws = (char*)d_ws;
  unsigned short* out = (unsigned short*)d_out;
  k1_ln_mod<<<368, 256, 0, stream>>>(x, c, wmod, ws);
  constexpr int L2 = K2_NS * K2_STAGE, L3 = K3_NS * K3_STAGE, L4 = K4_MOD + 5120, L5 = K5_NS * K5_STAGE;
  hipFuncSetAttribute((const void*)k2_seq_dft, hipFuncAttributeMaxDynamicSharedMemorySize, L2);
  hipFuncSetAttribute((const void*)k3_hid_dft, hipFuncAttributeMaxDynamicSharedMemorySize, L3);
  hipFuncSetAttribute((const void*)k4_mlp1, hipFuncAttributeMaxDynamicSharedMemorySize, L4);
  hipFuncSetAttribute((const void*)k5_mlp2, hipFuncAttributeMaxDynamicSharedMemorySize, L5);
  k2_seq_dft<<<256, 256, L2, stream>>>(wsre, wsim, ws);
  k3_hid_dft<<<256, 256, L3, stream>>>(wdre, wdim, x, ws);
  k4_mlp1<<<256, 256, L4, stream>>>(w1, ws);
  k5_mlp2<<<256, 256, L5, stream>>>(w2, ws, out);
}
```

```cpp
#include <hip/hip_runtime.h>
#include <hip/hip_bf16.h>
#include <stdint.h>

typedef __attribute__((ext_vector_type(8))) __bf16 bf16x8;
typedef __attribute__((ext_vector_type(4))) __bf16 bf16x4;
typedef __attribute__((ext_vector_type(2))) __bf16 bf16x2;
typedef __attribute__((ext_vector_type(4))) float f32x4;
typedef __attribute__((ext_vector_type(2))) float f32x2;
typedef __attribute__((ext_vector_type(4))) short s16x4;
typedef __attribute__((ext_vector_type(8))) short s16x8;
typedef __attribute__((ext_vector_type(4))) unsigned u32x4;
typedef __attribute__((ext_vector_type(2))) unsigned u32x2;
typedef __attribute__((address_space(3))) s16x4* lds_s16x4_ptr;
typedef __attribute__((address_space(3))) void* lds_vptr;

constexpr int S_ = 2048, D_ = 640, F_ = 1536, MODN = 3840;

constexpr size_t WS_MOD = 0;
constexpr size_t WS_E   = 16384;
constexpr size_t WS_ST  = 32768;
constexpr size_t WS_HT  = WS_ST + (size_t)S_ * 64;
constexpr size_t WS_PQ  = WS_HT + (size_t)D_ * S_ * 2;
constexpr size_t WS_X1  = WS_PQ + (size_t)S_ * 1280 * 2;
constexpr size_t WS_XC  = WS_X1 + (size_t)S_ * D_ * 2;
constexpr size_t WS_H1  = WS_XC + (size_t)S_ * D_ * 2;
constexpr size_t WS_W2T = WS_H1 + (size_t)S_ * F_ * 2;

#define PSTAMP_OUT(kid)
#define STAMP_DECL
#define STAMP(i)
#define STAMP_OUT(kid)
template <int V> struct IC { static constexpr int value = V; };
template <int N, int I = 0, class F> __device__ __forceinline__ void static_for(F f) {
  if constexpr (I < N) { f(IC<I>{}); static_for<N, I + 1>(f); }
}

__device__ __forceinline__ float bf2f(unsigned short u) { return __uint_as_float(((unsigned)u) << 16); }
__device__ __forceinline__ float bfr(float f) { return (float)(__bf16)f; }
__device__ __forceinline__ unsigned pk2(float a, float b) {
  bf16x2 v = __builtin_convertvector((f32x2){a, b}, bf16x2);
  return __builtin_bit_cast(unsigned, v);
}
__device__ __forceinline__ float lo16(unsigned u) { return __uint_as_float(u << 16); }
__device__ __forceinline__ float hi16(unsigned u) { return __uint_as_float(u & 0xffff0000u); }

__device__ __forceinline__ void dma16(unsigned voff, const char* sbase, unsigned lds) {
  asm volatile("s_mov_b32 m0, %2\n\ts_nop 0\n\tglobal_load_lds_dwordx4 %0, %1"
               ::"v"(voff), "s"(sbase), "s"(lds) : "memory");
}
__device__ __forceinline__ void dma16h(unsigned voff, const char* sbase, unsigned lds) {
  asm volatile("s_mov_b32 m0, %2\n\ts_mov_b32 exec_hi, 0\n\tglobal_load_lds_dwordx4 %0, %1\n\ts_mov_b32 exec_hi, -1"
               ::"v"(voff), "s"(sbase), "s"(lds) : "memory");
}
__device__ __forceinline__ unsigned lds_off(const void* p) {
  return (unsigned)(uintptr_t)(__attribute__((address_space(3))) const char*)p;
}
#define SB0() __builtin_amdgcn_sched_barrier(0)
template <int N> __device__ __forceinline__ void wait_vm_lgkm0() {
  asm volatile("s_waitcnt vmcnt(%0) lgkmcnt(0)" ::"n"(N) : "memory");
}
__device__ __forceinline__ void bar() {
  asm volatile("" ::: "memory");
  __builtin_amdgcn_s_barrier();
  asm volatile("" ::: "memory");
}
__device__ __forceinline__ bf16x8 lds_frag(const char* p) { return *(const bf16x8*)p; }
__device__ __forceinline__ s16x4 lds_tr(const char* p) {
  return __builtin_amdgcn_ds_read_tr16_b64_v4i16((lds_s16x4_ptr)p);
}
__device__ __forceinline__ bf16x8 cat_frag(s16x4 a, s16x4 b) {
  s16x8 v = __builtin_shufflevector(a, b, 0, 1, 2, 3, 4, 5, 6, 7);
  return __builtin_bit_cast(bf16x8, v);
}
#define MFMA16(a, b, c) __builtin_amdgcn_mfma_f32_16x16x32_bf16((a), (b), (c), 0, 0, 0)

template <class Sc, int POS, int NM, int NR, int DL, class FM, class FR>
__device__ __forceinline__ void run_sched(FM& fm, FR& fr) {
  if constexpr (Sc::s[POS] != 0) {
    constexpr char c = Sc::s[POS];
    if constexpr (c == 'M') {
      fm(IC<NM>{}); SB0();
      run_sched<Sc, POS + 1, NM + 1, NR, DL>(fm, fr);
    } else {
      if constexpr (DL) { fr(IC<NR>{}); SB0(); }
      run_sched<Sc, POS + 1, NM, NR + 1, DL>(fm, fr);
    }
  }
}
template <int N> __device__ __forceinline__ void wait_vm() { asm volatile("s_waitcnt vmcnt(%0)" ::"n"(N) : "memory"); }
__device__ __forceinline__ void wait_lgkm0() { asm volatile("s_waitcnt lgkmcnt(0)" ::: "memory"); }

template <int NS, int P, int TI>
__device__ __forceinline__ void prod_tail() {
  if constexpr (TI < NS - 1) {
    wait_vm<P*(NS - 2 - TI)>();
    bar();
    prod_tail<NS, P, TI + 1>();
  }
}
constexpr int ramp_A(int j, int NS) {
  int a = 2;
  for (int i = 1; i <= j; ++i) { int lim = (i == 1) ? NS : (i - 1) + NS; a = (a + 2 < lim) ? a + 2 : lim; }
  return a;
}
constexpr int ramp_len(int NS) { int j = 1; while (ramp_A(j, NS) != ((j == 1) ? NS : (j - 1) + NS) || j < 2) ++j; return j; }
template <int NS, int P, int J, int R, class Issue>
__device__ __forceinline__ void prod_ramp(Issue& issue) {
  if constexpr (J < R) {
    constexpr int a0 = (J == 0) ? 0 : ramp_A(J - 1, NS), a1 = ramp_A(J, NS);
#pragma unroll
    for (int s = a0; s < a1; ++s) issue(s, s % NS);
    wait_vm<P*(a1 - (J + 1))>();
    bar();
    prod_ramp<NS, P, J + 1, R>(issue);
  }
}
template <int NS, int NK, int P, class Issue, class Mid, class St>
__device__ __forceinline__ void producer(Issue issue, Mid mid, St stamp) {
  stamp(IC<1>{});
  constexpr int R = ramp_len(NS);
  static_assert(R - 1 + NS <= NK, "ramp too long");
  prod_ramp<NS, P, 0, R>(issue);
#pragma unroll
  for (int s = ramp_A(R - 1, NS); s < R - 1 + NS; ++s) issue(s, s % NS);
  mid();
  int wr = (R - 1) % NS;
  for (int i = R - 1; i < NK - NS; ++i) {
    wait_vm<P*(NS - 2)>();
    bar();
    issue(i + NS, wr);
    wr = (wr + 1 == NS) ? 0 : wr + 1;
  }
  stamp(IC<2>{});
  prod_tail<NS, P, 0>();
  stamp(IC<3>{});
}
template <int NS, int NK, int PFI, class Load0, class Body, class Pf, class St>
__device__ __forceinline__ void consumer(Load0 load0, Body body, Pf pf, St stamp) {
  static_assert((NK & 1) == 0 && (PFI & 1) == 0, "even number of stages");
  bar();
  stamp(IC<3>{});
  load0();
  int rd = 1;
  for (int i = 0; i < NK - 2; i += 2) {
    if (i == PFI) pf();
    wait_lgkm0();
    bar();
    SB0();
    body(IC<0>{}, IC<1>{}, rd);
    SB0();
    rd = (rd + 1 == NS) ? 0 : rd + 1;
    wait_lgkm0();
    bar();
    SB0();
    body(IC<1>{}, IC<1>{}, rd);
    SB0();
    rd = (rd + 1 == NS) ? 0 : rd + 1;
  }
  wait_lgkm0();
  bar();
  SB0();
  body(IC<0>{}, IC<1>{}, rd);
  SB0();
  stamp(IC<4>{});
  body(IC<1>{}, IC<0>{}, 0);
  SB0();
  stamp(IC<5>{});
}
__device__ __forceinline__ int swap23(int r) { return (r & ~12) | ((r & 4) << 1) | ((r & 8) >> 1); }

__global__ __launch_bounds__(256) void k1_ln_mod(const unsigned short* __restrict__ x,
                                                 const unsigned short* __restrict__ c,
                                                 const unsigned short* __restrict__ wmod,
                                                 const unsigned short* __restrict__ wre,
                                                 const unsigned short* __restrict__ wim,
                                                 const unsigned short* __restrict__ w2,
                                                 const unsigned short* __restrict__ dre,
                                                 const unsigned short* __restrict__ dim,
                                                 const unsigned short* __restrict__ w1,
                                                 char* __restrict__ ws) {
  __shared__ __attribute__((aligned(16))) char smem[16 * 1296 + 256];
  const int tid = threadIdx.x, lane = tid & 63;
  const int bid = blockIdx.x;
  if (bid < 128) {
    const int xcd = bid & 7, i = bid >> 3;
    const int rg = xcd * 4 + (i >> 2), sub = i & 3;
    const int row0 = rg * 64 + sub * 16;
    const int r = tid >> 4, j = tid & 15;
    const u32x4* src = (const u32x4*)(x + (size_t)(row0 + r) * D_);
    u32x4 v[5];
#pragma unroll
    for (int q = 0; q < 5; ++q) v[q] = src[j + 16 * q];
    float f[40];
#pragma unroll
    for (int q = 0; q < 5; ++q)
#pragma unroll
      for (int e = 0; e < 4; ++e) { f[q * 8 + 2 * e] = lo16(v[q][e]); f[q * 8 + 2 * e + 1] = hi16(v[q][e]); }
    float s = 0.f;
#pragma unroll
    for (int e = 0; e < 40; ++e) s += f[e];
#pragma unroll
    for (int o = 1; o < 16; o <<= 1) s += __shfl_xor(s, o, 64);
    const float mean = s * (1.0f / D_);
    float s2 = 0.f;
#pragma unroll
    for (int e = 0; e < 40; ++e) { float d = f[e] - mean; s2 += d * d; }
#pragma unroll
    for (int o = 1; o < 16; o <<= 1) s2 += __shfl_xor(s2, o, 64);
    const float mb = bfr(mean);
    const float vb = bfr(s2 * (1.0f / D_));
    const float rden = 1.0f / bfr(sqrtf(bfr(vb + 1e-5f)));
#pragma unroll
    for (int q = 0; q < 5; ++q) {
      u32x4 o;
#pragma unroll
      for (int e = 0; e < 4; ++e) {
        float a = bfr(f[q * 8 + 2 * e] - mb) * rden;
        float b = bfr(f[q * 8 + 2 * e + 1] - mb) * rden;
        o[e] = pk2(a, b);
      }
      *(u32x4*)(smem + r * 1296 + (j + 16 * q) * 16) = o;
    }
    wait_lgkm0();
    bar();
    const int grp = tid >> 4, li = tid & 15, qq = li >> 2, pp = li & 3;
    char* ht = ws + WS_HT;
#pragma unroll
    for (int it = 0; it < 5; ++it) {
      const int pr = it * 16 + grp;
      const int cblk = pr >> 1, half = pr & 1;
      const char* a0 = smem + (8 * half + qq) * 1296 + (16 * cblk + 4 * pp) * 2;
      s16x4 t0 = lds_tr(a0);
      s16x4 t1 = lds_tr(a0 + 4 * 1296);
      s16x8 o = __builtin_shufflevector(t0, t1, 0, 1, 2, 3, 4, 5, 6, 7);
      const int d = 16 * cblk + li;
      const int cb = d / 80, rr = d - cb * 80;
      const int chunk = 2 * sub + half;
      const int pos = chunk ^ ((rr >> 1) & 7);
      *(s16x8*)(ht + ((size_t)(cb * 32 + rg) * 80 + rr) * 128 + pos * 16) = o;
    }
    if (bid == 0 && tid < 96) *(u32x4*)(ws + WS_E + 2560 + tid * 16) = (u32x4){0u, 0u, 0u, 0u};
  } else if (bid < 368) {
    const int g = bid - 128;
    const int cgp = (g & 7) * 30 + (g >> 3);
    const int l2 = tid & 1, rg = tid >> 1;
    u32x4 v[5];
    unsigned short cv16[5];
#pragma unroll
    for (int q = 0; q < 5; ++q) {
      const int k = q * 128 + rg;
      v[q] = *(const u32x4*)(wmod + (size_t)k * MODN + cgp * 16 + l2 * 8);
      cv16[q] = c[k];
    }
    float acc[8];
#pragma unroll
    for (int e = 0; e < 8; ++e) acc[e] = 0.f;
#pragma unroll
    for (int q = 0; q < 5; ++q) {
      const float cv = bf2f(cv16[q]);
      const float sv = bfr(cv / (1.0f + __expf(-cv)));
#pragma unroll
      for (int e = 0; e < 4; ++e) {
        acc[2 * e] += sv * lo16(v[q][e]);
        acc[2 * e + 1] += sv * hi16(v[q][e]);
      }
    }
#pragma unroll
    for (int o = 2; o < 64; o <<= 1)
#pragma unroll
      for (int e = 0; e < 8; ++e) acc[e] += __shfl_xor(acc[e], o, 64);
    float* red = (float*)smem;
    if (lane < 2) {
#pragma unroll
      for (int e = 0; e < 8; ++e) red[(tid >> 6) * 16 + lane * 8 + e] = acc[e];
    }
    wait_lgkm0();
    bar();
    if (tid < 16) {
      float m = bfr((red[tid] + red[16 + tid]) + (red[32 + tid] + red[48 + tid]));
      const int n = cgp * 16 + tid;
      const int part = n / D_, d = n - part * D_;
      if (part == 1 || part == 4) m = bfr(1.0f + m);
      ((float*)(ws + WS_MOD))[n] = m;
      if (part == 4) ((unsigned short*)(ws + WS_E))[d] = (unsigned short)(__float_as_uint(m) >> 16);
      if (part == 3) ((unsigned short*)(ws + WS_E))[D_ + d] = (unsigned short)(__float_as_uint(m) >> 16);
    }
  } else if (bid >= 464) {
    const int pb = bid - 464, xcd = pb & 7;
    const int pw = (pb >> 3) * 4 + __builtin_amdgcn_readfirstlane(tid >> 6);
    u32x4 sink[16];
#pragma unroll
    for (int i = 0; i < 4; ++i) {
      const int pc = pw * 4 + i;
      const char* src;
      if (pc < 256) src = (const char*)wre + (size_t)xcd * (1u << 20) + pc * 4096;
      else if (pc < 512) src = (const char*)wim + (size_t)xcd * (1u << 20) + (pc - 256) * 4096;
      else {
        int e = (pc - 512) * 8 + xcd;
        if (e > 879) e = 879;
        src = e < 200 ? (const char*)dre + e * 4096 : (e < 400 ? (const char*)dim + (e - 200) * 4096 : (const char*)w1 + (e - 400) * 4096);
      }
#pragma unroll
      for (int q = 0; q < 4; ++q)
        asm volatile("global_load_dwordx4 %0, %1, %2 offset:%3" : "=v"(sink[4 * i + q]) : "v"(lane * 16), "s"(src), "n"(q * 1024) : "memory");
    }
    wait_vm<0>();
#pragma unroll
    for (int i = 0; i < 16; ++i) asm volatile("" ::"v"(sink[i]));
  } else {
    const int k0 = (bid - 368) * 16;
    u32x4 v[5];
#pragma unroll
    for (int q = 0; q < 5; ++q) v[q] = *(const u32x4*)((const char*)w2 + (size_t)k0 * (D_ * 2) + (tid + 256 * q) * 16);
#pragma unroll
    for (int q = 0; q < 5; ++q) {
      const int id = tid + 256 * q;
      const int r = id / 80, ch = id - r * 80;
      const int cb = ch / 10, cc = ch - cb * 10;
      *(u32x4*)(ws + WS_W2T + ((size_t)cb * F_ + k0 + swap23(r)) * 160 + cc * 16) = v[q];
    }
  }
}

struct SchedK2 { static constexpr char s[] = "MRMRMMRMRMMRMRMMRMRMMRMMMMMMMMMMM"; };
constexpr int K2_NS = 6, K2_STAGE = 128 * 128 + 80 * 128, K2_MOD = K2_NS * K2_STAGE, K2_LDS = K2_MOD + 2048;
constexpr int K2_T = 2 * K2_STAGE;
__global__ __launch_bounds__(512, 2) void k2_seq_dft(const unsigned short* __restrict__ wre,
                                                     const unsigned short* __restrict__ wim,
                                                     const unsigned short* __restrict__ dre,
                                                     const unsigned short* __restrict__ dim,
                                                     char* __restrict__ ws) {
  STAMP_DECL
  STAMP(0);
  extern __shared__ __attribute__((aligned(16))) char smem[];
  const int tid = threadIdx.x, lane = tid & 63;
  const int wv = __builtin_amdgcn_readfirstlane(tid >> 6);
  const int w = wv & 3;
  const int bid = blockIdx.x;
  const int xcd = bid & 7, bi = bid >> 3;
  const int cb = bi & 7, rsel = bi >> 3;
  const int isim = rsel >> 1;
  const int t0 = (xcd * 2 + (rsel & 1)) * 128;
  const int g = lane >> 4;
  const unsigned lds0 = lds_off(smem);
  auto stamp = [&](auto ic) { STAMP(decltype(ic)::value); };
  if (wv < 4) {
    const char* rbase = (const char*)(isim ? wim : wre) + (size_t)t0 * (S_ * 2);
    const char* cbase = ws + WS_HT + (size_t)cb * 32 * 10240;
    unsigned roff[4];
#pragma unroll
    for (int j = 0; j < 4; ++j) {
      const int row = 32 * w + 8 * j + (lane >> 3);
      roff[j] = row * (S_ * 2) + 16 * ((lane & 7) ^ ((row >> 1) & 7));
    }
    const unsigned coff = (20 * w) * 128 + lane * 16;
    if (w == 0) {
      const int l = lane < 40 ? lane : 39;
      dma16((l < 20 ? l * 16 : D_ * 4 + (l - 20) * 16) + cb * 320, ws + WS_MOD, lds0 + K2_MOD);
    }
    producer<K2_NS, 32, 7>([&](int ks, int buf) {
      const unsigned lb = lds0 + buf * K2_STAGE;
#pragma unroll
      for (int j = 0; j < 4; ++j) dma16(roff[j], rbase + ks * 128, lb + (32 * w + 8 * j) * 128);
      dma16(coff, cbase + ks * 10240, lb + 16384 + (20 * w) * 128);
      dma16(coff, cbase + ks * 10240 + 1024, lb + 16384 + (20 * w + 8) * 128);
      dma16h(coff, cbase + ks * 10240 + 2048, lb + 16384 + (20 * w + 16) * 128);
    }, [] {}, stamp);
    PSTAMP_OUT(1);
  } else {
    const int wm = w & 1, wk = w >> 1;
    const int fsw = ((4 * wk + g) ^ ((lane & 15) >> 1)) * 16;
    const int ra = (64 * wm + (lane & 15)) * 128 + fsw;
    const int ca_ = 16384 + (lane & 15) * 128 + fsw;
    bf16x8 rf[2][4], cf[2][5];
    f32x4 acc[6][4];
#pragma unroll
    for (int j = 0; j < 6; ++j)
#pragma unroll
      for (int i = 0; i < 4; ++i) acc[j][i] = (f32x4){0.f, 0.f, 0.f, 0.f};
    const s16x8 ones_s = {0x3F80, 0x3F80, 0x3F80, 0x3F80, 0x3F80, 0x3F80, 0x3F80, 0x3F80};
    const bf16x8 ones = __builtin_bit_cast(bf16x8, ones_s);
    auto ldf = [&](auto par, auto ic, int buf) {
      constexpr int PP = decltype(par)::value, n = decltype(ic)::value;
      const char* lb = smem + buf * K2_STAGE;
      if constexpr (n == 0) rf[PP][0] = lds_frag(lb + ra);
      else if constexpr (n < 6) cf[PP][n - 1] = lds_frag(lb + ca_ + (n - 1) * 2048);
      else rf[PP][n - 5] = lds_frag(lb + ra + (n - 5) * 2048);
    };
    auto body = [&](auto par, auto dl, int rd) {
      constexpr int PP = decltype(par)::value, DL = decltype(dl)::value;
      auto fm = [&](auto mc) {
        constexpr int m = decltype(mc)::value, i = m / 6, j = m % 6;
        if constexpr (j < 5) acc[j][i] = MFMA16(cf[PP][j], rf[PP][i], acc[j][i]);
        else acc[5][i] = MFMA16(ones, rf[PP][i], acc[5][i]);
      };
      auto fr = [&](auto rc) { ldf(IC<PP ^ 1>{}, rc, rd); };
      run_sched<SchedK2, 0, 0, 0, DL>(fm, fr);
    };
    auto load0 = [&] { static_for<9>([&](auto ic) { ldf(IC<0>{}, ic, 0); }); };
    auto pf = [] {};
    consumer<K2_NS, 32, 24>(load0, body, pf, stamp);
    char* tp = smem + K2_T + (wk * 128 + 64 * wm + (lane & 15)) * 336 + g * 16;
#pragma unroll
    for (int i = 0; i < 4; ++i) {
#pragma unroll
      for (int j = 0; j < 5; ++j) *(f32x4*)(tp + i * 16 * 336 + j * 64) = acc[j][i];
      if (g == 0) *(float*)(tp + i * 16 * 336 + 320) = acc[5][i][0];
    }
    wait_vm<0>();
  }
  wait_lgkm0();
  bar();
  {
    const float sgn = isim ? -1.0f : 1.0f;
    char* pq = ws + WS_PQ + (size_t)t0 * 2560 + (isim * 640 + cb * 80) * 2;
    for (int id = tid; id < 1280; id += 512) {
      const int row = id / 10, c = id - row * 10;
      const char* a = smem + K2_T + row * 336 + c * 32;
      const char* b = a + 128 * 336;
      const float* mod = (const float*)(smem + K2_MOD) + c * 8;
      const float rs = (*(const float*)(a - c * 32 + 320) + *(const float*)(b - c * 32 + 320)) * sgn;
      f32x4 v0 = (*(const f32x4*)a + *(const f32x4*)b) * (*(const f32x4*)(mod + 80) * sgn) + *(const f32x4*)mod * rs;
      f32x4 v1 = (*(const f32x4*)(a + 16) + *(const f32x4*)(b + 16)) * (*(const f32x4*)(mod + 84) * sgn) + *(const f32x4*)(mod + 4) * rs;
      u32x4 o = {pk2(v0[0], v0[1]), pk2(v0[2], v0[3]), pk2(v1[0], v1[1]), pk2(v1[2], v1[3])};
      *(u32x4*)(pq + (size_t)row * 2560 + c * 16) = o;
    }
  }
  STAMP(6);
  STAMP_OUT(1);
}

__device__ __forceinline__ void dma_res_tile(const char* base  , unsigned lds, int w, int lane) {
#pragma unroll
  for (int j = 0; j < 3; ++j) {
    int L = 160 * w + 64 * j + lane;
    if (L > 639) L = 639;
    const int r = L / 10, ch = L - r * 10;
    if (j < 2) dma16(r * (D_ * 2) + ch * 16, base, lds + (160 * w + 64 * j) * 16);
    else dma16h(r * (D_ * 2) + ch * 16, base, lds + (160 * w + 128) * 16);
  }
}

struct SchedK3 { static constexpr char s[] = "MRMRMRMRMRMRMRMRMRMRMRMRMRMRMMMMMM"; };
constexpr int K3_NS = 4, K3_STAGE = 36864, K3_MOD = K3_NS * K3_STAGE, K3_RES = K3_MOD + 1024, K3_LDS = K3_RES + 11264;
constexpr int K3_T = 2 * K3_STAGE;
__global__ __launch_bounds__(512, 2) void k3_hid_dft(const unsigned short* __restrict__ dre,
                                                     const unsigned short* __restrict__ dim,
                                                     const unsigned short* __restrict__ x,
                                                     const unsigned short* __restrict__ w1,
                                                     char* __restrict__ ws) {
  STAMP_DECL
  STAMP(0);
  extern __shared__ __attribute__((aligned(16))) char smem[];
  const int tid = threadIdx.x, lane = tid & 63;
  const int wv = __builtin_amdgcn_readfirstlane(tid >> 6);
  const int w = wv & 3;
  const int bid = blockIdx.x;
  const int xcd = bid & 7, bi = bid >> 3;
  const int cb = bi & 7, rb = xcd * 4 + (bi >> 3);
  const int t0 = rb * 64;
  const int g = lane >> 4;
  const unsigned lds0 = lds_off(smem);
  auto stamp = [&](auto ic) { STAMP(decltype(ic)::value); };
  if (wv < 4) {
    const char* rbase = ws + WS_PQ + (size_t)t0 * 2560;
    const char* cre = (const char*)dre + (size_t)cb * 80 * 1280;
    const char* cim = (const char*)dim + (size_t)cb * 80 * 1280 - 1280;
    unsigned roff[2], coff[5];
#pragma unroll
    for (int j = 0; j < 2; ++j) {
      const int row = 16 * w + 8 * j + (lane >> 3);
      roff[j] = row * 2560 + 16 * ((lane & 7) ^ ((row >> 1) & 7));
    }
#pragma unroll
    for (int j = 0; j < 5; ++j) {
      const int rs = 40 * w + 8 * j + (lane >> 3);
      const int h = rs >= 80, row = rs - 80 * h;
      coff[j] = row * 1280 + h * 128 + 16 * ((lane & 7) ^ ((row >> 1) & 7));
    }
    producer<K3_NS, 10, 9>([&](int ks, int buf) {
      const unsigned lb = lds0 + buf * K3_STAGE;
      const char* cgp = (ks < 5 ? cre : cim) + ks * 256;
      dma16(roff[0], rbase + ks * 256, lb + (16 * w) * 128);
      dma16(roff[1], rbase + ks * 256, lb + (16 * w + 8) * 128);
      dma16(roff[0], rbase + ks * 256 + 128, lb + 8192 + (16 * w) * 128);
      dma16(roff[1], rbase + ks * 256 + 128, lb + 8192 + (16 * w + 8) * 128);
#pragma unroll
      for (int j = 0; j < 5; ++j) dma16(coff[j], cgp, lb + 16384 + (40 * w + 8 * j) * 128);
    }, [] {}, stamp);
    PSTAMP_OUT(2);
  } else {
    if (w == 0) {
      const int l = lane < 40 ? lane : 39;
      dma16((l < 20 ? 2 * D_ * 4 + l * 16 : 4 * D_ * 4 + (l - 20) * 16) + cb * 320, ws + WS_MOD, lds0 + K3_MOD);
    }
    const int wm = w & 1, wk = w >> 1;
    const int fsw = ((4 * wk + g) ^ ((lane & 15) >> 1)) * 16;
    const int ra = (32 * wm + (lane & 15)) * 128 + fsw;
    const int ca_ = 16384 + (lane & 15) * 128 + fsw;
    bf16x8 rf[2][2][2], cf[2][2][5];
    f32x4 acc[5][2];
#pragma unroll
    for (int j = 0; j < 5; ++j) { acc[j][0] = (f32x4){0.f, 0.f, 0.f, 0.f}; acc[j][1] = (f32x4){0.f, 0.f, 0.f, 0.f}; }
    auto ldf = [&](auto par, auto ic, int buf) {
      constexpr int PP = decltype(par)::value, n = decltype(ic)::value, h = n / 7, m = n % 7;
      const char* lb = smem + buf * K3_STAGE;
      if constexpr (m == 0) rf[PP][h][0] = lds_frag(lb + ra + h * 8192);
      else if constexpr (m < 6) cf[PP][h][m - 1] = lds_frag(lb + ca_ + h * 10240 + (m - 1) * 2048);
      else rf[PP][h][1] = lds_frag(lb + ra + h * 8192 + 2048);
    };
    auto body = [&](auto par, auto dl, int rd) {
      constexpr int PP = decltype(par)::value, DL = decltype(dl)::value;
      auto fm = [&](auto mc) {
        constexpr int m = decltype(mc)::value, h = m / 10, i = (m % 10) / 5, j = m % 5;
        acc[j][i] = MFMA16(cf[PP][h][j], rf[PP][h][i], acc[j][i]);
      };
      auto fr = [&](auto rc) { ldf(IC<PP ^ 1>{}, rc, rd); };
      run_sched<SchedK3, 0, 0, 0, DL>(fm, fr);
    };
    auto load0 = [&] { static_for<14>([&](auto ic) { ldf(IC<0>{}, ic, 0); }); };
    auto pf = [&] {
      dma_res_tile((const char*)x + (size_t)t0 * (D_ * 2) + cb * 160, lds0 + K3_RES, w, lane);
    };
    consumer<K3_NS, 10, 2>(load0, body, pf, stamp);
    char* tp = smem + K3_T + (wk * 64 + 32 * wm + (lane & 15)) * 336 + g * 16;
#pragma unroll
    for (int i = 0; i < 2; ++i)
#pragma unroll
      for (int j = 0; j < 5; ++j) *(f32x4*)(tp + i * 16 * 336 + j * 64) = acc[j][i];
    wait_vm<0>();
  }
  wait_lgkm0();
  bar();
  {
    const int row = tid >> 3, c0 = tid & 7;
    const size_t ro = (size_t)(t0 + row) * (D_ * 2) + cb * 160;
    float xf[2][8];
    float s = 0.f;
#pragma unroll
    for (int it = 0; it < 2; ++it) {
      const int c = c0 + 8 * it;
      if (c < 10) {
        const char* a = smem + K3_T + row * 336 + c * 32;
        const char* b = a + 64 * 336;
        const float* mod = (const float*)(smem + K3_MOD) + c * 8;
        const u32x4 xr = *(const u32x4*)(smem + K3_RES + row * 160 + c * 16);
        u32x4 o, oc;
#pragma unroll
        for (int hh = 0; hh < 2; ++hh) {
          f32x4 y = *(const f32x4*)(a + hh * 16) + *(const f32x4*)(b + hh * 16);
          f32x4 ga = *(const f32x4*)(mod + hh * 4);
          f32x4 c1 = *(const f32x4*)(mod + 80 + hh * 4);
          o[2 * hh] = pk2(lo16(xr[2 * hh]) + ga[0] * y[0], hi16(xr[2 * hh]) + ga[1] * y[1]);
          o[2 * hh + 1] = pk2(lo16(xr[2 * hh + 1]) + ga[2] * y[2], hi16(xr[2 * hh + 1]) + ga[3] * y[3]);
          const float r0 = lo16(o[2 * hh]), r1 = hi16(o[2 * hh]), r2 = lo16(o[2 * hh + 1]), r3 = hi16(o[2 * hh + 1]);
          oc[2 * hh] = pk2(r0 * c1[0], r1 * c1[1]); oc[2 * hh + 1] = pk2(r2 * c1[2], r3 * c1[3]);
          xf[it][4 * hh] = r0; xf[it][4 * hh + 1] = r1; xf[it][4 * hh + 2] = r2; xf[it][4 * hh + 3] = r3;
          s += (r0 + r1) + (r2 + r3);
        }
        *(u32x4*)(ws + WS_X1 + ro + c * 16) = o;
        *(u32x4*)(ws + WS_XC + ro + c * 16) = oc;
      }
    }
#pragma unroll
    for (int o = 1; o < 8; o <<= 1) s += __shfl_xor(s, o, 64);
    const float ml = s * (1.0f / 80.0f);
    float m2 = 0.f;
#pragma unroll
    for (int e = 0; e < 8; ++e) { float d = xf[0][e] - ml; m2 += d * d; }
    if (c0 < 2) {
#pragma unroll
      for (int e = 0; e < 8; ++e) { float d = xf[1][e] - ml; m2 += d * d; }
    }
#pragma unroll
    for (int o = 1; o < 8; o <<= 1) m2 += __shfl_xor(m2, o, 64);
    if (c0 == 0) *(f32x2*)(ws + WS_ST + (size_t)(t0 + row) * 64 + cb * 8) = (f32x2){s, m2};
  }
  STAMP(6);
  STAMP_OUT(2);
}

struct SchedK4 { static constexpr char s[] = "MRMRMRMMRMRMRMMRMRMMRMRMRMMRMRMRMMRMMMMMMM"; };
constexpr int K4_NS = 5, K4_STAGE = 16384 + 64 * 192, K4_E = K4_NS * K4_STAGE, K4_ST = K4_E + 4096, K4_RS = K4_ST + 8192,
              K4_UV = K4_RS + 1024, K4_LDS = K4_UV + 2048;
__global__ __launch_bounds__(512, 2) void k4_mlp1(const unsigned short* __restrict__ w1,
                                                  char* __restrict__ ws) {
  STAMP_DECL
  STAMP(0);
  extern __shared__ __attribute__((aligned(16))) char smem[];
  const int tid = threadIdx.x, lane = tid & 63;
  const int wv = __builtin_amdgcn_readfirstlane(tid >> 6);
  const int w = wv & 3;
  const int bid = blockIdx.x;
  const int xcd = bid & 7, bi = bid >> 3;
  const int cb = bi & 15, rb = xcd * 2 + (bi >> 4);
  const int t0 = rb * 128;
  const int g = lane >> 4, q = (lane & 15) >> 2, p4 = lane & 3;
  const unsigned lds0 = lds_off(smem);
  auto stamp = [&](auto ic) { STAMP(decltype(ic)::value); };
  if (wv < 4) {
    dma16(w * 1024 + lane * 16, ws + WS_E, lds0 + K4_E + w * 1024);
    dma16(w * 2048 + lane * 16, ws + WS_ST + (size_t)t0 * 64, lds0 + K4_ST + w * 2048);
    dma16(w * 2048 + 1024 + lane * 16, ws + WS_ST + (size_t)t0 * 64, lds0 + K4_ST + w * 2048 + 1024);
    const char* rbase = ws + WS_XC + (size_t)t0 * (D_ * 2);
    const char* cbase = (const char*)w1 + cb * 192;
    unsigned roff[4], coff[3];
#pragma unroll
    for (int j = 0; j < 4; ++j) {
      const int row = 32 * w + 8 * j + (lane >> 3);
      roff[j] = row * (D_ * 2) + 16 * ((lane & 7) ^ ((row >> 1) & 7));
    }
#pragma unroll
    for (int j = 0; j < 3; ++j) {
      const int L = 192 * w + 64 * j + lane;
      const int r = L / 12, pc = L - r * 12;
      const int c = pc ^ (((r >> 2) & 1) << 1);
      coff[j] = swap23(r) * (F_ * 2) + c * 16;
    }
    producer<K4_NS, 10, 7>([&](int ks, int buf) {
      const unsigned lb = lds0 + buf * K4_STAGE;
#pragma unroll
      for (int j = 0; j < 4; ++j) dma16(roff[j], rbase + ks * 128, lb + (32 * w + 8 * j) * 128);
#pragma unroll
      for (int j = 0; j < 3; ++j) dma16(coff[j], cbase + (size_t)ks * 64 * (F_ * 2), lb + 16384 + (192 * w + 64 * j) * 16);
    }, [&] {
      if (tid < 128) {
        const char* sp = smem + K4_ST + tid * 64;
        f32x4 p0 = *(const f32x4*)(sp), p1 = *(const f32x4*)(sp + 16), p2 = *(const f32x4*)(sp + 32), p3 = *(const f32x4*)(sp + 48);
        const float mean = (((p0[0] + p0[2]) + (p1[0] + p1[2])) + ((p2[0] + p2[2]) + (p3[0] + p3[2]))) * (1.0f / D_);
        float m2 = ((p0[1] + p0[3]) + (p1[1] + p1[3])) + ((p2[1] + p2[3]) + (p3[1] + p3[3]));
        float dd = 0.f, d;
        d = p0[0] * (1.0f / 80.0f) - mean; dd += d * d;
        d = p0[2] * (1.0f / 80.0f) - mean; dd += d * d;
        d = p1[0] * (1.0f / 80.0f) - mean; dd += d * d;
        d = p1[2] * (1.0f / 80.0f) - mean; dd += d * d;
        d = p2[0] * (1.0f / 80.0f) - mean; dd += d * d;
        d = p2[2] * (1.0f / 80.0f) - mean; dd += d * d;
        d = p3[0] * (1.0f / 80.0f) - mean; dd += d * d;
        d = p3[2] * (1.0f / 80.0f) - mean; dd += d * d;
        m2 += 80.0f * dd;
        const float rden = 1.0f / bfr(sqrtf(bfr(bfr(m2 * (1.0f / D_)) + 1e-5f)));
        *(f32x2*)(smem + K4_RS + tid * 8) = (f32x2){bfr(mean), rden};
      }
    }, stamp);
    PSTAMP_OUT(3);
  } else {
    const int wm = w & 1, wn = w >> 1;
    const int rpos = 16 * (g >> 1) + 4 * (g & 1) + q;
    const int xs = (rpos >> 2) & 1;
    int cadr[3];
#pragma unroll
    for (int j = 0; j < 3; ++j) {
      const int jj = 3 * wn + j;
      cadr[j] = 16384 + rpos * 192 + (2 * (jj ^ xs) + (p4 >> 1)) * 16 + (p4 & 1) * 8;
    }
    const int ra = (64 * wm + (lane & 15)) * 128;
    const int sw0 = (g ^ ((lane & 15) >> 1)) * 16, sw1 = ((4 + g) ^ ((lane & 15) >> 1)) * 16;
    int ea = K4_E + ((lane & 15) < 2 ? (lane & 15) : 2) * 1280 + g * 16 + wm * 64;
    bf16x8 rf[2][2][4], cf[2][2][3], ef[2], ce[2][3];
    f32x4 acc[3][4], acce[3];
#pragma unroll
    for (int j = 0; j < 3; ++j) {
      acce[j] = (f32x4){0.f, 0.f, 0.f, 0.f};
#pragma unroll
      for (int i = 0; i < 4; ++i) acc[j][i] = (f32x4){0.f, 0.f, 0.f, 0.f};
    }
    auto ldf = [&](auto par, auto ic, int buf) {
      constexpr int PP = decltype(par)::value, n = decltype(ic)::value, kk = n / 7, m = n % 7;
      const char* lb = smem + buf * K4_STAGE;
      if constexpr (n == 14) { ef[PP] = lds_frag(smem + ea); ea += 128; }
      else if constexpr (m == 0) rf[PP][kk][0] = lds_frag(lb + ra + (kk ? sw1 : sw0));
      else if constexpr (m < 4) cf[PP][kk][m - 1] = cat_frag(lds_tr(lb + cadr[m - 1] + kk * 6144), lds_tr(lb + cadr[m - 1] + kk * 6144 + 1536));
      else rf[PP][kk][m - 3] = lds_frag(lb + ra + (kk ? sw1 : sw0) + (m - 3) * 2048);
    };
    auto body = [&](auto par, auto dl, int rd) {
      constexpr int PP = decltype(par)::value, DL = decltype(dl)::value;
#pragma unroll
      for (int j = 0; j < 3; ++j) ce[PP][j] = wm ? cf[PP][1][j] : cf[PP][0][j];
      auto fm = [&](auto mc) {
        constexpr int m = decltype(mc)::value;
        if constexpr (m < 24) {
          constexpr int kk = m / 12, i = (m % 12) / 3, j = m % 3;
          acc[j][i] = MFMA16(cf[PP][kk][j], rf[PP][kk][i], acc[j][i]);
        } else {
          acce[m - 24] = MFMA16(ce[PP][m - 24], ef[PP], acce[m - 24]);
        }
      };
      auto fr = [&](auto rc) { ldf(IC<PP ^ 1>{}, rc, rd); };
      run_sched<SchedK4, 0, 0, 0, DL>(fm, fr);
    };
    auto load0 = [&] { static_for<15>([&](auto ic) { ldf(IC<0>{}, ic, 0); }); };
    auto pf = [] {};
    consumer<K4_NS, 10, 2>(load0, body, pf, stamp);
    char* tp = smem + (64 * wm + (lane & 15)) * 400 + (48 * wn + 4 * g) * 4;
#pragma unroll
    for (int i = 0; i < 4; ++i)
#pragma unroll
      for (int j = 0; j < 3; ++j) *(f32x4*)(tp + i * 16 * 400 + j * 64) = acc[j][i];
    if ((lane & 15) < 2) {
#pragma unroll
      for (int j = 0; j < 3; ++j)
        *(f32x4*)(smem + K4_UV + ((wm * 2 + (lane & 15)) * 96 + 48 * wn + 16 * j + 4 * g) * 4) = acce[j];
    }
    wait_vm<0>();
  }
  wait_lgkm0();
  bar();
  {
    char* h1 = ws + WS_H1 + (size_t)t0 * (F_ * 2) + cb * 192;
#pragma unroll
    for (int it = 0; it < 3; ++it) {
      const int id = tid + 512 * it;
      const int row = id / 12, c = id - row * 12;
      const char* a = smem + row * 400 + c * 32;
      const float* uv = (const float*)(smem + K4_UV) + c * 8;
      const f32x2 rs = *(const f32x2*)(smem + K4_RS + row * 8);
      u32x4 o;
#pragma unroll
      for (int hh = 0; hh < 2; ++hh) {
        f32x4 u = *(const f32x4*)(uv + hh * 4) + *(const f32x4*)(uv + 192 + hh * 4);
        f32x4 v = *(const f32x4*)(uv + 96 + hh * 4) + *(const f32x4*)(uv + 288 + hh * 4);
        f32x4 hv = (*(const f32x4*)(a + hh * 16) - u * rs[0]) * rs[1] + v;
        o[2 * hh] = pk2(fmaxf(hv[0], 0.f), fmaxf(hv[1], 0.f));
        o[2 * hh + 1] = pk2(fmaxf(hv[2], 0.f), fmaxf(hv[3], 0.f));
      }
      *(u32x4*)(h1 + (size_t)row * (F_ * 2) + c * 16) = o;
    }
  }
  STAMP(6);
  STAMP_OUT(3);
}

constexpr int K5_NS = 4, K5_STAGE = 36864, K5_MOD = K5_NS * K5_STAGE, K5_RES = K5_MOD + 1024, K5_LDS = K5_RES + 10240;
__global__ __launch_bounds__(512, 2) void k5_mlp2(char* __restrict__ ws,
                                                  unsigned short* __restrict__ out) {
  STAMP_DECL
  STAMP(0);
  extern __shared__ __attribute__((aligned(16))) char smem[];
  const int tid = threadIdx.x, lane = tid & 63;
  const int wv = __builtin_amdgcn_readfirstlane(tid >> 6);
  const int w = wv & 3;
  const int bid = blockIdx.x;
  const int xcd = bid & 7, bi = bid >> 3;
  const int cb = bi & 7, rb = xcd * 4 + (bi >> 3);
  const int t0 = rb * 64;
  const int g = lane >> 4, q = (lane & 15) >> 2, p4 = lane & 3;
  const unsigned lds0 = lds_off(smem);
  auto stamp = [&](auto ic) { STAMP(decltype(ic)::value); };
  if (wv < 4) {
    const char* rbase = ws + WS_H1 + (size_t)t0 * (F_ * 2);
    const char* cbase = ws + WS_W2T + (size_t)cb * F_ * 160 + w * 5120;
    unsigned roff[2];
#pragma unroll
    for (int j = 0; j < 2; ++j) {
      const int row = 16 * w + 8 * j + (lane >> 3);
      roff[j] = row * (F_ * 2) + 16 * ((lane & 7) ^ ((row >> 1) & 7));
    }
    const unsigned coff = lane * 16;
    producer<K5_NS, 12, 9>([&](int ks, int buf) {
      const unsigned lb = lds0 + buf * K5_STAGE;
      dma16(roff[0], rbase + ks * 256, lb + (16 * w) * 128);
      dma16(roff[1], rbase + ks * 256, lb + (16 * w + 8) * 128);
      dma16(roff[0], rbase + ks * 256 + 128, lb + 8192 + (16 * w) * 128);
      dma16(roff[1], rbase + ks * 256 + 128, lb + 8192 + (16 * w + 8) * 128);
#pragma unroll
      for (int j = 0; j < 5; ++j) dma16(coff, cbase + ks * 20480 + j * 1024, lb + 16384 + w * 5120 + j * 1024);
    }, [] {}, stamp);
    PSTAMP_OUT(4);
  } else {
    if (w == 0) {
      const int l = lane < 20 ? lane : 19;
      dma16(5 * D_ * 4 + l * 16 + cb * 320, ws + WS_MOD, lds0 + K5_MOD);
    }
    const int wm = w & 1, wk = w >> 1;
    const int fsw = ((4 * wk + g) ^ ((lane & 15) >> 1)) * 16;
    const int ra = (32 * wm + (lane & 15)) * 128 + fsw;
    const int ca_ = 16384 + (32 * wk + 16 * (g >> 1) + 4 * (g & 1) + q) * 160 + p4 * 8;
    bf16x8 rf[2][2][2], cf[2][2][5];
    f32x4 acc[5][2];
#pragma unroll
    for (int j = 0; j < 5; ++j) { acc[j][0] = (f32x4){0.f, 0.f, 0.f, 0.f}; acc[j][1] = (f32x4){0.f, 0.f, 0.f, 0.f}; }
    auto ldf = [&](auto par, auto ic, int buf) {
      constexpr int PP = decltype(par)::value, n = decltype(ic)::value, h = n / 7, m = n % 7;
      const char* lb = smem + buf * K5_STAGE;
      if constexpr (m == 0) rf[PP][h][0] = lds_frag(lb + ra + h * 8192);
      else if constexpr (m < 6) cf[PP][h][m - 1] = cat_frag(lds_tr(lb + ca_ + h * 10240 + (m - 1) * 32), lds_tr(lb + ca_ + h * 10240 + (m - 1) * 32 + 1280));
      else rf[PP][h][1] = lds_frag(lb + ra + h * 8192 + 2048);
    };
    auto body = [&](auto par, auto dl, int rd) {
      constexpr int PP = decltype(par)::value, DL = decltype(dl)::value;
      auto fm = [&](auto mc) {
        constexpr int m = decltype(mc)::value, h = m / 10, i = (m % 10) / 5, j = m % 5;
        acc[j][i] = MFMA16(cf[PP][h][j], rf[PP][h][i], acc[j][i]);
      };
      auto fr = [&](auto rc) { ldf(IC<PP ^ 1>{}, rc, rd); };
      run_sched<SchedK3, 0, 0, 0, DL>(fm, fr);
    };
    auto load0 = [&] { static_for<14>([&](auto ic) { ldf(IC<0>{}, ic, 0); }); };
    auto pf = [&] { dma_res_tile(ws + WS_X1 + (size_t)t0 * (D_ * 2) + cb * 160, lds0 + K5_RES, w, lane); };
    consumer<K5_NS, 12, 2>(load0, body, pf, stamp);
    char* tp = smem + (wk * 64 + 32 * wm + (lane & 15)) * 336 + g * 16;
#pragma unroll
    for (int i = 0; i < 2; ++i)
#pragma unroll
      for (int j = 0; j < 5; ++j) *(f32x4*)(tp + i * 16 * 336 + j * 64) = acc[j][i];
    wait_vm<0>();
  }
  wait_lgkm0();
  bar();
  for (int id = tid; id < 640; id += 512) {
    const int row = id / 10, c = id - row * 10;
    const char* a = smem + row * 336 + c * 32;
    const char* b = a + 64 * 336;
    const float* mod = (const float*)(smem + K5_MOD) + c * 8;
    const u32x4 xr = *(const u32x4*)(smem + K5_RES + row * 160 + c * 16);
    u32x4 o;
#pragma unroll
    for (int hh = 0; hh < 2; ++hh) {
      f32x4 y = *(const f32x4*)(a + hh * 16) + *(const f32x4*)(b + hh * 16);
      f32x4 gb = *(const f32x4*)(mod + hh * 4);
      o[2 * hh] = pk2(lo16(xr[2 * hh]) + gb[0] * y[0], hi16(xr[2 * hh]) + gb[1] * y[1]);
      o[2 * hh + 1] = pk2(lo16(xr[2 * hh + 1]) + gb[2] * y[2], hi16(xr[2 * hh + 1]) + gb[3] * y[3]);
    }
    *(u32x4*)((char*)out + (size_t)(t0 + row) * (D_ * 2) + cb * 160 + c * 16) = o;
  }
  STAMP(6);
  STAMP_OUT(4);
}

extern "C" void kernel_launch(void* const* d_in, const int* in_sizes, int n_in,
                              void* d_out, int out_size, void* d_ws, size_t ws_size,
                              hipStream_t stream) {
  const unsigned short* x    = (const unsigned short*)d_in[0];
  const unsigned short* wsre = (const unsigned short*)d_in[1];
  const unsigned short* wsim = (const unsigned short*)d_in[2];
  const unsigned short* wdre = (const unsigned short*)d_in[3];
  const unsigned short* wdim = (const unsigned short*)d_in[4];
  const unsigned short* w1   = (const unsigned short*)d_in[6];
  const unsigned short* w2   = (const unsigned short*)d_in[7];
  const unsigned short* c    = (const unsigned short*)d_in[8];
  const unsigned short* wmod = (const unsigned short*)d_in[9];
  char* ws = (char*)d_ws;
  unsigned short* out = (unsigned short*)d_out;
  (void)hipFuncSetAttribute((const void*)k2_seq_dft, hipFuncAttributeMaxDynamicSharedMemorySize, K2_LDS);
  (void)hipFuncSetAttribute((const void*)k3_hid_dft, hipFuncAttributeMaxDynamicSharedMemorySize, K3_LDS);
  (void)hipFuncSetAttribute((const void*)k4_mlp1, hipFuncAttributeMaxDynamicSharedMemorySize, K4_LDS);
  (void)hipFuncSetAttribute((const void*)k5_mlp2, hipFuncAttributeMaxDynamicSharedMemorySize, K5_LDS);
  k1_ln_mod<<<464 + 320, 256, 0, stream>>>(x, c, wmod, wsre, wsim, w2, wdre, wdim, w1, ws);
  k2_seq_dft<<<256, 512, K2_LDS, stream>>>(wsre, wsim, wdre, wdim, ws);
  k3_hid_dft<<<256, 512, K3_LDS, stream>>>(wdre, wdim, x, w1, ws);
  k4_mlp1<<<256, 512, K4_LDS, stream>>>(w1, ws);
  k5_mlp2<<<256, 512, K5_LDS, stream>>>(ws, out);
}
```

```cpp
#include <hip/hip_runtime.h>
#include <hip/hip_bf16.h>
#include <stdint.h>

typedef __attribute__((ext_vector_type(8))) __bf16 bf16x8;
typedef __attribute__((ext_vector_type(4))) __bf16 bf16x4;
typedef __attribute__((ext_vector_type(2))) __bf16 bf16x2;
typedef __attribute__((ext_vector_type(4))) float f32x4;
typedef __attribute__((ext_vector_type(2))) float f32x2;
typedef __attribute__((ext_vector_type(4))) short s16x4;
typedef __attribute__((ext_vector_type(8))) short s16x8;
typedef __attribute__((ext_vector_type(4))) unsigned u32x4;
typedef __attribute__((ext_vector_type(2))) unsigned u32x2;
typedef __attribute__((address_space(3))) s16x4* lds_s16x4_ptr;
typedef __attribute__((address_space(3))) void* lds_vptr;

constexpr int S_ = 2048, D_ = 640, F_ = 1536, MODN = 3840;

constexpr size_t WS_MOD = 0;
constexpr size_t WS_E   = 16384;
constexpr size_t WS_CNT = 20480;
constexpr size_t WS_ST  = 32768;
constexpr size_t WS_HT  = WS_ST + (size_t)S_ * 64;
constexpr size_t WS_PQ  = WS_HT + (size_t)D_ * S_ * 2;
constexpr size_t WS_XC  = WS_PQ + (size_t)S_ * 1280 * 2;
constexpr size_t WS_H1  = WS_XC + (size_t)S_ * D_ * 2;
constexpr size_t WS_W2T = WS_H1 + (size_t)S_ * F_ * 2;
constexpr size_t WS_W1T = WS_W2T + (size_t)F_ * D_ * 2;
constexpr size_t WS_WDT = WS_W1T + (size_t)F_ * D_ * 2;

#define STAMP_DECL
#define STAMP(i)
#define STAMP_OUT()
template <int V> struct IC { static constexpr int value = V; };
template <int N, int I = 0, class F> __device__ __forceinline__ void static_for(F f) {
  if constexpr (I < N) { f(IC<I>{}); static_for<N, I + 1>(f); }
}

__device__ __forceinline__ float bf2f(unsigned short u) { return __uint_as_float(((unsigned)u) << 16); }
__device__ __forceinline__ float bfr(float f) { return (float)(__bf16)f; }
__device__ __forceinline__ unsigned pk2(float a, float b) {
  bf16x2 v = __builtin_convertvector((f32x2){a, b}, bf16x2);
  return __builtin_bit_cast(unsigned, v);
}
__device__ __forceinline__ float lo16(unsigned u) { return __uint_as_float(u << 16); }
__device__ __forceinline__ float hi16(unsigned u) { return __uint_as_float(u & 0xffff0000u); }

__device__ __forceinline__ void dma16(unsigned voff, const char* sbase, unsigned lds) {
  asm volatile("s_mov_b32 m0, %2\n\ts_nop 0\n\tglobal_load_lds_dwordx4 %0, %1"
               ::"v"(voff), "s"(sbase), "s"(lds) : "memory");
}
__device__ __forceinline__ void dma16s(unsigned voff, const char* sbase, unsigned lds) {
  asm volatile("s_mov_b32 m0, %2\n\ts_nop 0\n\tglobal_load_lds_dwordx4 %0, %1 sc1"
               ::"v"(voff), "s"(sbase), "s"(lds) : "memory");
}
#define LD1(p) __builtin_nontemporal_load(p)
__device__ __forceinline__ void dma16h(unsigned voff, const char* sbase, unsigned lds) {
  asm volatile("s_mov_b32 m0, %2\n\ts_mov_b32 exec_hi, 0\n\tglobal_load_lds_dwordx4 %0, %1\n\ts_mov_b32 exec_hi, -1"
               ::"v"(voff), "s"(sbase), "s"(lds) : "memory");
}
__device__ __forceinline__ unsigned lds_off(const void* p) {
  return (unsigned)(uintptr_t)(__attribute__((address_space(3))) const char*)p;
}
#define SB0() __builtin_amdgcn_sched_barrier(0)
template <int N> __device__ __forceinline__ void wait_vm_lgkm0() {
  asm volatile("s_waitcnt vmcnt(%0) lgkmcnt(0)" ::"n"(N) : "memory");
}
__device__ __forceinline__ void bar() {
  asm volatile("" ::: "memory");
  __builtin_amdgcn_s_barrier();
  asm volatile("" ::: "memory");
}
__device__ __forceinline__ bf16x8 lds_frag(const char* p) { return *(const bf16x8*)p; }
__device__ __forceinline__ s16x4 lds_tr(const char* p) {
  return __builtin_amdgcn_ds_read_tr16_b64_v4i16((lds_s16x4_ptr)p);
}
__device__ __forceinline__ bf16x8 cat_frag(s16x4 a, s16x4 b) {
  s16x8 v = __builtin_shufflevector(a, b, 0, 1, 2, 3, 4, 5, 6, 7);
  return __builtin_bit_cast(bf16x8, v);
}
#define MFMA16(a, b, c) __builtin_amdgcn_mfma_f32_16x16x32_bf16((a), (b), (c), 0, 0, 0)

template <class Sc, int POS, int NM, int NR, int DL, class FM, class FR>
__device__ __forceinline__ void run_sched(FM& fm, FR& fr) {
  if constexpr (Sc::s[POS] != 0) {
    constexpr char c = Sc::s[POS];
    if constexpr (c == 'M') {
      fm(IC<NM>{}); SB0();
      run_sched<Sc, POS + 1, NM + 1, NR, DL>(fm, fr);
    } else {
      if constexpr (DL) { fr(IC<NR>{}); SB0(); }
      run_sched<Sc, POS + 1, NM, NR + 1, DL>(fm, fr);
    }
  }
}
template <int N> __device__ __forceinline__ void wait_vm() { asm volatile("s_waitcnt vmcnt(%0)" ::"n"(N) : "memory"); }
__device__ __forceinline__ void wait_lgkm0() { asm volatile("s_waitcnt lgkmcnt(0)" ::: "memory"); }

template <int NS, int P, int TI>
__device__ __forceinline__ void prod_tail() {
  if constexpr (TI < NS - 1) {
    wait_vm<P*(NS - 2 - TI)>();
    bar();
    prod_tail<NS, P, TI + 1>();
  }
}
constexpr int ramp_A(int j, int NS) {
  int a = 2;
  for (int i = 1; i <= j; ++i) { int lim = (i == 1) ? NS : (i - 1) + NS; a = (a + 2 < lim) ? a + 2 : lim; }
  return a;
}
constexpr int ramp_len(int NS) { int j = 1; while (ramp_A(j, NS) != ((j == 1) ? NS : (j - 1) + NS) || j < 2) ++j; return j; }
template <int NS, int P, int J, int R, class Issue>
__device__ __forceinline__ void prod_ramp(Issue& issue) {
  if constexpr (J < R) {
    constexpr int a0 = (J == 0) ? 0 : ramp_A(J - 1, NS), a1 = ramp_A(J, NS);
#pragma unroll
    for (int s = a0; s < a1; ++s) issue(s, s % NS);
    wait_vm<P*(a1 - (J + 1))>();
    bar();
    prod_ramp<NS, P, J + 1, R>(issue);
  }
}
template <int NS, int NK, int P, class Issue>
__device__ __forceinline__ void producer(Issue issue) {
  constexpr int R = ramp_len(NS);
  static_assert(R - 1 + NS <= NK, "ramp too long");
  prod_ramp<NS, P, 0, R>(issue);
#pragma unroll
  for (int s = ramp_A(R - 1, NS); s < R - 1 + NS; ++s) issue(s, s % NS);
  int wr = (R - 1) % NS;
#pragma nounroll
  for (int i = R - 1; i < NK - NS; ++i) {
    wait_vm<P*(NS - 2)>();
    bar();
    issue(i + NS, wr);
    wr = (wr + 1 == NS) ? 0 : wr + 1;
  }
  prod_tail<NS, P, 0>();
}
template <int NK, int PR, int PC, class IR, class ICC, class Mid>
__device__ __forceinline__ void producer_seam(IR issueR, ICC issueC, Mid mid) {
  constexpr int P = PR + PC;
  issueR(0, 0); issueR(1, 1);
  wait_vm<PR>(); bar();
  issueR(2, 2); issueR(3, 3);
  wait_vm<2 * PR>(); bar();
  mid();
  issueR(4, 0); issueC(4, 0);
  wait_vm<PR + P>(); bar();
  issueR(5, 1); issueC(5, 1);
  wait_vm<2 * P>(); bar();
#pragma nounroll
  for (int j = 4; j <= NK - 3; ++j) {
    issueR(j + 2, (j + 2) & 3); issueC(j + 2, (j + 2) & 3);
    wait_vm<2 * P>(); bar();
  }
  wait_vm<P>(); bar();
  wait_vm<0>(); bar();
}
template <int NS, int NK, class Load0, class Body>
__device__ __forceinline__ void consumer(Load0 load0, Body body) {
  static_assert((NK & 1) == 0, "even number of stages");
  bar();
  load0();
  int rd = 1;
#pragma nounroll
  for (int i = 0; i < NK - 2; i += 2) {
    wait_lgkm0();
    bar();
    SB0();
    body(IC<0>{}, IC<1>{}, rd);
    SB0();
    rd = (rd + 1 == NS) ? 0 : rd + 1;
    wait_lgkm0();
    bar();
    SB0();
    body(IC<1>{}, IC<1>{}, rd);
    SB0();
    rd = (rd + 1 == NS) ? 0 : rd + 1;
  }
  wait_lgkm0();
  bar();
  SB0();
  body(IC<0>{}, IC<1>{}, rd);
  SB0();
  body(IC<1>{}, IC<0>{}, 0);
  SB0();
}
template <int NK, class Load0, class Half>
__device__ __forceinline__ void consumer_half(Load0 load0, Half half) {
  bar();
  load0();
#pragma nounroll
  for (int s = 0; s < NK - 1; ++s) {
    SB0();
    half(IC<0>{}, IC<1>{}, s & 3);
    SB0();
    wait_lgkm0();
    bar();
    SB0();
    half(IC<1>{}, IC<1>{}, (s + 1) & 3);
    SB0();
  }
  half(IC<0>{}, IC<1>{}, (NK - 1) & 3);
  SB0();
  half(IC<1>{}, IC<0>{}, 0);
  SB0();
}
typedef __attribute__((address_space(1))) unsigned gu32;
__device__ __forceinline__ void st16_wt(void* p, u32x4 v) {
  asm volatile("global_store_dwordx4 %0, %1, off sc1\n\ts_nop 1" ::"v"(p), "v"(v) : "memory");
}
__device__ __forceinline__ void st8_wt(void* p, f32x2 v) {
  asm volatile("global_store_dwordx2 %0, %1, off sc1\n\ts_nop 1" ::"v"(p), "v"(v) : "memory");
}
__device__ __forceinline__ void wait_count(char* ws, int idx, unsigned need) {
  gu32* cnt = (gu32*)(ws + WS_CNT + idx * 256);
#pragma nounroll
  for (unsigned spins = 0; spins < (1u << 22); ++spins) {
    if ((unsigned)__builtin_amdgcn_readfirstlane(__hip_atomic_load(cnt, __ATOMIC_RELAXED, __HIP_MEMORY_SCOPE_AGENT)) >= need) break;
    __builtin_amdgcn_s_sleep(1);
  }
  asm volatile("" ::: "memory");
}
__device__ __forceinline__ void add_count(char* ws, int idx) {
  __hip_atomic_fetch_add((gu32*)(ws + WS_CNT + idx * 256), 1u, __ATOMIC_RELAXED, __HIP_MEMORY_SCOPE_AGENT);
}
__device__ __forceinline__ int swap23(int r) { return (r & ~12) | ((r & 4) << 1) | ((r & 8) >> 1); }

__global__ __launch_bounds__(256) void k1_ln_mod(const unsigned short* __restrict__ x,
                                                 const unsigned short* __restrict__ c,
                                                 const unsigned short* __restrict__ wmod,
                                                 const unsigned short* __restrict__ wre,
                                                 const unsigned short* __restrict__ wim,
                                                 const unsigned short* __restrict__ w2,
                                                 const unsigned short* __restrict__ dre,
                                                 const unsigned short* __restrict__ dim,
                                                 const unsigned short* __restrict__ w1,
                                                 char* __restrict__ ws) {
  __shared__ __attribute__((aligned(16))) char smem[16 * 1296 + 256];
  const int tid = threadIdx.x, lane = tid & 63;
  const int bid = blockIdx.x;
  if (bid < 128) {
    const int xcd = bid & 7, i = bid >> 3;
    const int rg = xcd * 4 + (i >> 2), sub = i & 3;
    const int row0 = rg * 64 + sub * 16;
    const int r = tid >> 4, j = tid & 15;
    const u32x4* src = (const u32x4*)(x + (size_t)(row0 + r) * D_);
    u32x4 v[5];
#pragma unroll
    for (int q = 0; q < 5; ++q) v[q] = LD1(src + j + 16 * q);
    float f[40];
#pragma unroll
    for (int q = 0; q < 5; ++q)
#pragma unroll
      for (int e = 0; e < 4; ++e) { f[q * 8 + 2 * e] = lo16(v[q][e]); f[q * 8 + 2 * e + 1] = hi16(v[q][e]); }
    float s = 0.f;
#pragma unroll
    for (int e = 0; e < 40; ++e) s += f[e];
#pragma unroll
    for (int o = 1; o < 16; o <<= 1) s += __shfl_xor(s, o, 64);
    const float mean = s * (1.0f / D_);
    float s2 = 0.f;
#pragma unroll
    for (int e = 0; e < 40; ++e) { float d = f[e] - mean; s2 += d * d; }
#pragma unroll
    for (int o = 1; o < 16; o <<= 1) s2 += __shfl_xor(s2, o, 64);
    const float mb = bfr(mean);
    const float vb = bfr(s2 * (1.0f / D_));
    const float rden = 1.0f / bfr(sqrtf(bfr(vb + 1e-5f)));
#pragma unroll
    for (int q = 0; q < 5; ++q) {
      u32x4 o;
#pragma unroll
      for (int e = 0; e < 4; ++e) {
        float a = bfr(f[q * 8 + 2 * e] - mb) * rden;
        float b = bfr(f[q * 8 + 2 * e + 1] - mb) * rden;
        o[e] = pk2(a, b);
      }
      *(u32x4*)(smem + r * 1296 + (j + 16 * q) * 16) = o;
    }
    wait_lgkm0();
    bar();
    const int grp = tid >> 4, li = tid & 15, qq = li >> 2, pp = li & 3;
    char* ht = ws + WS_HT;
#pragma unroll
    for (int it = 0; it < 5; ++it) {
      const int pr = it * 16 + grp;
      const int cblk = pr >> 1, half = pr & 1;
      const char* a0 = smem + (8 * half + qq) * 1296 + (16 * cblk + 4 * pp) * 2;
      s16x4 t0 = lds_tr(a0);
      s16x4 t1 = lds_tr(a0 + 4 * 1296);
      s16x8 o = __builtin_shufflevector(t0, t1, 0, 1, 2, 3, 4, 5, 6, 7);
      const int d = 16 * cblk + li;
      const int cb = d / 80, rr = d - cb * 80;
      const int chunk = 2 * sub + half;
      const int pos = chunk ^ ((rr >> 1) & 7);
      *(s16x8*)(ht + ((size_t)(cb * 32 + rg) * 80 + rr) * 128 + pos * 16) = o;
    }
    if (bid == 0 && tid < 96) *(u32x4*)(ws + WS_E + 2560 + tid * 16) = (u32x4){0u, 0u, 0u, 0u};
    if (bid == 1 && tid < 48) *(unsigned*)(ws + WS_CNT + tid * 256) = 0u;
  } else if (bid < 368) {
    const int g = bid - 128;
    const int cgp = (g & 7) * 30 + (g >> 3);
    const int l2 = tid & 1, rg = tid >> 1;
    u32x4 v[5];
    unsigned short cv16[5];
#pragma unroll
    for (int q = 0; q < 5; ++q) {
      const int k = q * 128 + rg;
      v[q] = *(const u32x4*)(wmod + (size_t)k * MODN + cgp * 16 + l2 * 8);
      cv16[q] = c[k];
    }
    float acc[8];
#pragma unroll
    for (int e = 0; e < 8; ++e) acc[e] = 0.f;
#pragma unroll
    for (int q = 0; q < 5; ++q) {
      const float cv = bf2f(cv16[q]);
      const float sv = bfr(cv / (1.0f + __expf(-cv)));
#pragma unroll
      for (int e = 0; e < 4; ++e) {
        acc[2 * e] += sv * lo16(v[q][e]);
        acc[2 * e + 1] += sv * hi16(v[q][e]);
      }
    }
#pragma unroll
    for (int o = 2; o < 64; o <<= 1)
#pragma unroll
      for (int e = 0; e < 8; ++e) acc[e] += __shfl_xor(acc[e], o, 64);
    float* red = (float*)smem;
    if (lane < 2) {
#pragma unroll
      for (int e = 0; e < 8; ++e) red[(tid >> 6) * 16 + lane * 8 + e] = acc[e];
    }
    wait_lgkm0();
    bar();
    if (tid < 16) {
      float m = bfr((red[tid] + red[16 + tid]) + (red[32 + tid] + red[48 + tid]));
      const int n = cgp * 16 + tid;
      const int part = n / D_, d = n - part * D_;
      if (part == 1 || part == 4) m = bfr(1.0f + m);
      ((float*)(ws + WS_MOD))[n] = m;
      if (part == 4) ((unsigned short*)(ws + WS_E))[d] = (unsigned short)(__float_as_uint(m) >> 16);
      if (part == 3) ((unsigned short*)(ws + WS_E))[D_ + d] = (unsigned short)(__float_as_uint(m) >> 16);
    }
  } else if (bid < 464) {
    const int k0 = (bid - 368) * 16;
    u32x4 v[5];
#pragma unroll
    for (int q = 0; q < 5; ++q) v[q] = LD1((const u32x4*)((const char*)w2 + (size_t)k0 * (D_ * 2) + (tid + 256 * q) * 16));
#pragma unroll
    for (int q = 0; q < 5; ++q) {
      const int id = tid + 256 * q;
      const int r = id / 80, ch = id - r * 80;
      const int cb = ch / 10, cc = ch - cb * 10;
      *(u32x4*)(ws + WS_W2T + ((size_t)cb * F_ + k0 + swap23(r)) * 160 + cc * 16) = v[q];
    }
  } else if (bid < 560) {
    const int c0 = (bid - 464) * 1280;
    u32x4 v[5];
#pragma unroll
    for (int q = 0; q < 5; ++q) v[q] = LD1((const u32x4*)((const char*)w1 + (size_t)(c0 + tid + 256 * q) * 16));
#pragma unroll
    for (int q = 0; q < 5; ++q) {
      const int id = c0 + tid + 256 * q;
      const int k = id / 192, ch = id - k * 192;
      const int cb = ch / 12, cc = ch - cb * 12;
      const int r = swap23(k);
      *(u32x4*)(ws + WS_W1T + ((size_t)cb * D_ + r) * 192 + (cc ^ (((r >> 2) & 1) << 1)) * 16) = v[q];
    }
  } else if (bid < 640) {
    const int m = bid >= 600, c0 = (bid - 560 - 40 * m) * 1280;
    u32x4 v[5];
#pragma unroll
    for (int q = 0; q < 5; ++q) v[q] = LD1((const u32x4*)((const char*)(m ? dim : dre) + (size_t)(c0 + tid + 256 * q) * 16));
#pragma unroll
    for (int q = 0; q < 5; ++q) {
      const int id = c0 + tid + 256 * q;
      const int e = id / 80, ch = id - e * 80;
      const int cb = e / 80, row = e - cb * 80;
      *(u32x4*)(ws + WS_WDT + ((size_t)(cb * 20 + m * 10 + (ch >> 3)) * 80 + row) * 128 + ((ch & 7) ^ ((row >> 1) & 7)) * 16) = v[q];
    }
  } else {
    const int pw = (bid - 640) * 4 + __builtin_amdgcn_readfirstlane(tid >> 6);
    const char* src = (const char*)(pw < 512 ? wre : wim) + (size_t)(pw & 511) * 16384;
    u32x4 sink[16];
#pragma unroll
    for (int i = 0; i < 4; ++i)
#pragma unroll
      for (int q = 0; q < 4; ++q)
        asm volatile("global_load_dwordx4 %0, %1, %2 offset:%3" : "=v"(sink[4 * i + q]) : "v"(lane * 16), "s"(src + i * 4096), "n"(q * 1024) : "memory");
    wait_vm<0>();
#pragma unroll
    for (int i = 0; i < 16; ++i) asm volatile("" ::"v"(sink[i]));
  }
}

__device__ __forceinline__ void dma_res_tile(const char* base  , unsigned lds, int w, int lane) {
#pragma unroll
  for (int j = 0; j < 3; ++j) {
    int L = 160 * w + 64 * j + lane;
    if (L > 639) L = 639;
    const int r = L / 10, ch = L - r * 10;
    if (j < 2) dma16(r * (D_ * 2) + ch * 16, base, lds + (160 * w + 64 * j) * 16);
    else dma16h(r * (D_ * 2) + ch * 16, base, lds + (160 * w + 128) * 16);
  }
}
template <int N>
__device__ __forceinline__ void l2_warm(const char* base, unsigned dummy_lds, int lane) {
#pragma unroll
  for (int i = 0; i < N; ++i) dma16(lane * 16, base + i * 1024, dummy_lds);
}

struct SchedP2 { static constexpr char s[] = "MRMRMMRMRMMRMRMMRMRMMRMMMMMMMMMMM"; };
struct SchedP3 { static constexpr char s[] = "RRMRMRRMRMRRMRMRRMRMMM"; };
struct SchedP4 { static constexpr char s[] = "MRMRMRMMRMRMRMMRMRMMRMRMRMMRMRMRMMRMRMMMMMMMMM"; };
constexpr int cnt_ch(const char* s, char c) { int n = 0; for (; *s; ++s) n += (*s == c); return n; }
static_assert(cnt_ch(SchedP2::s, 'M') == 24 && cnt_ch(SchedP2::s, 'R') == 9, "");
static_assert(cnt_ch(SchedP3::s, 'M') == 10 && cnt_ch(SchedP3::s, 'R') == 12, "");
static_assert(cnt_ch(SchedP4::s, 'M') == 30 && cnt_ch(SchedP4::s, 'R') == 16, "");
constexpr int L_RES = 147456, L_MOD = L_RES + 10240, L_DUM = L_MOD + 2048, L_TOTAL = L_DUM + 1024;
constexpr int P2_NS = 5, P2_STAGE = 26624, P2_X = 2 * P2_STAGE, P2_STG = P2_X + 49152;
constexpr int P3_STAGE = 36864;
constexpr int P4_STAGE = 28672, P4_E = 4 * P4_STAGE, P4_ST = P4_E + 4096, P4_RS = P4_ST + 8192, P4_STG = 2 * P4_STAGE;

__global__ __launch_bounds__(512, 2) void mega(const unsigned short* __restrict__ wre,
                                               const unsigned short* __restrict__ wim,
                                               const unsigned short* __restrict__ x,
                                               char* __restrict__ ws,
                                               unsigned short* __restrict__ out) {
  STAMP_DECL
  STAMP(0);
  extern __shared__ __attribute__((aligned(16))) char smem[];
  const int tid = threadIdx.x, lane = tid & 63;
  const int wv = __builtin_amdgcn_readfirstlane(tid >> 6);
  const int w = wv & 3;
  const bool prod = wv < 4;
  const int bid = blockIdx.x;
  const int xcd = bid & 7, bi = bid >> 3;
  const int cb = bi & 7, rsel = bi >> 3;
  const int g = lane >> 4, l15 = lane & 15;
  const unsigned lds0 = lds_off(smem);
  const float* mod = (const float*)(smem + L_MOD);
  const int t3 = (xcd * 4 + rsel) * 64;
  const int grp3 = xcd * 2 + (rsel >> 1);

  {
    const int isim = rsel >> 1;
    const int grp2 = xcd * 2 + (rsel & 1);
    const int t0 = grp2 * 128;
    if (prod) {
      const char* rbase = (const char*)(isim ? wim : wre) + (size_t)t0 * (S_ * 2);
      const char* cbase = ws + WS_HT + (size_t)cb * 32 * 10240;
      unsigned roff[4];
#pragma unroll
      for (int j = 0; j < 4; ++j) {
        const int row = 32 * w + 8 * j + (lane >> 3);
        roff[j] = row * (S_ * 2) + 16 * ((lane & 7) ^ ((row >> 1) & 7));
      }
      const unsigned coff = (20 * w) * 128 + lane * 16;
      producer<P2_NS, 32, 7>([&](int ks, int buf) {
        const unsigned lb = lds0 + buf * P2_STAGE;
#pragma unroll
        for (int j = 0; j < 4; ++j) dma16(roff[j], rbase + ks * 128, lb + (32 * w + 8 * j) * 128);
        dma16(coff, cbase + ks * 10240, lb + 16384 + (20 * w) * 128);
        dma16(coff, cbase + ks * 10240 + 1024, lb + 16384 + (20 * w + 8) * 128);
        dma16h(coff, cbase + ks * 10240 + 2048, lb + 16384 + (20 * w + 16) * 128);
      });
      bar();
    } else {
      if (w < 2) {
        int L = 64 * w + lane; if (L > 99) L = 99;
        const int vec = L / 20, part = vec + (vec >= 3);
        dma16(part * 2560 + cb * 320 + (L - vec * 20) * 16, ws + WS_MOD, lds0 + L_MOD + w * 1024);
      }
      dma_res_tile((const char*)x + (size_t)t3 * (D_ * 2) + cb * 160, lds0 + L_RES, w, lane);
      const int wm = w & 1, wk = w >> 1;
      const int fsw = ((4 * wk + g) ^ (l15 >> 1)) * 16;
      const int ra = (64 * wm + l15) * 128 + fsw;
      const int ca_ = 16384 + l15 * 128 + fsw;
      bf16x8 rf[2][4], cf[2][5];
      f32x4 acc[6][4];
#pragma unroll
      for (int j = 0; j < 6; ++j)
#pragma unroll
        for (int i = 0; i < 4; ++i) acc[j][i] = (f32x4){0.f, 0.f, 0.f, 0.f};
      const s16x8 ones_s = {0x3F80, 0x3F80, 0x3F80, 0x3F80, 0x3F80, 0x3F80, 0x3F80, 0x3F80};
      const bf16x8 ones = __builtin_bit_cast(bf16x8, ones_s);
      auto ldf = [&](auto par, auto ic, int buf) {
        constexpr int PP = decltype(par)::value, n = decltype(ic)::value;
        const char* lb = smem + buf * P2_STAGE;
        if constexpr (n == 0) rf[PP][0] = lds_frag(lb + ra);
        else if constexpr (n < 6) cf[PP][n - 1] = lds_frag(lb + ca_ + (n - 1) * 2048);
        else rf[PP][n - 5] = lds_frag(lb + ra + (n - 5) * 2048);
      };
      auto body = [&](auto par, auto dl, int rd) {
        constexpr int PP = decltype(par)::value, DL = decltype(dl)::value;
        auto fm = [&](auto mc) {
          constexpr int m = decltype(mc)::value, i = m / 6, j = m % 6;
          if constexpr (j < 5) acc[j][i] = MFMA16(cf[PP][j], rf[PP][i], acc[j][i]);
          else acc[5][i] = MFMA16(ones, rf[PP][i], acc[5][i]);
        };
        auto fr = [&](auto rc) { ldf(IC<PP ^ 1>{}, rc, rd); };
        run_sched<SchedP2, 0, 0, 0, DL>(fm, fr);
      };
      auto load0 = [&] { static_for<9>([&](auto ic) { ldf(IC<0>{}, ic, 0); }); };
      consumer<P2_NS, 32>(load0, body);
      STAMP(1);
      auto finish = [&](auto keepc) {
        constexpr int KP = decltype(keepc)::value;
        char* dst = smem + P2_X + w * 12288 + lane * 16;
#pragma unroll
        for (int j = 0; j < 5; ++j) {
          *(f32x4*)(dst + (2 * j) * 1024) = acc[j][2 * (KP ^ 1)];
          *(f32x4*)(dst + (2 * j + 1) * 1024) = acc[j][2 * (KP ^ 1) + 1];
        }
        *(f32x2*)(dst + 10240) = (f32x2){acc[5][2 * (KP ^ 1)][0], acc[5][2 * (KP ^ 1) + 1][0]};
        wait_vm<0>();
        wait_lgkm0();
        bar();
        const char* srcp = smem + P2_X + (w ^ 2) * 12288 + lane * 16;
        const float sgn = isim ? -1.0f : 1.0f;
        const f32x2 rp = *(const f32x2*)(srcp + 10240);
        const float rs0 = (acc[5][2 * KP][0] + rp[0]) * sgn, rs1 = (acc[5][2 * KP + 1][0] + rp[1]) * sgn;
        char* stg = smem + P2_STG + (64 * wm + 32 * KP + l15) * 176 + g * 8;
#pragma unroll
        for (int j = 0; j < 5; ++j) {
          const f32x4 sa = *(const f32x4*)(mod + 16 * j + 4 * g);
          const f32x4 c1 = *(const f32x4*)(mod + 80 + 16 * j + 4 * g) * sgn;
          const f32x4 v0 = c1 * (acc[j][2 * KP] + *(const f32x4*)(srcp + (2 * j) * 1024)) + sa * rs0;
          const f32x4 v1 = c1 * (acc[j][2 * KP + 1] + *(const f32x4*)(srcp + (2 * j + 1) * 1024)) + sa * rs1;
          *(u32x2*)(stg + j * 32) = (u32x2){pk2(v0[0], v0[1]), pk2(v0[2], v0[3])};
          *(u32x2*)(stg + 16 * 176 + j * 32) = (u32x2){pk2(v1[0], v1[1]), pk2(v1[2], v1[3])};
        }
      };
      if (wk == 0) finish(IC<0>{}); else finish(IC<1>{});
    }
    wait_lgkm0();
    bar();
    {
      char* pq = ws + WS_PQ + (size_t)t0 * 2560 + (isim * 640 + cb * 80) * 2;
      for (int id = tid; id < 1280; id += 512) {
        const int row = id / 10, c = id - row * 10;
        st16_wt(pq + (size_t)row * 2560 + c * 16, *(const u32x4*)(smem + P2_STG + row * 176 + c * 16));
      }
    }
    wait_lgkm0();
    bar();
    STAMP(2);
    const char* wdt = ws + WS_WDT + (size_t)cb * 204800;
    if (prod) {
#pragma unroll
      for (int s = 0; s < 4; ++s)
#pragma unroll
        for (int j = 0; j < 5; ++j)
          dma16(lane * 16, wdt + s * 20480 + w * 5120 + j * 1024, lds0 + s * P3_STAGE + 16384 + w * 5120 + j * 1024);
      wait_vm<20>();
    } else {
      wait_vm<0>();
    }
    bar();
    STAMP(3);
    if (wv == 4) {
      if (lane == 0) add_count(ws, grp2);
      wait_count(ws, grp3, 16);
    }
    bar();
  }

  {
    const char* wdt = ws + WS_WDT + (size_t)cb * 204800;
    if (prod) {
      const char* rbase = ws + WS_PQ + (size_t)t3 * 2560;
      unsigned roff[2];
#pragma unroll
      for (int j = 0; j < 2; ++j) {
        const int row = 16 * w + 8 * j + (lane >> 3);
        roff[j] = row * 2560 + 16 * ((lane & 7) ^ ((row >> 1) & 7));
      }
      STAMP(4);
      producer_seam<10, 4, 5>([&](int ks, int buf) {
        const unsigned lb = lds0 + buf * P3_STAGE;
        dma16s(roff[0], rbase + ks * 256, lb + (16 * w) * 128);
        dma16s(roff[1], rbase + ks * 256, lb + (16 * w + 8) * 128);
        dma16s(roff[0], rbase + ks * 256 + 128, lb + 8192 + (16 * w) * 128);
        dma16s(roff[1], rbase + ks * 256 + 128, lb + 8192 + (16 * w + 8) * 128);
      }, [&](int ks, int buf) {
#pragma unroll
        for (int j = 0; j < 5; ++j)
          dma16(lane * 16, wdt + ks * 20480 + w * 5120 + j * 1024, lds0 + buf * P3_STAGE + 16384 + w * 5120 + j * 1024);
      }, [] {});
    } else {
      const int sw0 = (g ^ (l15 >> 1)) * 16, sw1 = ((4 + g) ^ (l15 >> 1)) * 16;
      const int ra = (16 * w + l15) * 128, ca_ = 16384 + l15 * 128;
      bf16x8 rf[2][2], cf[2][2][5];
      f32x4 acc[5];
#pragma unroll
      for (int j = 0; j < 5; ++j) acc[j] = (f32x4){0.f, 0.f, 0.f, 0.f};
      auto ldf = [&](auto hc, auto ic, int buf) {
        constexpr int H = decltype(hc)::value, n = decltype(ic)::value, kk = n / 6, m = n % 6;
        const char* lb = smem + buf * P3_STAGE + (kk ? sw1 : sw0);
        if constexpr (m == 0) rf[H][kk] = lds_frag(lb + ra + H * 8192);
        else cf[H][kk][m - 1] = lds_frag(lb + ca_ + H * 10240 + (m - 1) * 2048);
      };
      auto half = [&](auto par, auto dl, int buf) {
        constexpr int PP = decltype(par)::value, DL = decltype(dl)::value;
        auto fm = [&](auto mc) {
          constexpr int m = decltype(mc)::value, kk = m / 5, j = m % 5;
          acc[j] = MFMA16(cf[PP][kk][j], rf[PP][kk], acc[j]);
        };
        auto fr = [&](auto rc) { ldf(IC<PP ^ 1>{}, rc, buf); };
        run_sched<SchedP3, 0, 0, 0, DL>(fm, fr);
      };
      auto load0 = [&] { STAMP(4); static_for<12>([&](auto ic) { ldf(IC<0>{}, ic, 0); }); };
      consumer_half<10>(load0, half);
      STAMP(5);
      char* res = smem + L_RES + (16 * w + l15) * 160 + g * 8;
#pragma unroll
      for (int j = 0; j < 5; ++j) {
        const u32x2 xr = *(const u32x2*)(res + j * 32);
        const f32x4 ga = *(const f32x4*)(mod + 160 + 16 * j + 4 * g);
        const f32x4 y = acc[j];
        *(u32x2*)(res + j * 32) = (u32x2){pk2(lo16(xr[0]) + ga[0] * y[0], hi16(xr[0]) + ga[1] * y[1]),
                                          pk2(lo16(xr[1]) + ga[2] * y[2], hi16(xr[1]) + ga[3] * y[3])};
      }
    }
    wait_lgkm0();
    bar();
    {
      const int row = tid >> 3, c0 = tid & 7;
      char* xc = ws + WS_XC + (size_t)(t3 + row) * (D_ * 2) + cb * 160;
      float xf[2][8];
      float s = 0.f;
#pragma unroll
      for (int it = 0; it < 2; ++it) {
        const int c = c0 + 8 * it;
        if (c < 10) {
          const u32x4 xr = *(const u32x4*)(smem + L_RES + row * 160 + c * 16);
          const f32x4 c10 = *(const f32x4*)(mod + 240 + c * 8), c11 = *(const f32x4*)(mod + 244 + c * 8);
#pragma unroll
          for (int e = 0; e < 4; ++e) { xf[it][2 * e] = lo16(xr[e]); xf[it][2 * e + 1] = hi16(xr[e]); s += xf[it][2 * e] + xf[it][2 * e + 1]; }
          const u32x4 oc = {pk2(xf[it][0] * c10[0], xf[it][1] * c10[1]), pk2(xf[it][2] * c10[2], xf[it][3] * c10[3]),
                            pk2(xf[it][4] * c11[0], xf[it][5] * c11[1]), pk2(xf[it][6] * c11[2], xf[it][7] * c11[3])};
          st16_wt(xc + c * 16, oc);
        }
      }
#pragma unroll
      for (int o = 1; o < 8; o <<= 1) s += __shfl_xor(s, o, 64);
      const float ml = s * (1.0f / 80.0f);
      float m2 = 0.f;
#pragma unroll
      for (int e = 0; e < 8; ++e) { float d = xf[0][e] - ml; m2 += d * d; }
      if (c0 < 2) {
#pragma unroll
        for (int e = 0; e < 8; ++e) { float d = xf[1][e] - ml; m2 += d * d; }
      }
#pragma unroll
      for (int o = 1; o < 8; o <<= 1) m2 += __shfl_xor(m2, o, 64);
      if (c0 == 0) st8_wt(ws + WS_ST + (size_t)(t3 + row) * 64 + cb * 8, (f32x2){s, m2});
    }
    STAMP(6);
    const char* w1t = ws + WS_W1T + (size_t)(bi & 15) * 122880;
    if (prod) {
      dma16(w * 1024 + lane * 16, ws + WS_E, lds0 + P4_E + w * 1024);
#pragma unroll
      for (int s = 0; s < 4; ++s)
#pragma unroll
        for (int j = 0; j < 3; ++j)
          dma16(lane * 16, w1t + s * 12288 + w * 3072 + j * 1024, lds0 + s * P4_STAGE + 16384 + w * 3072 + j * 1024);
      wait_vm<13>();
    } else {
      wait_vm<0>();
    }
    bar();
    STAMP(7);
    if (wv == 4) {
      if (lane == 0) add_count(ws, 16 + grp3);
      wait_count(ws, 16 + xcd * 2 + (bi >> 4), 16);
    }
    bar();
  }

  {
    const int cb4 = bi & 15, grp4 = xcd * 2 + (bi >> 4);
    const int t0 = grp4 * 128;
    const char* w1t = ws + WS_W1T + (size_t)cb4 * 122880;
    if (prod) {
      const char* rbase = ws + WS_XC + (size_t)t0 * (D_ * 2);
      unsigned roff[4];
#pragma unroll
      for (int j = 0; j < 4; ++j) {
        const int row = 32 * w + 8 * j + (lane >> 3);
        roff[j] = row * (D_ * 2) + 16 * ((lane & 7) ^ ((row >> 1) & 7));
      }
      STAMP(8);
      dma16s(w * 2048 + lane * 16, ws + WS_ST + (size_t)t0 * 64, lds0 + P4_ST + w * 2048);
      dma16s(w * 2048 + 1024 + lane * 16, ws + WS_ST + (size_t)t0 * 64, lds0 + P4_ST + w * 2048 + 1024);
      producer_seam<10, 4, 3>([&](int ks, int buf) {
#pragma unroll
        for (int j = 0; j < 4; ++j) dma16s(roff[j], rbase + ks * 128, lds0 + buf * P4_STAGE + (32 * w + 8 * j) * 128);
      }, [&](int ks, int buf) {
#pragma unroll
        for (int j = 0; j < 3; ++j)
          dma16(lane * 16, w1t + ks * 12288 + w * 3072 + j * 1024, lds0 + buf * P4_STAGE + 16384 + w * 3072 + j * 1024);
      }, [&] {
        if (tid < 128) {
          const char* sp = smem + P4_ST + tid * 64;
          f32x4 p0 = *(const f32x4*)(sp), p1 = *(const f32x4*)(sp + 16), p2 = *(const f32x4*)(sp + 32), p3 = *(const f32x4*)(sp + 48);
          const float mean = (((p0[0] + p0[2]) + (p1[0] + p1[2])) + ((p2[0] + p2[2]) + (p3[0] + p3[2]))) * (1.0f / D_);
          float m2 = ((p0[1] + p0[3]) + (p1[1] + p1[3])) + ((p2[1] + p2[3]) + (p3[1] + p3[3]));
          float dd = 0.f, d;
          d = p0[0] * (1.0f / 80.0f) - mean; dd += d * d;
          d = p0[2] * (1.0f / 80.0f) - mean; dd += d * d;
          d = p1[0] * (1.0f / 80.0f) - mean; dd += d * d;
          d = p1[2] * (1.0f / 80.0f) - mean; dd += d * d;
          d = p2[0] * (1.0f / 80.0f) - mean; dd += d * d;
          d = p2[2] * (1.0f / 80.0f) - mean; dd += d * d;
          d = p3[0] * (1.0f / 80.0f) - mean; dd += d * d;
          d = p3[2] * (1.0f / 80.0f) - mean; dd += d * d;
          m2 += 80.0f * dd;
          const float rden = 1.0f / bfr(sqrtf(bfr(bfr(m2 * (1.0f / D_)) + 1e-5f)));
          *(f32x2*)(smem + P4_RS + tid * 8) = (f32x2){bfr(mean), rden};
        }
        wait_lgkm0();
      });
    } else {
      const int q = l15 >> 2, p4 = lane & 3;
      const int wm = w & 1, wn = w >> 1;
      const int rpos = 16 * (g >> 1) + 4 * (g & 1) + q;
      const int xs = (rpos >> 2) & 1;
      int cadr[3];
#pragma unroll
      for (int j = 0; j < 3; ++j) {
        const int jj = 3 * wn + j;
        cadr[j] = 16384 + rpos * 192 + (2 * (jj ^ xs) + (p4 >> 1)) * 16 + (p4 & 1) * 8;
      }
      const int ra = (64 * wm + l15) * 128;
      const int sw0 = (g ^ (l15 >> 1)) * 16, sw1 = ((4 + g) ^ (l15 >> 1)) * 16;
      int ea = P4_E + (l15 < 2 ? l15 : 2) * 1280 + g * 16;
      bf16x8 rf[2][2][5], cf[2][2][3];
      f32x4 acc[3][5];
#pragma unroll
      for (int j = 0; j < 3; ++j)
#pragma unroll
        for (int i = 0; i < 5; ++i) acc[j][i] = (f32x4){0.f, 0.f, 0.f, 0.f};
      auto ldf = [&](auto par, auto ic, int buf) {
        constexpr int PP = decltype(par)::value, n = decltype(ic)::value, kk = n / 8, m = n % 8;
        const char* lb = smem + buf * P4_STAGE;
        if constexpr (m == 0) rf[PP][kk][0] = lds_frag(lb + ra + (kk ? sw1 : sw0));
        else if constexpr (m < 4) cf[PP][kk][m - 1] = cat_frag(lds_tr(lb + cadr[m - 1] + kk * 6144), lds_tr(lb + cadr[m - 1] + kk * 6144 + 1536));
        else if constexpr (m < 7) rf[PP][kk][m - 3] = lds_frag(lb + ra + (kk ? sw1 : sw0) + (m - 3) * 2048);
        else {
          rf[PP][kk][4] = lds_frag(smem + ea + kk * 64);
          if constexpr (kk == 1) ea += 128;
        }
      };
      auto body = [&](auto par, auto dl, int rd) {
        constexpr int PP = decltype(par)::value, DL = decltype(dl)::value;
        auto fm = [&](auto mc) {
          constexpr int m = decltype(mc)::value, kk = m / 15, i = (m % 15) / 3, j = m % 3;
          acc[j][i] = MFMA16(cf[PP][kk][j], rf[PP][kk][i], acc[j][i]);
        };
        auto fr = [&](auto rc) { ldf(IC<PP ^ 1>{}, rc, rd); };
        run_sched<SchedP4, 0, 0, 0, DL>(fm, fr);
      };
      auto load0 = [&] { STAMP(8); static_for<16>([&](auto ic) { ldf(IC<0>{}, ic, 0); }); };
      consumer<4, 10>(load0, body);
      STAMP(9);
      f32x4 u[3], v[3];
#pragma unroll
      for (int j = 0; j < 3; ++j)
#pragma unroll
        for (int e = 0; e < 4; ++e) {
          const int a = __float_as_int(acc[j][4][e]);
          u[j][e] = __int_as_float(__builtin_amdgcn_update_dpp(0, a, 0x150, 0xf, 0xf, false));
          v[j][e] = __int_as_float(__builtin_amdgcn_update_dpp(0, a, 0x151, 0xf, 0xf, false));
        }
      char* stg = smem + P4_STG + (64 * wm + l15) * 208 + (48 * wn + 4 * g) * 2;
#pragma unroll
      for (int i = 0; i < 4; ++i) {
        const f32x2 rs = *(const f32x2*)(smem + P4_RS + (64 * wm + 16 * i + l15) * 8);
#pragma unroll
        for (int j = 0; j < 3; ++j) {
          const f32x4 hv = (acc[j][i] - u[j] * rs[0]) * rs[1] + v[j];
          *(u32x2*)(stg + i * 16 * 208 + j * 32) =
              (u32x2){pk2(fmaxf(hv[0], 0.f), fmaxf(hv[1], 0.f)), pk2(fmaxf(hv[2], 0.f), fmaxf(hv[3], 0.f))};
        }
      }
    }
    wait_lgkm0();
    bar();
    {
      char* h1 = ws + WS_H1 + (size_t)t0 * (F_ * 2) + cb4 * 192;
#pragma unroll
      for (int it = 0; it < 3; ++it) {
        const int id = tid + 512 * it;
        const int row = id / 12, c = id - row * 12;
        st16_wt(h1 + (size_t)row * (F_ * 2) + c * 16, *(const u32x4*)(smem + P4_STG + row * 208 + c * 16));
      }
    }
    wait_lgkm0();
    bar();
    STAMP(10);
    const char* w2t = ws + WS_W2T + (size_t)cb * F_ * 160;
    if (prod) {
#pragma unroll
      for (int s = 0; s < 4; ++s)
#pragma unroll
        for (int j = 0; j < 5; ++j)
          dma16(lane * 16, w2t + s * 20480 + w * 5120 + j * 1024, lds0 + s * P3_STAGE + 16384 + w * 5120 + j * 1024);
      wait_vm<20>();
    } else {
      wait_vm<0>();
    }
    bar();
    STAMP(11);
    if (wv == 4) {
      if (lane == 0) add_count(ws, 32 + grp4);
      wait_count(ws, 32 + grp3, 16);
    }
    bar();
  }

  {
    const char* w2t = ws + WS_W2T + (size_t)cb * F_ * 160;
    if (prod) {
      const char* rbase = ws + WS_H1 + (size_t)t3 * (F_ * 2);
      unsigned roff[2];
#pragma unroll
      for (int j = 0; j < 2; ++j) {
        const int row = 16 * w + 8 * j + (lane >> 3);
        roff[j] = row * (F_ * 2) + 16 * ((lane & 7) ^ ((row >> 1) & 7));
      }
      STAMP(12);
      producer_seam<12, 4, 5>([&](int ks, int buf) {
        const unsigned lb = lds0 + buf * P3_STAGE;
        dma16s(roff[0], rbase + ks * 256, lb + (16 * w) * 128);
        dma16s(roff[1], rbase + ks * 256, lb + (16 * w + 8) * 128);
        dma16s(roff[0], rbase + ks * 256 + 128, lb + 8192 + (16 * w) * 128);
        dma16s(roff[1], rbase + ks * 256 + 128, lb + 8192 + (16 * w + 8) * 128);
      }, [&](int ks, int buf) {
#pragma unroll
        for (int j = 0; j < 5; ++j)
          dma16(lane * 16, w2t + ks * 20480 + w * 5120 + j * 1024, lds0 + buf * P3_STAGE + 16384 + w * 5120 + j * 1024);
      }, [] {});
    } else {
      const int q = l15 >> 2, p4 = lane & 3;
      const int sw0 = (g ^ (l15 >> 1)) * 16, sw1 = ((4 + g) ^ (l15 >> 1)) * 16;
      const int ra = (16 * w + l15) * 128;
      const int ca_ = 16384 + (16 * (g >> 1) + 4 * (g & 1) + q) * 160 + p4 * 8;
      bf16x8 rf[2][2], cf[2][2][5];
      f32x4 acc[5];
#pragma unroll
      for (int j = 0; j < 5; ++j) acc[j] = (f32x4){0.f, 0.f, 0.f, 0.f};
      auto ldf = [&](auto hc, auto ic, int buf) {
        constexpr int H = decltype(hc)::value, n = decltype(ic)::value, kk = n / 6, m = n % 6;
        const char* lb = smem + buf * P3_STAGE;
        if constexpr (m == 0) rf[H][kk] = lds_frag(lb + ra + H * 8192 + (kk ? sw1 : sw0));
        else cf[H][kk][m - 1] = cat_frag(lds_tr(lb + ca_ + H * 10240 + kk * 5120 + (m - 1) * 32),
                                         lds_tr(lb + ca_ + H * 10240 + kk * 5120 + (m - 1) * 32 + 1280));
      };
      auto half = [&](auto par, auto dl, int buf) {
        constexpr int PP = decltype(par)::value, DL = decltype(dl)::value;
        auto fm = [&](auto mc) {
          constexpr int m = decltype(mc)::value, kk = m / 5, j = m % 5;
          acc[j] = MFMA16(cf[PP][kk][j], rf[PP][kk], acc[j]);
        };
        auto fr = [&](auto rc) { ldf(IC<PP ^ 1>{}, rc, buf); };
        run_sched<SchedP3, 0, 0, 0, DL>(fm, fr);
      };
      auto load0 = [&] { STAMP(12); static_for<12>([&](auto ic) { ldf(IC<0>{}, ic, 0); }); };
      consumer_half<12>(load0, half);
      STAMP(13);
      char* res = smem + L_RES + (16 * w + l15) * 160 + g * 8;
#pragma unroll
      for (int j = 0; j < 5; ++j) {
        const u32x2 xr = *(const u32x2*)(res + j * 32);
        const f32x4 gb = *(const f32x4*)(mod + 320 + 16 * j + 4 * g);
        const f32x4 y = acc[j];
        *(u32x2*)(res + j * 32) = (u32x2){pk2(lo16(xr[0]) + gb[0] * y[0], hi16(xr[0]) + gb[1] * y[1]),
                                          pk2(lo16(xr[1]) + gb[2] * y[2], hi16(xr[1]) + gb[3] * y[3])};
      }
      wait_vm<0>();
    }
    wait_lgkm0();
    bar();
    for (int id = tid; id < 640; id += 512) {
      const int row = id / 10, c = id - row * 10;
      *(u32x4*)((char*)out + (size_t)(t3 + row) * (D_ * 2) + cb * 160 + c * 16) = *(const u32x4*)(smem + L_RES + row * 160 + c * 16);
    }
  }
  STAMP(14);
  STAMP_OUT();
}

extern "C" void kernel_launch(void* const* d_in, const int* in_sizes, int n_in,
                              void* d_out, int out_size, void* d_ws, size_t ws_size,
                              hipStream_t stream) {
  const unsigned short* x    = (const unsigned short*)d_in[0];
  const unsigned short* wsre = (const unsigned short*)d_in[1];
  const unsigned short* wsim = (const unsigned short*)d_in[2];
  const unsigned short* wdre = (const unsigned short*)d_in[3];
  const unsigned short* wdim = (const unsigned short*)d_in[4];
  const unsigned short* w1   = (const unsigned short*)d_in[6];
  const unsigned short* w2   = (const unsigned short*)d_in[7];
  const unsigned short* c    = (const unsigned short*)d_in[8];
  const unsigned short* wmod = (const unsigned short*)d_in[9];
  char* ws = (char*)d_ws;
  unsigned short* out = (unsigned short*)d_out;
  (void)hipFuncSetAttribute((const void*)mega, hipFuncAttributeMaxDynamicSharedMemorySize, L_TOTAL);
  k1_ln_mod<<<640 + 256, 256, 0, stream>>>(x, c, wmod, wsre, wsim, w2, wdre, wdim, w1, ws);
  mega<<<256, 512, L_TOTAL, stream>>>(wsre, wsim, x, ws, out);
}
```

```cpp
#include <hip/hip_runtime.h>
#include <hip/hip_bf16.h>
#include <stdint.h>

typedef __attribute__((ext_vector_type(8))) __bf16 bf16x8;
typedef __attribute__((ext_vector_type(4))) __bf16 bf16x4;
typedef __attribute__((ext_vector_type(2))) __bf16 bf16x2;
typedef __attribute__((ext_vector_type(4))) float f32x4;
typedef __attribute__((ext_vector_type(2))) float f32x2;
typedef __attribute__((ext_vector_type(4))) short s16x4;
typedef __attribute__((ext_vector_type(8))) short s16x8;
typedef __attribute__((ext_vector_type(4))) unsigned u32x4;
typedef __attribute__((ext_vector_type(2))) unsigned u32x2;
typedef __attribute__((address_space(3))) s16x4* lds_s16x4_ptr;
typedef __attribute__((address_space(3))) void* lds_vptr;

constexpr int S_ = 2048, D_ = 640, F_ = 1536, MODN = 3840;

constexpr size_t WS_MOD = 0;
constexpr size_t WS_E   = 16384;
constexpr size_t WS_CNT = 20480;
constexpr size_t WS_ST  = 32768;
constexpr size_t WS_HT  = WS_ST + (size_t)S_ * 64;
constexpr size_t WS_PQ  = WS_HT + (size_t)D_ * S_ * 2;
constexpr size_t WS_XC  = WS_PQ + (size_t)S_ * 1280 * 2;
constexpr size_t WS_H1  = WS_XC + (size_t)S_ * D_ * 2;
constexpr size_t WS_W2T = WS_H1 + (size_t)S_ * F_ * 2;
constexpr size_t WS_W1T = WS_W2T + (size_t)F_ * D_ * 2;
constexpr size_t WS_WDT = WS_W1T + (size_t)F_ * D_ * 2;

#define STAMP_DECL
#define STAMP(i)
#define STAMP_OUT()
template <int V> struct IC { static constexpr int value = V; };
template <int N, int I = 0, class F> __device__ __forceinline__ void static_for(F f) {
  if constexpr (I < N) { f(IC<I>{}); static_for<N, I + 1>(f); }
}

__device__ __forceinline__ float bf2f(unsigned short u) { return __uint_as_float(((unsigned)u) << 16); }
__device__ __forceinline__ float bfr(float f) { return (float)(__bf16)f; }
__device__ __forceinline__ unsigned pk2(float a, float b) {
  bf16x2 v = __builtin_convertvector((f32x2){a, b}, bf16x2);
  return __builtin_bit_cast(unsigned, v);
}
__device__ __forceinline__ float lo16(unsigned u) { return __uint_as_float(u << 16); }
__device__ __forceinline__ float hi16(unsigned u) { return __uint_as_float(u & 0xffff0000u); }

__device__ __forceinline__ void dma16(unsigned voff, const char* sbase, unsigned lds) {
  asm volatile("s_mov_b32 m0, %2\n\ts_nop 0\n\tglobal_load_lds_dwordx4 %0, %1"
               ::"v"(voff), "s"(sbase), "s"(lds) : "memory");
}
__device__ __forceinline__ void dma16nt(unsigned voff, const char* sbase, unsigned lds) {
  asm volatile("s_mov_b32 m0, %2\n\ts_nop 0\n\tglobal_load_lds_dwordx4 %0, %1 nt"
               ::"v"(voff), "s"(sbase), "s"(lds) : "memory");
}
__device__ __forceinline__ void dma16s(unsigned voff, const char* sbase, unsigned lds) {
  asm volatile("s_mov_b32 m0, %2\n\ts_nop 0\n\tglobal_load_lds_dwordx4 %0, %1 sc1"
               ::"v"(voff), "s"(sbase), "s"(lds) : "memory");
}
#define LD1(p) __builtin_nontemporal_load(p)
__device__ __forceinline__ void dma16h(unsigned voff, const char* sbase, unsigned lds) {
  asm volatile("s_mov_b32 m0, %2\n\ts_mov_b32 exec_hi, 0\n\tglobal_load_lds_dwordx4 %0, %1\n\ts_mov_b32 exec_hi, -1"
               ::"v"(voff), "s"(sbase), "s"(lds) : "memory");
}
__device__ __forceinline__ unsigned lds_off(const void* p) {
  return (unsigned)(uintptr_t)(__attribute__((address_space(3))) const char*)p;
}
#define SB0() __builtin_amdgcn_sched_barrier(0)
template <int N> __device__ __forceinline__ void wait_vm_lgkm0() {
  asm volatile("s_waitcnt vmcnt(%0) lgkmcnt(0)" ::"n"(N) : "memory");
}
__device__ __forceinline__ void bar() {
  asm volatile("" ::: "memory");
  __builtin_amdgcn_s_barrier();
  asm volatile("" ::: "memory");
}
__device__ __forceinline__ bf16x8 lds_frag(const char* p) { return *(const bf16x8*)p; }
__device__ __forceinline__ s16x4 lds_tr(const char* p) {
  return __builtin_amdgcn_ds_read_tr16_b64_v4i16((lds_s16x4_ptr)p);
}
__device__ __forceinline__ bf16x8 cat_frag(s16x4 a, s16x4 b) {
  s16x8 v = __builtin_shufflevector(a, b, 0, 1, 2, 3, 4, 5, 6, 7);
  return __builtin_bit_cast(bf16x8, v);
}
#define MFMA16(a, b, c) __builtin_amdgcn_mfma_f32_16x16x32_bf16((a), (b), (c), 0, 0, 0)

template <class Sc, int POS, int NM, int NR, int DL, class FM, class FR>
__device__ __forceinline__ void run_sched(FM& fm, FR& fr) {
  if constexpr (Sc::s[POS] != 0) {
    constexpr char c = Sc::s[POS];
    if constexpr (c == 'M') {
      fm(IC<NM>{}); SB0();
      run_sched<Sc, POS + 1, NM + 1, NR, DL>(fm, fr);
    } else {
      if constexpr (DL) { fr(IC<NR>{}); SB0(); }
      run_sched<Sc, POS + 1, NM, NR + 1, DL>(fm, fr);
    }
  }
}
template <int N> __device__ __forceinline__ void wait_vm() { asm volatile("s_waitcnt vmcnt(%0)" ::"n"(N) : "memory"); }
__device__ __forceinline__ void wait_lgkm0() { asm volatile("s_waitcnt lgkmcnt(0)" ::: "memory"); }

template <int NS, int P, int TI>
__device__ __forceinline__ void prod_tail() {
  if constexpr (TI < NS - 1) {
    wait_vm<P*(NS - 2 - TI)>();
    bar();
    prod_tail<NS, P, TI + 1>();
  }
}
constexpr int ramp_A(int j, int NS) {
  int a = 2;
  for (int i = 1; i <= j; ++i) { int lim = (i == 1) ? NS : (i - 1) + NS; a = (a + 2 < lim) ? a + 2 : lim; }
  return a;
}
constexpr int ramp_len(int NS) { int j = 1; while (ramp_A(j, NS) != ((j == 1) ? NS : (j - 1) + NS) || j < 2) ++j; return j; }
template <int NS, int P, int J, int R, class Issue>
__device__ __forceinline__ void prod_ramp(Issue& issue) {
  if constexpr (J < R) {
    constexpr int a0 = (J == 0) ? 0 : ramp_A(J - 1, NS), a1 = ramp_A(J, NS);
#pragma unroll
    for (int s = a0; s < a1; ++s) issue(s, s % NS);
    wait_vm<P*(a1 - (J + 1))>();
    bar();
    prod_ramp<NS, P, J + 1, R>(issue);
  }
}
template <int NS, int NK, int P, int SPL, class Issue, class M1, class M2>
__device__ __forceinline__ void producer(Issue issue, M1 mid1, M2 mid2) {
  constexpr int R = ramp_len(NS);
  static_assert(R - 1 + NS <= NK && SPL > R + NS && SPL < NK - NS, "bad config");
  prod_ramp<NS, P, 0, R>(issue);
#pragma unroll
  for (int s = ramp_A(R - 1, NS); s < R - 1 + NS; ++s) issue(s, s % NS);
  mid1();
  int wr = (R - 1) % NS;
#pragma nounroll
  for (int i = R - 1; i < SPL; ++i) {
    wait_vm<P*(NS - 2)>();
    bar();
    issue(i + NS, wr);
    wr = (wr + 1 == NS) ? 0 : wr + 1;
  }
  mid2();
#pragma nounroll
  for (int i = SPL; i < NK - NS; ++i) {
    wait_vm<P*(NS - 2)>();
    bar();
    issue(i + NS, wr);
    wr = (wr + 1 == NS) ? 0 : wr + 1;
  }
  prod_tail<NS, P, 0>();
}
__device__ __forceinline__ void gld16nt(u32x4& r, unsigned voff, const char* sb) {
  asm volatile("global_load_dwordx4 %0, %1, %2 nt" : "=v"(r) : "v"(voff), "s"(sb) : "memory");
}
#define PFD 2
template <int NK, int PR, int PC, class IR, class ICC, class Mid, class Last>
__device__ __forceinline__ void producer_seam(IR issueR, ICC issueC, Mid mid, Last last) {
  constexpr int P = PR + PC;
  issueR(0, 0); issueR(1, 1);
  wait_vm<PR>(); bar();
  issueR(2, 2); issueC(2, 2); issueR(3, 3); issueC(3, 3);
  wait_vm<2 * P>(); bar();
  mid();
  constexpr int J0 = 2;
#pragma nounroll
  for (int j = J0; j <= NK - 3; ++j) {
    issueR(j + 2, (j + 2) & 3); issueC(j + 2, (j + 2) & 3);
    wait_vm<2 * P>(); bar();
  }
  wait_vm<P>(); bar();
  wait_vm<0>();
  last();
  bar();
}
template <int NS, int NK, class Load0, class Body>
__device__ __forceinline__ void consumer(Load0 load0, Body body) {
  static_assert((NK & 1) == 0, "even number of stages");
  bar();
  load0();
  int rd = 1;
#pragma nounroll
  for (int i = 0; i < NK - 2; i += 2) {
    wait_lgkm0();
    bar();
    SB0();
    body(IC<0>{}, IC<1>{}, rd);
    SB0();
    rd = (rd + 1 == NS) ? 0 : rd + 1;
    wait_lgkm0();
    bar();
    SB0();
    body(IC<1>{}, IC<1>{}, rd);
    SB0();
    rd = (rd + 1 == NS) ? 0 : rd + 1;
  }
  wait_lgkm0();
  bar();
  SB0();
  body(IC<0>{}, IC<1>{}, rd);
  SB0();
  body(IC<1>{}, IC<0>{}, 0);
  SB0();
}
template <int NK, class Load0, class Half>
__device__ __forceinline__ void consumer_half(Load0 load0, Half half) {
  bar();
  load0();
#pragma nounroll
  for (int s = 0; s < NK - 1; ++s) {
    SB0();
    half(IC<0>{}, IC<1>{}, s & 3);
    SB0();
    wait_lgkm0();
    bar();
    SB0();
    half(IC<1>{}, IC<1>{}, (s + 1) & 3);
    SB0();
  }
  half(IC<0>{}, IC<1>{}, (NK - 1) & 3);
  SB0();
  half(IC<1>{}, IC<0>{}, 0);
  SB0();
}
typedef __attribute__((address_space(1))) unsigned gu32;
__device__ __forceinline__ void st16_wt(void* p, u32x4 v) {
  asm volatile("global_store_dwordx4 %0, %1, off sc1\n\ts_nop 1" ::"v"(p), "v"(v) : "memory");
}
__device__ __forceinline__ void st8_wt(void* p, f32x2 v) {
  asm volatile("global_store_dwordx2 %0, %1, off sc1\n\ts_nop 1" ::"v"(p), "v"(v) : "memory");
}
__device__ __forceinline__ void wait_count(char* ws, int idx, unsigned need) {
  gu32* cnt = (gu32*)(ws + WS_CNT + idx * 128);
#pragma nounroll
  for (unsigned spins = 0; spins < (1u << 22); ++spins) {
    if ((unsigned)__builtin_amdgcn_readfirstlane(__hip_atomic_load(cnt, __ATOMIC_RELAXED, __HIP_MEMORY_SCOPE_AGENT)) >= need) break;
    __builtin_amdgcn_s_sleep(1);
  }
  asm volatile("" ::: "memory");
}
__device__ __forceinline__ void add_count(char* ws, int idx) {
  __hip_atomic_fetch_add((gu32*)(ws + WS_CNT + idx * 128), 1u, __ATOMIC_RELAXED, __HIP_MEMORY_SCOPE_AGENT);
}
__device__ __forceinline__ int swap23(int r) { return (r & ~12) | ((r & 4) << 1) | ((r & 8) >> 1); }

#define WPF_KB 4
__global__ __launch_bounds__(256) void k1_ln_mod(const unsigned short* __restrict__ x,
                                                 const unsigned short* __restrict__ c,
                                                 const unsigned short* __restrict__ wmod,
                                                 char* __restrict__ ws) {
  __shared__ __attribute__((aligned(16))) char smem[16 * 1296 + 256];
  const int tid = threadIdx.x, lane = tid & 63;
  const int bid = blockIdx.x;
  if (bid < 128) {
    const int xcd = bid & 7, i = bid >> 3;
    const int rg = xcd * 4 + (i >> 2), sub = i & 3;
    const int row0 = rg * 64 + sub * 16;
    const int r = tid >> 4, j = tid & 15;
    const u32x4* src = (const u32x4*)(x + (size_t)(row0 + r) * D_);
    u32x4 v[5];
#pragma unroll
    for (int q = 0; q < 5; ++q) v[q] = LD1(src + j + 16 * q);
    float f[40];
#pragma unroll
    for (int q = 0; q < 5; ++q)
#pragma unroll
      for (int e = 0; e < 4; ++e) { f[q * 8 + 2 * e] = lo16(v[q][e]); f[q * 8 + 2 * e + 1] = hi16(v[q][e]); }
    float s = 0.f;
#pragma unroll
    for (int e = 0; e < 40; ++e) s += f[e];
#pragma unroll
    for (int o = 1; o < 16; o <<= 1) s += __shfl_xor(s, o, 64);
    const float mean = s * (1.0f / D_);
    float s2 = 0.f;
#pragma unroll
    for (int e = 0; e < 40; ++e) { float d = f[e] - mean; s2 += d * d; }
#pragma unroll
    for (int o = 1; o < 16; o <<= 1) s2 += __shfl_xor(s2, o, 64);
    const float mb = bfr(mean);
    const float vb = bfr(s2 * (1.0f / D_));
    const float rden = 1.0f / bfr(sqrtf(bfr(vb + 1e-5f)));
#pragma unroll
    for (int q = 0; q < 5; ++q) {
      u32x4 o;
#pragma unroll
      for (int e = 0; e < 4; ++e) {
        float a = bfr(f[q * 8 + 2 * e] - mb) * rden;
        float b = bfr(f[q * 8 + 2 * e + 1] - mb) * rden;
        o[e] = pk2(a, b);
      }
      *(u32x4*)(smem + r * 1296 + (j + 16 * q) * 16) = o;
    }
    wait_lgkm0();
    bar();
    const int grp = tid >> 4, li = tid & 15, qq = li >> 2, pp = li & 3;
    char* ht = ws + WS_HT + (size_t)(row0 >> 4) * 2560;
#pragma unroll
    for (int it = 0; it < 5; ++it) {
      const int pr = it * 16 + grp;
      const int cblk = pr >> 1, half = pr & 1;
      const char* a0 = smem + (8 * half + qq) * 1296 + (16 * cblk + 4 * pp) * 2;
      s16x4 t0 = lds_tr(a0);
      s16x4 t1 = lds_tr(a0 + 4 * 1296);
      s16x8 o = __builtin_shufflevector(t0, t1, 0, 1, 2, 3, 4, 5, 6, 7);
      const int d = 16 * cblk + li;
      const int cb = d / 80, rr = d - cb * 80;
      *(s16x8*)(ht + (size_t)cb * (128 * 2560) + rr * 32 + half * 16) = o;
    }
    if (bid == 0 && tid < 96) *(u32x4*)(ws + WS_E + 2560 + tid * 16) = (u32x4){0u, 0u, 0u, 0u};
    if (bid == 1 && tid < 64) *(unsigned*)(ws + WS_CNT + tid * 128) = 0u;
  } else {
    const int g = bid - 128;
    const int cgp = (g & 7) * 30 + (g >> 3);
    const int l2 = tid & 1, rg = tid >> 1;
    u32x4 v[5];
    unsigned short cv16[5];
#pragma unroll
    for (int q = 0; q < 5; ++q) {
      const int k = q * 128 + rg;
      v[q] = *(const u32x4*)(wmod + (size_t)k * MODN + cgp * 16 + l2 * 8);
      cv16[q] = c[k];
    }
    float acc[8];
#pragma unroll
    for (int e = 0; e < 8; ++e) acc[e] = 0.f;
#pragma unroll
    for (int q = 0; q < 5; ++q) {
      const float cv = bf2f(cv16[q]);
      const float sv = bfr(cv / (1.0f + __expf(-cv)));
#pragma unroll
      for (int e = 0; e < 4; ++e) {
        acc[2 * e] += sv * lo16(v[q][e]);
        acc[2 * e + 1] += sv * hi16(v[q][e]);
      }
    }
#pragma unroll
    for (int o = 2; o < 64; o <<= 1)
#pragma unroll
      for (int e = 0; e < 8; ++e) acc[e] += __shfl_xor(acc[e], o, 64);
    float* red = (float*)smem;
    if (lane < 2) {
#pragma unroll
      for (int e = 0; e < 8; ++e) red[(tid >> 6) * 16 + lane * 8 + e] = acc[e];
    }
    wait_lgkm0();
    bar();
    if (tid < 16) {
      float m = bfr((red[tid] + red[16 + tid]) + (red[32 + tid] + red[48 + tid]));
      const int n = cgp * 16 + tid;
      const int part = n / D_, d = n - part * D_;
      if (part == 1 || part == 4) m = bfr(1.0f + m);
      ((float*)(ws + WS_MOD))[n] = m;
      if (part == 4) ((unsigned short*)(ws + WS_E))[d] = (unsigned short)(__float_as_uint(m) >> 16);
      if (part == 3) ((unsigned short*)(ws + WS_E))[D_ + d] = (unsigned short)(__float_as_uint(m) >> 16);
    }
  }
}

__device__ __forceinline__ void dma_res_tile(const char* base  , unsigned lds, int w, int lane) {
#pragma unroll
  for (int j = 0; j < 3; ++j) {
    int L = 160 * w + 64 * j + lane;
    if (L > 639) L = 639;
    const int r = L / 10, ch = L - r * 10;
    if (j < 2) dma16(r * (D_ * 2) + ch * 16, base, lds + (160 * w + 64 * j) * 16);
    else dma16h(r * (D_ * 2) + ch * 16, base, lds + (160 * w + 128) * 16);
  }
}
template <int N>
__device__ __forceinline__ void l2_warm(const char* base, unsigned dummy_lds, int lane) {
#pragma unroll
  for (int i = 0; i < N; ++i) dma16(lane * 16, base + i * 1024, dummy_lds);
}

struct SchedP2 { static constexpr char s[] = "MRMRMMRMRMMRMRMMRMRMMRMMMMMMMMMMM"; };
struct SchedP3 { static constexpr char s[] = "RRMRMRRMRMRRMRMRRMRMMM"; };
struct SchedP4 { static constexpr char s[] = "MRMRMRMMRMRMRMMRMRMMRMRMRMMRMRMRMMRMRMMMMMMMMM"; };
constexpr int cnt_ch(const char* s, char c) { int n = 0; for (; *s; ++s) n += (*s == c); return n; }
static_assert(cnt_ch(SchedP2::s, 'M') == 24 && cnt_ch(SchedP2::s, 'R') == 9, "");
static_assert(cnt_ch(SchedP3::s, 'M') == 10 && cnt_ch(SchedP3::s, 'R') == 12, "");
static_assert(cnt_ch(SchedP4::s, 'M') == 30 && cnt_ch(SchedP4::s, 'R') == 16, "");
constexpr int L_RES = 147456, L_MOD = 159744, L_DUM = L_MOD + 2048, L_TOTAL = L_DUM + 1024;
#define P2NS 5
constexpr int P2_NS = P2NS, P2_STAGE = 26624, P2_X = 2 * P2_STAGE, P2_STG = P2_X + 49152;
constexpr int P3_STAGE = 36864;
constexpr int P4_STAGE = 28672, P4_E = 4 * P4_STAGE, P4_ST = P4_E + 4096, P4_RS = P4_ST + 8192, P4_STG = 2 * P4_STAGE;

__global__ __launch_bounds__(512, 2) void mega(const unsigned short* __restrict__ wre,
                                               const unsigned short* __restrict__ wim,
                                               const unsigned short* __restrict__ x,
                                               const unsigned short* __restrict__ dre,
                                               const unsigned short* __restrict__ dim,
                                               const unsigned short* __restrict__ w1,
                                               const unsigned short* __restrict__ w2,
                                               char* __restrict__ ws,
                                               unsigned short* __restrict__ out) {
  STAMP_DECL
  STAMP(0);
  extern __shared__ __attribute__((aligned(16))) char smem[];
  const int tid = threadIdx.x, lane = tid & 63;
  const int wv = __builtin_amdgcn_readfirstlane(tid >> 6);
  const int w = wv & 3;
  const bool prod = wv < 4;
  const int bid = blockIdx.x;
  const int xcd = bid & 7, bi = bid >> 3;
  const int cb = bi & 7, rsel = bi >> 3;
  const int g = lane >> 4, l15 = lane & 15;
  const unsigned lds0 = lds_off(smem);
  const float* mod = (const float*)(smem + L_MOD);
  const int t3 = (xcd * 4 + rsel) * 64;
  const int grp3 = xcd * 2 + (rsel >> 1);

  auto issue_wd = [&](int ks, int buf) {
    const char* cgp = (const char*)(ks < 5 ? dre : dim) + (size_t)cb * 80 * 1280 + (ks < 5 ? ks : ks - 5) * 256;
#pragma unroll
    for (int j = 0; j < 5; ++j) {
      const int rs = 40 * w + 8 * j + (lane >> 3);
      const int h = rs >= 80, row = rs - 80 * h;
      dma16(row * 1280 + h * 128 + 16 * ((lane & 7) ^ ((row >> 1) & 7)), cgp, lds0 + buf * P3_STAGE + 16384 + (40 * w + 8 * j) * 128);
    }
  };
  {
    const int isim = rsel >> 1;
    const int grp2 = xcd * 2 + (rsel & 1);
    const int t0 = grp2 * 128;
    if (prod) {
      const char* rbase = (const char*)(isim ? wim : wre) + (size_t)t0 * (S_ * 2);
      const char* cbase = ws + WS_HT + (size_t)cb * 32 * 10240;
      unsigned roff[4];
#pragma unroll
      for (int j = 0; j < 4; ++j) {
        const int row = 32 * w + 8 * j + (lane >> 3);
        roff[j] = row * (S_ * 2) + 16 * ((lane & 7) ^ ((row >> 1) & 7));
      }
      const unsigned coff = (20 * w) * 128 + lane * 16;
      const int gw = bid * 4 + w;
      const int i1 = gw < 960 ? gw * 2 : 1918;
      u32x4 rt[4];
      producer<P2_NS, 32, 7, 14>([&](int ks, int buf) {
        const unsigned lb = lds0 + buf * P2_STAGE;
#pragma unroll
        for (int j = 0; j < 4; ++j) dma16nt(roff[j], rbase + ks * 128, lb + (32 * w + 8 * j) * 128);
        dma16(coff, cbase + ks * 10240, lb + 16384 + (20 * w) * 128);
        dma16(coff, cbase + ks * 10240 + 1024, lb + 16384 + (20 * w + 8) * 128);
        dma16h(coff, cbase + ks * 10240 + 2048, lb + 16384 + (20 * w + 16) * 128);
      }, [&] {
#pragma unroll
        for (int q = 0; q < 2; ++q) {
          const int id = (i1 + q) * 64 + lane;
          {
            const int rr = id / 12, pc = id - rr * 12;
            const int c4 = rr / D_, r = rr - c4 * D_;
            gld16nt(rt[q], swap23(r) * (F_ * 2) + (c4 * 12 + (pc ^ (((r >> 2) & 1) << 1))) * 16, (const char*)w1);
          }
          {
            const int rr = id / 10, cc = id - rr * 10;
            const int c8 = rr / F_, r = rr - c8 * F_;
            gld16nt(rt[2 + q], swap23(r) * (D_ * 2) + (c8 * 10 + cc) * 16, (const char*)w2);
          }
        }
        const int id = gw < 800 ? gw : 799;
        dma16(lane * 16, (const char*)dre + (size_t)id * 1024, lds0 + L_DUM);
        dma16(lane * 16, (const char*)dim + (size_t)id * 1024, lds0 + L_DUM);
      }, [&] {
#pragma unroll
        for (int q = 0; q < 2; ++q) {
          st16_wt(ws + WS_W1T + (size_t)(i1 + q) * 1024 + lane * 16, rt[q]);
          st16_wt(ws + WS_W2T + (size_t)(i1 + q) * 1024 + lane * 16, rt[2 + q]);
        }
      });
      bar();
    } else {
      if (w < 2) {
        int L = 64 * w + lane; if (L > 99) L = 99;
        const int vec = L / 20, part = vec + (vec >= 3);
        dma16(part * 2560 + cb * 320 + (L - vec * 20) * 16, ws + WS_MOD, lds0 + L_MOD + w * 1024);
      }
      dma_res_tile((const char*)x + (size_t)t3 * (D_ * 2) + cb * 160, lds0 + L_RES, w, lane);
      const int wm = w & 1, wk = w >> 1;
      const int fsw = ((4 * wk + g) ^ (l15 >> 1)) * 16;
      const int ra = (64 * wm + l15) * 128 + fsw;
      const int ca_ = 16384 + (2 * wk + (g >> 1)) * 2560 + l15 * 32 + (g & 1) * 16;
      bf16x8 rf[2][4], cf[2][5];
      f32x4 acc[6][4];
#pragma unroll
      for (int j = 0; j < 6; ++j)
#pragma unroll
        for (int i = 0; i < 4; ++i) acc[j][i] = (f32x4){0.f, 0.f, 0.f, 0.f};
      const s16x8 ones_s = {0x3F80, 0x3F80, 0x3F80, 0x3F80, 0x3F80, 0x3F80, 0x3F80, 0x3F80};
      const bf16x8 ones = __builtin_bit_cast(bf16x8, ones_s);
      auto ldf = [&](auto par, auto ic, int buf) {
        constexpr int PP = decltype(par)::value, n = decltype(ic)::value;
        const char* lb = smem + buf * P2_STAGE;
        if constexpr (n == 0) rf[PP][0] = lds_frag(lb + ra);
        else if constexpr (n < 6) cf[PP][n - 1] = lds_frag(lb + ca_ + (n - 1) * 512);
        else rf[PP][n - 5] = lds_frag(lb + ra + (n - 5) * 2048);
      };
      auto body = [&](auto par, auto dl, int rd) {
        constexpr int PP = decltype(par)::value, DL = decltype(dl)::value;
        auto fm = [&](auto mc) {
          constexpr int m = decltype(mc)::value, i = m / 6, j = m % 6;
          if constexpr (j < 5) acc[j][i] = MFMA16(cf[PP][j], rf[PP][i], acc[j][i]);
          else acc[5][i] = MFMA16(ones, rf[PP][i], acc[5][i]);
        };
        auto fr = [&](auto rc) { ldf(IC<PP ^ 1>{}, rc, rd); };
        run_sched<SchedP2, 0, 0, 0, DL>(fm, fr);
      };
      auto load0 = [&] { static_for<9>([&](auto ic) { ldf(IC<0>{}, ic, 0); }); };
      consumer<P2_NS, 32>(load0, body);
      STAMP(1);
      auto finish = [&](auto keepc) {
        constexpr int KP = decltype(keepc)::value;
        char* dst = smem + P2_X + w * 12288 + lane * 16;
#pragma unroll
        for (int j = 0; j < 5; ++j) {
          *(f32x4*)(dst + (2 * j) * 1024) = acc[j][2 * (KP ^ 1)];
          *(f32x4*)(dst + (2 * j + 1) * 1024) = acc[j][2 * (KP ^ 1) + 1];
        }
        *(f32x2*)(dst + 10240) = (f32x2){acc[5][2 * (KP ^ 1)][0], acc[5][2 * (KP ^ 1) + 1][0]};
        wait_vm<0>();
        wait_lgkm0();
        bar();
        const char* srcp = smem + P2_X + (w ^ 2) * 12288 + lane * 16;
        const float sgn = isim ? -1.0f : 1.0f;
        const f32x2 rp = *(const f32x2*)(srcp + 10240);
        const float rs0 = (acc[5][2 * KP][0] + rp[0]) * sgn, rs1 = (acc[5][2 * KP + 1][0] + rp[1]) * sgn;
        char* stg = smem + P2_STG + (64 * wm + 32 * KP + l15) * 176 + g * 8;
#pragma unroll
        for (int j = 0; j < 5; ++j) {
          const f32x4 sa = *(const f32x4*)(mod + 16 * j + 4 * g);
          const f32x4 c1 = *(const f32x4*)(mod + 80 + 16 * j + 4 * g) * sgn;
          const f32x4 v0 = c1 * (acc[j][2 * KP] + *(const f32x4*)(srcp + (2 * j) * 1024)) + sa * rs0;
          const f32x4 v1 = c1 * (acc[j][2 * KP + 1] + *(const f32x4*)(srcp + (2 * j + 1) * 1024)) + sa * rs1;
          *(u32x2*)(stg + j * 32) = (u32x2){pk2(v0[0], v0[1]), pk2(v0[2], v0[3])};
          *(u32x2*)(stg + 16 * 176 + j * 32) = (u32x2){pk2(v1[0], v1[1]), pk2(v1[2], v1[3])};
        }
      };
      if (wk == 0) finish(IC<0>{}); else finish(IC<1>{});
    }
    wait_lgkm0();
    bar();
    {
      char* pq = ws + WS_PQ + ((size_t)(grp2 * 2) * 80 + isim * 40 + cb * 5) * 2048;
      for (int id = tid; id < 1280; id += 512) {
        const int rbh = id >= 640, r_ = id - 640 * rbh;
        const int gq = r_ >> 7, row = rbh * 64 + ((r_ & 127) >> 1), c = 2 * gq + (r_ & 1);
        st16_wt(pq + (size_t)rbh * (80 * 2048) + r_ * 16, *(const u32x4*)(smem + P2_STG + row * 176 + c * 16));
      }
    }
    wait_lgkm0();
    bar();
    STAMP(2);
    if (prod) {
#pragma unroll
      for (int s = 0; s < PFD; ++s) issue_wd(s, s);
      wait_vm<5 * PFD>();
    } else {
      wait_vm<0>();
    }
    bar();
    STAMP(3);
    if (wv == 4) {
      if (lane == 0) add_count(ws, grp2);
      wait_count(ws, grp3, 16);
    }
    if (tid == 320) add_count(ws, 48);
    bar();
  }

  {
    if (prod) {
      const char* rbase = ws + WS_PQ + (size_t)(t3 >> 6) * (80 * 2048) + w * 4096;
      STAMP(4);
      producer_seam<10, 4, 5>([&](int ks, int buf) {
#pragma unroll
        for (int j = 0; j < 4; ++j) dma16s(lane * 16, rbase + ks * 16384 + j * 1024, lds0 + buf * P3_STAGE + w * 4096 + j * 1024);
      }, issue_wd, [] {}, [&] { if (w == 0) wait_count(ws, 48, 256); });
    } else {
      const int sw0 = (g ^ (l15 >> 1)) * 16, sw1 = ((4 + g) ^ (l15 >> 1)) * 16;
      const int ra = (g >> 1) * 2048 + (16 * w + l15) * 32 + (g & 1) * 16, ca_ = 16384 + l15 * 128;
      bf16x8 rf[2][2], cf[2][2][5];
      f32x4 acc[5];
#pragma unroll
      for (int j = 0; j < 5; ++j) acc[j] = (f32x4){0.f, 0.f, 0.f, 0.f};
      auto ldf = [&](auto hc, auto ic, int buf) {
        constexpr int H = decltype(hc)::value, n = decltype(ic)::value, kk = n / 6, m = n % 6;
        const char* lb = smem + buf * P3_STAGE;
        if constexpr (m == 0) rf[H][kk] = lds_frag(lb + ra + H * 8192 + kk * 4096);
        else cf[H][kk][m - 1] = lds_frag(lb + (kk ? sw1 : sw0) + ca_ + H * 10240 + (m - 1) * 2048);
      };
      auto half = [&](auto par, auto dl, int buf) {
        constexpr int PP = decltype(par)::value, DL = decltype(dl)::value;
        auto fm = [&](auto mc) {
          constexpr int m = decltype(mc)::value, kk = m / 5, j = m % 5;
          acc[j] = MFMA16(cf[PP][kk][j], rf[PP][kk], acc[j]);
        };
        auto fr = [&](auto rc) { ldf(IC<PP ^ 1>{}, rc, buf); };
        run_sched<SchedP3, 0, 0, 0, DL>(fm, fr);
      };
      auto load0 = [&] { STAMP(4); static_for<12>([&](auto ic) { ldf(IC<0>{}, ic, 0); }); };
      consumer_half<10>(load0, half);
      STAMP(5);
      char* res = smem + L_RES + (16 * w + l15) * 160 + g * 8;
#pragma unroll
      for (int j = 0; j < 5; ++j) {
        const u32x2 xr = *(const u32x2*)(res + j * 32);
        const f32x4 ga = *(const f32x4*)(mod + 160 + 16 * j + 4 * g);
        const f32x4 y = acc[j];
        *(u32x2*)(res + j * 32) = (u32x2){pk2(lo16(xr[0]) + ga[0] * y[0], hi16(xr[0]) + ga[1] * y[1]),
                                          pk2(lo16(xr[1]) + ga[2] * y[2], hi16(xr[1]) + ga[3] * y[3])};
      }
    }
    wait_lgkm0();
    bar();
    {
      const int row = tid >> 3, c0 = tid & 7;
      char* xc = ws + WS_XC + ((size_t)(t3 >> 6) * 40 + cb * 5) * 2048 + row * 32;
      float xf[2][8];
      float s = 0.f;
#pragma unroll
      for (int it = 0; it < 2; ++it) {
        const int c = c0 + 8 * it;
        if (c < 10) {
          const u32x4 xr = *(const u32x4*)(smem + L_RES + row * 160 + c * 16);
          const f32x4 c10 = *(const f32x4*)(mod + 240 + c * 8), c11 = *(const f32x4*)(mod + 244 + c * 8);
#pragma unroll
          for (int e = 0; e < 4; ++e) { xf[it][2 * e] = lo16(xr[e]); xf[it][2 * e + 1] = hi16(xr[e]); s += xf[it][2 * e] + xf[it][2 * e + 1]; }
          const u32x4 oc = {pk2(xf[it][0] * c10[0], xf[it][1] * c10[1]), pk2(xf[it][2] * c10[2], xf[it][3] * c10[3]),
                            pk2(xf[it][4] * c11[0], xf[it][5] * c11[1]), pk2(xf[it][6] * c11[2], xf[it][7] * c11[3])};
          st16_wt(xc + (c >> 1) * 2048 + (c & 1) * 16, oc);
        }
      }
#pragma unroll
      for (int o = 1; o < 8; o <<= 1) s += __shfl_xor(s, o, 64);
      const float ml = s * (1.0f / 80.0f);
      float m2 = 0.f;
#pragma unroll
      for (int e = 0; e < 8; ++e) { float d = xf[0][e] - ml; m2 += d * d; }
      if (c0 < 2) {
#pragma unroll
        for (int e = 0; e < 8; ++e) { float d = xf[1][e] - ml; m2 += d * d; }
      }
#pragma unroll
      for (int o = 1; o < 8; o <<= 1) m2 += __shfl_xor(m2, o, 64);
      if (c0 == 0) st8_wt(ws + WS_ST + ((size_t)(t3 >> 6) * 8 + cb) * 512 + row * 8, (f32x2){s, m2});
    }
    STAMP(6);
    const char* w1t = ws + WS_W1T + (size_t)(bi & 15) * 122880;
    if (prod) {
      dma16(w * 1024 + lane * 16, ws + WS_E, lds0 + P4_E + w * 1024);
#pragma unroll
      for (int s = 0; s < PFD; ++s)
#pragma unroll
        for (int j = 0; j < 3; ++j)
          dma16s(lane * 16, w1t + s * 12288 + w * 3072 + j * 1024, lds0 + s * P4_STAGE + 16384 + w * 3072 + j * 1024);
      wait_vm<1 + 3 * PFD>();
    } else {
      wait_vm<0>();
    }
    bar();
    STAMP(7);
    if (wv == 4) {
      if (lane == 0) add_count(ws, 16 + grp3);
      wait_count(ws, 16 + xcd * 2 + (bi >> 4), 16);
    }
    bar();
  }

  {
    const int cb4 = bi & 15, grp4 = xcd * 2 + (bi >> 4);
    const int t0 = grp4 * 128;
    const char* w1t = ws + WS_W1T + (size_t)cb4 * 122880;
    if (prod) {
      const char* rbase = ws + WS_XC + (size_t)(grp4 * 2 + (w >> 1)) * (40 * 2048) + (w & 1) * 4096;
      STAMP(8);
      dma16s(w * 2048 + lane * 16, ws + WS_ST + (size_t)t0 * 64, lds0 + P4_ST + w * 2048);
      dma16s(w * 2048 + 1024 + lane * 16, ws + WS_ST + (size_t)t0 * 64, lds0 + P4_ST + w * 2048 + 1024);
      producer_seam<10, 4, 3>([&](int ks, int buf) {
#pragma unroll
        for (int j = 0; j < 4; ++j) dma16s(lane * 16, rbase + ks * 8192 + j * 1024, lds0 + buf * P4_STAGE + w * 4096 + j * 1024);
      }, [&](int ks, int buf) {
#pragma unroll
        for (int j = 0; j < 3; ++j)
          dma16s(lane * 16, w1t + ks * 12288 + w * 3072 + j * 1024, lds0 + buf * P4_STAGE + 16384 + w * 3072 + j * 1024);
      }, [&] {
        if (tid < 128) {
          const char* sp = smem + P4_ST + (tid >> 6) * 4096 + (tid & 63) * 8;
          f32x2 pp[8];
#pragma unroll
          for (int e = 0; e < 8; ++e) pp[e] = *(const f32x2*)(sp + e * 512);
          const float mean = (((pp[0][0] + pp[1][0]) + (pp[2][0] + pp[3][0])) + ((pp[4][0] + pp[5][0]) + (pp[6][0] + pp[7][0]))) * (1.0f / D_);
          float m2 = ((pp[0][1] + pp[1][1]) + (pp[2][1] + pp[3][1])) + ((pp[4][1] + pp[5][1]) + (pp[6][1] + pp[7][1]));
          float dd = 0.f;
#pragma unroll
          for (int e = 0; e < 8; ++e) { const float d = pp[e][0] * (1.0f / 80.0f) - mean; dd += d * d; }
          m2 += 80.0f * dd;
          const float rden = 1.0f / bfr(sqrtf(bfr(bfr(m2 * (1.0f / D_)) + 1e-5f)));
          *(f32x2*)(smem + P4_RS + tid * 8) = (f32x2){bfr(mean), rden};
        }
        wait_lgkm0();
      }, [] {});
    } else {
      const int q = l15 >> 2, p4 = lane & 3;
      const int wm = w & 1, wn = w >> 1;
      const int rpos = 16 * (g >> 1) + 4 * (g & 1) + q;
      const int xs = (rpos >> 2) & 1;
      int cadr[3];
#pragma unroll
      for (int j = 0; j < 3; ++j) {
        const int jj = 3 * wn + j;
        cadr[j] = 16384 + rpos * 192 + (2 * (jj ^ xs) + (p4 >> 1)) * 16 + (p4 & 1) * 8;
      }
      const int ra = wm * 8192 + (g >> 1) * 2048 + l15 * 32 + (g & 1) * 16;
      int ea = P4_E + (l15 < 2 ? l15 : 2) * 1280 + g * 16;
      bf16x8 rf[2][2][5], cf[2][2][3];
      f32x4 acc[3][5];
#pragma unroll
      for (int j = 0; j < 3; ++j)
#pragma unroll
        for (int i = 0; i < 5; ++i) acc[j][i] = (f32x4){0.f, 0.f, 0.f, 0.f};
      auto ldf = [&](auto par, auto ic, int buf) {
        constexpr int PP = decltype(par)::value, n = decltype(ic)::value, kk = n / 8, m = n % 8;
        const char* lb = smem + buf * P4_STAGE;
        if constexpr (m == 0) rf[PP][kk][0] = lds_frag(lb + ra + kk * 4096);
        else if constexpr (m < 4) cf[PP][kk][m - 1] = cat_frag(lds_tr(lb + cadr[m - 1] + kk * 6144), lds_tr(lb + cadr[m - 1] + kk * 6144 + 1536));
        else if constexpr (m < 7) rf[PP][kk][m - 3] = lds_frag(lb + ra + kk * 4096 + (m - 3) * 512);
        else {
          rf[PP][kk][4] = lds_frag(smem + ea + kk * 64);
          if constexpr (kk == 1) ea += 128;
        }
      };
      auto body = [&](auto par, auto dl, int rd) {
        constexpr int PP = decltype(par)::value, DL = decltype(dl)::value;
        auto fm = [&](auto mc) {
          constexpr int m = decltype(mc)::value, kk = m / 15, i = (m % 15) / 3, j = m % 3;
          acc[j][i] = MFMA16(cf[PP][kk][j], rf[PP][kk][i], acc[j][i]);
        };
        auto fr = [&](auto rc) { ldf(IC<PP ^ 1>{}, rc, rd); };
        run_sched<SchedP4, 0, 0, 0, DL>(fm, fr);
      };
      auto load0 = [&] { STAMP(8); static_for<16>([&](auto ic) { ldf(IC<0>{}, ic, 0); }); };
      consumer<4, 10>(load0, body);
      STAMP(9);
      f32x4 u[3], v[3];
#pragma unroll
      for (int j = 0; j < 3; ++j)
#pragma unroll
        for (int e = 0; e < 4; ++e) {
          const int a = __float_as_int(acc[j][4][e]);
          u[j][e] = __int_as_float(__builtin_amdgcn_update_dpp(0, a, 0x150, 0xf, 0xf, false));
          v[j][e] = __int_as_float(__builtin_amdgcn_update_dpp(0, a, 0x151, 0xf, 0xf, false));
        }
      char* stg = smem + P4_STG + (64 * wm + l15) * 208 + (48 * wn + 4 * g) * 2;
#pragma unroll
      for (int i = 0; i < 4; ++i) {
        const f32x2 rs = *(const f32x2*)(smem + P4_RS + (64 * wm + 16 * i + l15) * 8);
#pragma unroll
        for (int j = 0; j < 3; ++j) {
          const f32x4 hv = (acc[j][i] - u[j] * rs[0]) * rs[1] + v[j];
          *(u32x2*)(stg + i * 16 * 208 + j * 32) =
              (u32x2){pk2(fmaxf(hv[0], 0.f), fmaxf(hv[1], 0.f)), pk2(fmaxf(hv[2], 0.f), fmaxf(hv[3], 0.f))};
        }
      }
    }
    wait_lgkm0();
    bar();
    {
      char* h1 = ws + WS_H1 + ((size_t)(grp4 * 2) * 96 + cb4 * 6) * 2048;
#pragma unroll
      for (int it = 0; it < 3; ++it) {
        const int id = tid + 512 * it;
        const int rbh = id >= 768, r_ = id - 768 * rbh;
        const int gq = r_ >> 7, row = rbh * 64 + ((r_ & 127) >> 1), c = 2 * gq + (r_ & 1);
        st16_wt(h1 + (size_t)rbh * (96 * 2048) + r_ * 16, *(const u32x4*)(smem + P4_STG + row * 208 + c * 16));
      }
    }
    wait_lgkm0();
    bar();
    STAMP(10);
    const char* w2t = ws + WS_W2T + (size_t)cb * F_ * 160;
    if (prod) {
#pragma unroll
      for (int s = 0; s < PFD; ++s)
#pragma unroll
        for (int j = 0; j < 5; ++j)
          dma16s(lane * 16, w2t + s * 20480 + w * 5120 + j * 1024, lds0 + s * P3_STAGE + 16384 + w * 5120 + j * 1024);
      wait_vm<5 * PFD>();
    } else {
      wait_vm<0>();
    }
    bar();
    STAMP(11);
    if (wv == 4) {
      if (lane == 0) add_count(ws, 32 + grp4);
      wait_count(ws, 32 + grp3, 16);
    }
    bar();
  }

  {
    const char* w2t = ws + WS_W2T + (size_t)cb * F_ * 160;
    if (prod) {
      const char* rbase = ws + WS_H1 + (size_t)(t3 >> 6) * (96 * 2048) + w * 4096;
      STAMP(12);
      producer_seam<12, 4, 5>([&](int ks, int buf) {
#pragma unroll
        for (int j = 0; j < 4; ++j) dma16s(lane * 16, rbase + ks * 16384 + j * 1024, lds0 + buf * P3_STAGE + w * 4096 + j * 1024);
      }, [&](int ks, int buf) {
#pragma unroll
        for (int j = 0; j < 5; ++j)
          dma16s(lane * 16, w2t + ks * 20480 + w * 5120 + j * 1024, lds0 + buf * P3_STAGE + 16384 + w * 5120 + j * 1024);
      }, [] {}, [] {});
    } else {
      const int q = l15 >> 2, p4 = lane & 3;
      const int ra = (g >> 1) * 2048 + (16 * w + l15) * 32 + (g & 1) * 16;
      const int ca_ = 16384 + (16 * (g >> 1) + 4 * (g & 1) + q) * 160 + p4 * 8;
      bf16x8 rf[2][2], cf[2][2][5];
      f32x4 acc[5];
#pragma unroll
      for (int j = 0; j < 5; ++j) acc[j] = (f32x4){0.f, 0.f, 0.f, 0.f};
      auto ldf = [&](auto hc, auto ic, int buf) {
        constexpr int H = decltype(hc)::value, n = decltype(ic)::value, kk = n / 6, m = n % 6;
        const char* lb = smem + buf * P3_STAGE;
        if constexpr (m == 0) rf[H][kk] = lds_frag(lb + ra + H * 8192 + kk * 4096);
        else cf[H][kk][m - 1] = cat_frag(lds_tr(lb + ca_ + H * 10240 + kk * 5120 + (m - 1) * 32),
                                         lds_tr(lb + ca_ + H * 10240 + kk * 5120 + (m - 1) * 32 + 1280));
      };
      auto half = [&](auto par, auto dl, int buf) {
        constexpr int PP = decltype(par)::value, DL = decltype(dl)::value;
        auto fm = [&](auto mc) {
          constexpr int m = decltype(mc)::value, kk = m / 5, j = m % 5;
          acc[j] = MFMA16(cf[PP][kk][j], rf[PP][kk], acc[j]);
        };
        auto fr = [&](auto rc) { ldf(IC<PP ^ 1>{}, rc, buf); };
        run_sched<SchedP3, 0, 0, 0, DL>(fm, fr);
      };
      auto load0 = [&] { STAMP(12); static_for<12>([&](auto ic) { ldf(IC<0>{}, ic, 0); }); };
      consumer_half<12>(load0, half);
      STAMP(13);
      char* res = smem + L_RES + (16 * w + l15) * 160 + g * 8;
#pragma unroll
      for (int j = 0; j < 5; ++j) {
        const u32x2 xr = *(const u32x2*)(res + j * 32);
        const f32x4 gb = *(const f32x4*)(mod + 320 + 16 * j + 4 * g);
        const f32x4 y = acc[j];
        *(u32x2*)(res + j * 32) = (u32x2){pk2(lo16(xr[0]) + gb[0] * y[0], hi16(xr[0]) + gb[1] * y[1]),
                                          pk2(lo16(xr[1]) + gb[2] * y[2], hi16(xr[1]) + gb[3] * y[3])};
      }
      wait_vm<0>();
    }
    wait_lgkm0();
    bar();
    for (int id = tid; id < 640; id += 512) {
      const int row = id / 10, c = id - row * 10;
      *(u32x4*)((char*)out + (size_t)(t3 + row) * (D_ * 2) + cb * 160 + c * 16) = *(const u32x4*)(smem + L_RES + row * 160 + c * 16);
    }
  }
  STAMP(14);
  STAMP_OUT();
}

extern "C" void kernel_launch(void* const* d_in, const int* in_sizes, int n_in,
                              void* d_out, int out_size, void* d_ws, size_t ws_size,
                              hipStream_t stream) {
  const unsigned short* x    = (const unsigned short*)d_in[0];
  const unsigned short* wsre = (const unsigned short*)d_in[1];
  const unsigned short* wsim = (const unsigned short*)d_in[2];
  const unsigned short* wdre = (const unsigned short*)d_in[3];
  const unsigned short* wdim = (const unsigned short*)d_in[4];
  const unsigned short* w1   = (const unsigned short*)d_in[6];
  const unsigned short* w2   = (const unsigned short*)d_in[7];
  const unsigned short* c    = (const unsigned short*)d_in[8];
  const unsigned short* wmod = (const unsigned short*)d_in[9];
  char* ws = (char*)d_ws;
  unsigned short* out = (unsigned short*)d_out;
  (void)hipFuncSetAttribute((const void*)mega, hipFuncAttributeMaxDynamicSharedMemorySize, L_TOTAL);
  k1_ln_mod<<<368, 256, 0, stream>>>(x, c, wmod, ws);
  mega<<<256, 512, L_TOTAL, stream>>>(wsre, wsim, x, wdre, wdim, w1, w2, ws, out);
}
```

```cpp
#include <hip/hip_runtime.h>
#include <hip/hip_bf16.h>
#include <stdint.h>

typedef __attribute__((ext_vector_type(8))) __bf16 bf16x8;
typedef __attribute__((ext_vector_type(4))) __bf16 bf16x4;
typedef __attribute__((ext_vector_type(2))) __bf16 bf16x2;
typedef __attribute__((ext_vector_type(4))) float f32x4;
typedef __attribute__((ext_vector_type(2))) float f32x2;
typedef __attribute__((ext_vector_type(4))) short s16x4;
typedef __attribute__((ext_vector_type(8))) short s16x8;
typedef __attribute__((ext_vector_type(4))) unsigned u32x4;
typedef __attribute__((ext_vector_type(2))) unsigned u32x2;
typedef __attribute__((address_space(3))) s16x4* lds_s16x4_ptr;
typedef __attribute__((address_space(3))) void* lds_vptr;

constexpr int S_ = 2048, D_ = 640, F_ = 1536, MODN = 3840;

constexpr size_t WS_MOD = 0;
constexpr size_t WS_E   = 16384;
constexpr size_t WS_CNT = 20480;
constexpr size_t WS_ST  = 32768;
constexpr size_t WS_HT  = WS_ST + (size_t)S_ * 64;
constexpr size_t WS_PQ  = WS_HT + (size_t)D_ * S_ * 2;
constexpr size_t WS_XC  = WS_PQ + (size_t)S_ * 1280 * 2;
constexpr size_t WS_H1  = WS_XC + (size_t)S_ * D_ * 2;
constexpr size_t WS_W2T = WS_H1 + (size_t)S_ * F_ * 2;
constexpr size_t WS_W1T = WS_W2T + (size_t)F_ * D_ * 2;
constexpr size_t WS_WDT = WS_W1T + (size_t)F_ * D_ * 2;

#define STAMP_DECL
#define STAMP(i)
#define STAMP_OUT()
template <int V> struct IC { static constexpr int value = V; };
template <int N, int I = 0, class F> __device__ __forceinline__ void static_for(F f) {
  if constexpr (I < N) { f(IC<I>{}); static_for<N, I + 1>(f); }
}

__device__ __forceinline__ float bf2f(unsigned short u) { return __uint_as_float(((unsigned)u) << 16); }
__device__ __forceinline__ float bfr(float f) { return (float)(__bf16)f; }
__device__ __forceinline__ unsigned pk2(float a, float b) {
  bf16x2 v = __builtin_convertvector((f32x2){a, b}, bf16x2);
  return __builtin_bit_cast(unsigned, v);
}
__device__ __forceinline__ float lo16(unsigned u) { return __uint_as_float(u << 16); }
__device__ __forceinline__ float hi16(unsigned u) { return __uint_as_float(u & 0xffff0000u); }

__device__ __forceinline__ void dma16(unsigned voff, const char* sbase, unsigned lds) {
  asm volatile("s_mov_b32 m0, %2\n\ts_nop 0\n\tglobal_load_lds_dwordx4 %0, %1"
               ::"v"(voff), "s"(sbase), "s"(lds) : "memory");
}
__device__ __forceinline__ void dma16nt(unsigned voff, const char* sbase, unsigned lds) {
  asm volatile("s_mov_b32 m0, %2\n\ts_nop 0\n\tglobal_load_lds_dwordx4 %0, %1 nt"
               ::"v"(voff), "s"(sbase), "s"(lds) : "memory");
}
__device__ __forceinline__ void dma16s(unsigned voff, const char* sbase, unsigned lds) {
  asm volatile("s_mov_b32 m0, %2\n\ts_nop 0\n\tglobal_load_lds_dwordx4 %0, %1 sc1"
               ::"v"(voff), "s"(sbase), "s"(lds) : "memory");
}
#define LD1(p) __builtin_nontemporal_load(p)
__device__ __forceinline__ void dma16h(unsigned voff, const char* sbase, unsigned lds) {
  asm volatile("s_mov_b32 m0, %2\n\ts_mov_b32 exec_hi, 0\n\tglobal_load_lds_dwordx4 %0, %1\n\ts_mov_b32 exec_hi, -1"
               ::"v"(voff), "s"(sbase), "s"(lds) : "memory");
}
__device__ __forceinline__ unsigned lds_off(const void* p) {
  return (unsigned)(uintptr_t)(__attribute__((address_space(3))) const char*)p;
}
#define SB0() __builtin_amdgcn_sched_barrier(0)
template <int N> __device__ __forceinline__ void wait_vm_lgkm0() {
  asm volatile("s_waitcnt vmcnt(%0) lgkmcnt(0)" ::"n"(N) : "memory");
}
__device__ __forceinline__ void bar() {
  asm volatile("" ::: "memory");
  __builtin_amdgcn_s_barrier();
  asm volatile("" ::: "memory");
}
__device__ __forceinline__ bf16x8 lds_frag(const char* p) { return *(const bf16x8*)p; }
__device__ __forceinline__ s16x4 lds_tr(const char* p) {
  return __builtin_amdgcn_ds_read_tr16_b64_v4i16((lds_s16x4_ptr)p);
}
__device__ __forceinline__ bf16x8 cat_frag(s16x4 a, s16x4 b) {
  s16x8 v = __builtin_shufflevector(a, b, 0, 1, 2, 3, 4, 5, 6, 7);
  return __builtin_bit_cast(bf16x8, v);
}
#define MFMA16(a, b, c) __builtin_amdgcn_mfma_f32_16x16x32_bf16((a), (b), (c), 0, 0, 0)

template <class Sc, int POS, int NM, int NR, int DL, class FM, class FR>
__device__ __forceinline__ void run_sched(FM& fm, FR& fr) {
  if constexpr (Sc::s[POS] != 0) {
    constexpr char c = Sc::s[POS];
    if constexpr (c == 'M') {
      fm(IC<NM>{}); SB0();
      run_sched<Sc, POS + 1, NM + 1, NR, DL>(fm, fr);
    } else {
      if constexpr (DL) { fr(IC<NR>{}); SB0(); }
      run_sched<Sc, POS + 1, NM, NR + 1, DL>(fm, fr);
    }
  }
}
template <int N> __device__ __forceinline__ void wait_vm() { asm volatile("s_waitcnt vmcnt(%0)" ::"n"(N) : "memory"); }
__device__ __forceinline__ void wait_lgkm0() { asm volatile("s_waitcnt lgkmcnt(0)" ::: "memory"); }

template <int NS, int P, int TI>
__device__ __forceinline__ void prod_tail() {
  if constexpr (TI < NS - 1) {
    wait_vm<P*(NS - 2 - TI)>();
    bar();
    prod_tail<NS, P, TI + 1>();
  }
}
constexpr int ramp_A(int j, int NS) {
  int a = 2;
  for (int i = 1; i <= j; ++i) { int lim = (i == 1) ? NS : (i - 1) + NS; a = (a + 2 < lim) ? a + 2 : lim; }
  return a;
}
constexpr int ramp_len(int NS) { int j = 1; while (ramp_A(j, NS) != ((j == 1) ? NS : (j - 1) + NS) || j < 2) ++j; return j; }
template <int NS, int P, int J, int R, class Issue>
__device__ __forceinline__ void prod_ramp(Issue& issue) {
  if constexpr (J < R) {
    constexpr int a0 = (J == 0) ? 0 : ramp_A(J - 1, NS), a1 = ramp_A(J, NS);
#pragma unroll
    for (int s = a0; s < a1; ++s) issue(s, s % NS);
    wait_vm<P*(a1 - (J + 1))>();
    bar();
    prod_ramp<NS, P, J + 1, R>(issue);
  }
}
template <int NS, int NK, int P, class Issue>
__device__ __forceinline__ void producer(Issue issue) {
  constexpr int R = ramp_len(NS);
  static_assert(R - 1 + NS <= NK, "ramp too long");
  prod_ramp<NS, P, 0, R>(issue);
#pragma unroll
  for (int s = ramp_A(R - 1, NS); s < R - 1 + NS; ++s) issue(s, s % NS);
  int wr = (R - 1) % NS;
#pragma nounroll
  for (int i = R - 1; i < NK - NS; ++i) {
    wait_vm<P*(NS - 2)>();
    bar();
    issue(i + NS, wr);
    wr = (wr + 1 == NS) ? 0 : wr + 1;
  }
  prod_tail<NS, P, 0>();
}
#define PFD 2
template <int NK, int PR, int PC, class IR, class ICC, class Mid>
__device__ __forceinline__ void producer_seam(IR issueR, ICC issueC, Mid mid) {
  constexpr int P = PR + PC;
  issueR(0, 0); issueR(1, 1);
  wait_vm<PR>(); bar();
  issueR(2, 2); issueC(2, 2); issueR(3, 3); issueC(3, 3);
  wait_vm<2 * P>(); bar();
  mid();
  constexpr int J0 = 2;
#pragma nounroll
  for (int j = J0; j <= NK - 3; ++j) {
    issueR(j + 2, (j + 2) & 3); issueC(j + 2, (j + 2) & 3);
    wait_vm<2 * P>(); bar();
  }
  wait_vm<P>(); bar();
  wait_vm<0>(); bar();
}
template <int NS, int NK, class Load0, class Body>
__device__ __forceinline__ void consumer(Load0 load0, Body body) {
  static_assert((NK & 1) == 0, "even number of stages");
  bar();
  load0();
  int rd = 1;
#pragma nounroll
  for (int i = 0; i < NK - 2; i += 2) {
    wait_lgkm0();
    bar();
    SB0();
    body(IC<0>{}, IC<1>{}, rd);
    SB0();
    rd = (rd + 1 == NS) ? 0 : rd + 1;
    wait_lgkm0();
    bar();
    SB0();
    body(IC<1>{}, IC<1>{}, rd);
    SB0();
    rd = (rd + 1 == NS) ? 0 : rd + 1;
  }
  wait_lgkm0();
  bar();
  SB0();
  body(IC<0>{}, IC<1>{}, rd);
  SB0();
  body(IC<1>{}, IC<0>{}, 0);
  SB0();
}
template <int NK, class Load0, class Half>
__device__ __forceinline__ void consumer_half(Load0 load0, Half half) {
  bar();
  load0();
#pragma nounroll
  for (int s = 0; s < NK - 1; ++s) {
    SB0();
    half(IC<0>{}, IC<1>{}, s & 3);
    SB0();
    wait_lgkm0();
    bar();
    SB0();
    half(IC<1>{}, IC<1>{}, (s + 1) & 3);
    SB0();
  }
  half(IC<0>{}, IC<1>{}, (NK - 1) & 3);
  SB0();
  half(IC<1>{}, IC<0>{}, 0);
  SB0();
}
typedef __attribute__((address_space(1))) unsigned gu32;
__device__ __forceinline__ void st16_wt(void* p, u32x4 v) {
  asm volatile("global_store_dwordx4 %0, %1, off sc1\n\ts_nop 1" ::"v"(p), "v"(v) : "memory");
}
__device__ __forceinline__ void st8_wt(void* p, f32x2 v) {
  asm volatile("global_store_dwordx2 %0, %1, off sc1\n\ts_nop 1" ::"v"(p), "v"(v) : "memory");
}
__device__ __forceinline__ void wait_count(char* ws, int idx, unsigned need) {
  gu32* cnt = (gu32*)(ws + WS_CNT + idx * 256);
#pragma nounroll
  for (unsigned spins = 0; spins < (1u << 22); ++spins) {
    if ((unsigned)__builtin_amdgcn_readfirstlane(__hip_atomic_load(cnt, __ATOMIC_RELAXED, __HIP_MEMORY_SCOPE_AGENT)) >= need) break;
    __builtin_amdgcn_s_sleep(1);
  }
  asm volatile("" ::: "memory");
}
__device__ __forceinline__ void add_count(char* ws, int idx) {
  __hip_atomic_fetch_add((gu32*)(ws + WS_CNT + idx * 256), 1u, __ATOMIC_RELAXED, __HIP_MEMORY_SCOPE_AGENT);
}
__device__ __forceinline__ int swap23(int r) { return (r & ~12) | ((r & 4) << 1) | ((r & 8) >> 1); }

#define WPF_KB 4
__global__ __launch_bounds__(256) void k1_ln_mod(const unsigned short* __restrict__ x,
                                                 const unsigned short* __restrict__ c,
                                                 const unsigned short* __restrict__ wmod,
                                                 const unsigned short* __restrict__ wre,
                                                 const unsigned short* __restrict__ wim,
                                                 const unsigned short* __restrict__ w2,
                                                 const unsigned short* __restrict__ dre,
                                                 const unsigned short* __restrict__ dim,
                                                 const unsigned short* __restrict__ w1,
                                                 char* __restrict__ ws) {
  __shared__ __attribute__((aligned(16))) char smem[16 * 1296 + 256];
  const int tid = threadIdx.x, lane = tid & 63;
  const int bid = blockIdx.x;
  if (bid < 128) {
    const int xcd = bid & 7, i = bid >> 3;
    const int rg = xcd * 4 + (i >> 2), sub = i & 3;
    const int row0 = rg * 64 + sub * 16;
    const int r = tid >> 4, j = tid & 15;
    const u32x4* src = (const u32x4*)(x + (size_t)(row0 + r) * D_);
    u32x4 v[5];
#pragma unroll
    for (int q = 0; q < 5; ++q) v[q] = LD1(src + j + 16 * q);
    float f[40];
#pragma unroll
    for (int q = 0; q < 5; ++q)
#pragma unroll
      for (int e = 0; e < 4; ++e) { f[q * 8 + 2 * e] = lo16(v[q][e]); f[q * 8 + 2 * e + 1] = hi16(v[q][e]); }
    float s = 0.f;
#pragma unroll
    for (int e = 0; e < 40; ++e) s += f[e];
#pragma unroll
    for (int o = 1; o < 16; o <<= 1) s += __shfl_xor(s, o, 64);
    const float mean = s * (1.0f / D_);
    float s2 = 0.f;
#pragma unroll
    for (int e = 0; e < 40; ++e) { float d = f[e] - mean; s2 += d * d; }
#pragma unroll
    for (int o = 1; o < 16; o <<= 1) s2 += __shfl_xor(s2, o, 64);
    const float mb = bfr(mean);
    const float vb = bfr(s2 * (1.0f / D_));
    const float rden = 1.0f / bfr(sqrtf(bfr(vb + 1e-5f)));
#pragma unroll
    for (int q = 0; q < 5; ++q) {
      u32x4 o;
#pragma unroll
      for (int e = 0; e < 4; ++e) {
        float a = bfr(f[q * 8 + 2 * e] - mb) * rden;
        float b = bfr(f[q * 8 + 2 * e + 1] - mb) * rden;
        o[e] = pk2(a, b);
      }
      *(u32x4*)(smem + r * 1296 + (j + 16 * q) * 16) = o;
    }
    wait_lgkm0();
    bar();
    const int grp = tid >> 4, li = tid & 15, qq = li >> 2, pp = li & 3;
    char* ht = ws + WS_HT + (size_t)(row0 >> 4) * 2560;
#pragma unroll
    for (int it = 0; it < 5; ++it) {
      const int pr = it * 16 + grp;
      const int cblk = pr >> 1, half = pr & 1;
      const char* a0 = smem + (8 * half + qq) * 1296 + (16 * cblk + 4 * pp) * 2;
      s16x4 t0 = lds_tr(a0);
      s16x4 t1 = lds_tr(a0 + 4 * 1296);
      s16x8 o = __builtin_shufflevector(t0, t1, 0, 1, 2, 3, 4, 5, 6, 7);
      const int d = 16 * cblk + li;
      const int cb = d / 80, rr = d - cb * 80;
      *(s16x8*)(ht + (size_t)cb * (128 * 2560) + rr * 32 + half * 16) = o;
    }
    if (bid == 0 && tid < 96) *(u32x4*)(ws + WS_E + 2560 + tid * 16) = (u32x4){0u, 0u, 0u, 0u};
    if (bid == 1 && tid < 48) *(unsigned*)(ws + WS_CNT + tid * 256) = 0u;
  } else if (bid < 368) {
    const int g = bid - 128;
    const int cgp = (g & 7) * 30 + (g >> 3);
    const int l2 = tid & 1, rg = tid >> 1;
    u32x4 v[5];
    unsigned short cv16[5];
#pragma unroll
    for (int q = 0; q < 5; ++q) {
      const int k = q * 128 + rg;
      v[q] = *(const u32x4*)(wmod + (size_t)k * MODN + cgp * 16 + l2 * 8);
      cv16[q] = c[k];
    }
    float acc[8];
#pragma unroll
    for (int e = 0; e < 8; ++e) acc[e] = 0.f;
#pragma unroll
    for (int q = 0; q < 5; ++q) {
      const float cv = bf2f(cv16[q]);
      const float sv = bfr(cv / (1.0f + __expf(-cv)));
#pragma unroll
      for (int e = 0; e < 4; ++e) {
        acc[2 * e] += sv * lo16(v[q][e]);
        acc[2 * e + 1] += sv * hi16(v[q][e]);
      }
    }
#pragma unroll
    for (int o = 2; o < 64; o <<= 1)
#pragma unroll
      for (int e = 0; e < 8; ++e) acc[e] += __shfl_xor(acc[e], o, 64);
    float* red = (float*)smem;
    if (lane < 2) {
#pragma unroll
      for (int e = 0; e < 8; ++e) red[(tid >> 6) * 16 + lane * 8 + e] = acc[e];
    }
    wait_lgkm0();
    bar();
    if (tid < 16) {
      float m = bfr((red[tid] + red[16 + tid]) + (red[32 + tid] + red[48 + tid]));
      const int n = cgp * 16 + tid;
      const int part = n / D_, d = n - part * D_;
      if (part == 1 || part == 4) m = bfr(1.0f + m);
      ((float*)(ws + WS_MOD))[n] = m;
      if (part == 4) ((unsigned short*)(ws + WS_E))[d] = (unsigned short)(__float_as_uint(m) >> 16);
      if (part == 3) ((unsigned short*)(ws + WS_E))[D_ + d] = (unsigned short)(__float_as_uint(m) >> 16);
    }
  } else if (bid < 464) {
    const int k0 = (bid - 368) * 16;
    u32x4 v[5];
#pragma unroll
    for (int q = 0; q < 5; ++q) v[q] = LD1((const u32x4*)((const char*)w2 + (size_t)k0 * (D_ * 2) + (tid + 256 * q) * 16));
#pragma unroll
    for (int q = 0; q < 5; ++q) {
      const int id = tid + 256 * q;
      const int r = id / 80, ch = id - r * 80;
      const int cb = ch / 10, cc = ch - cb * 10;
      *(u32x4*)(ws + WS_W2T + ((size_t)cb * F_ + k0 + swap23(r)) * 160 + cc * 16) = v[q];
    }
  } else if (bid < 560) {
    const int c0 = (bid - 464) * 1280;
    u32x4 v[5];
#pragma unroll
    for (int q = 0; q < 5; ++q) v[q] = LD1((const u32x4*)((const char*)w1 + (size_t)(c0 + tid + 256 * q) * 16));
#pragma unroll
    for (int q = 0; q < 5; ++q) {
      const int id = c0 + tid + 256 * q;
      const int k = id / 192, ch = id - k * 192;
      const int cb = ch / 12, cc = ch - cb * 12;
      const int r = swap23(k);
      *(u32x4*)(ws + WS_W1T + ((size_t)cb * D_ + r) * 192 + (cc ^ (((r >> 2) & 1) << 1)) * 16) = v[q];
    }
  } else if (bid < 640) {
    const int m = bid >= 600, c0 = (bid - 560 - 40 * m) * 1280;
    u32x4 v[5];
#pragma unroll
    for (int q = 0; q < 5; ++q) v[q] = LD1((const u32x4*)((const char*)(m ? dim : dre) + (size_t)(c0 + tid + 256 * q) * 16));
#pragma unroll
    for (int q = 0; q < 5; ++q) {
      const int id = c0 + tid + 256 * q;
      const int e = id / 80, ch = id - e * 80;
      const int cb = e / 80, row = e - cb * 80;
      *(u32x4*)(ws + WS_WDT + ((size_t)(cb * 20 + m * 10 + (ch >> 3)) * 80 + row) * 128 + ((ch & 7) ^ ((row >> 1) & 7)) * 16) = v[q];
    }
  } else {
    const int pw = (bid - 640) * 4 + __builtin_amdgcn_readfirstlane(tid >> 6);
    constexpr int RPW = 16 / WPF_KB;
    const int row0 = pw * RPW;
    const char* src = (const char*)(row0 < 2048 ? wre : wim) + (size_t)(row0 & 2047) * 4096;
    u32x4 sink[16];
#pragma unroll
    for (int i = 0; i < RPW; ++i)
#pragma unroll
      for (int q = 0; q < WPF_KB; ++q)
        asm volatile("global_load_dwordx4 %0, %1, %2 offset:%3" : "=v"(sink[WPF_KB * i + q]) : "v"(lane * 16), "s"(src + i * 4096), "n"(q * 1024) : "memory");
    wait_vm<0>();
#pragma unroll
    for (int i = 0; i < 16; ++i) asm volatile("" ::"v"(sink[i]));
  }
}

__device__ __forceinline__ void dma_res_tile(const char* base  , unsigned lds, int w, int lane) {
#pragma unroll
  for (int j = 0; j < 3; ++j) {
    int L = 160 * w + 64 * j + lane;
    if (L > 639) L = 639;
    const int r = L / 10, ch = L - r * 10;
    if (j < 2) dma16(r * (D_ * 2) + ch * 16, base, lds + (160 * w + 64 * j) * 16);
    else dma16h(r * (D_ * 2) + ch * 16, base, lds + (160 * w + 128) * 16);
  }
}
template <int N>
__device__ __forceinline__ void l2_warm(const char* base, unsigned dummy_lds, int lane) {
#pragma unroll
  for (int i = 0; i < N; ++i) dma16(lane * 16, base + i * 1024, dummy_lds);
}

struct SchedP2 { static constexpr char s[] = "MRMRMMRMRMMRMRMMRMRMMRMMMMMMMMMMM"; };
struct SchedP3 { static constexpr char s[] = "RRMRMRRMRMRRMRMRRMRMMM"; };
struct SchedP4 { static constexpr char s[] = "MRMRMRMMRMRMRMMRMRMMRMRMRMMRMRMRMMRMRMMMMMMMMM"; };
constexpr int cnt_ch(const char* s, char c) { int n = 0; for (; *s; ++s) n += (*s == c); return n; }
static_assert(cnt_ch(SchedP2::s, 'M') == 24 && cnt_ch(SchedP2::s, 'R') == 9, "");
static_assert(cnt_ch(SchedP3::s, 'M') == 10 && cnt_ch(SchedP3::s, 'R') == 12, "");
static_assert(cnt_ch(SchedP4::s, 'M') == 30 && cnt_ch(SchedP4::s, 'R') == 16, "");
constexpr int L_RES = 147456, L_MOD = 159744, L_DUM = L_MOD + 2048, L_TOTAL = L_DUM + 1024;
#define P2NS 5
constexpr int P2_NS = P2NS, P2_STAGE = 26624, P2_X = 2 * P2_STAGE, P2_STG = P2_X + 49152;
constexpr int P3_STAGE = 36864;
constexpr int P4_STAGE = 28672, P4_E = 4 * P4_STAGE, P4_ST = P4_E + 4096, P4_RS = P4_ST + 8192, P4_STG = 2 * P4_STAGE;

__global__ __launch_bounds__(512, 2) void mega(const unsigned short* __restrict__ wre,
                                               const unsigned short* __restrict__ wim,
                                               const unsigned short* __restrict__ x,
                                               char* __restrict__ ws,
                                               unsigned short* __restrict__ out) {
  STAMP_DECL
  STAMP(0);
  extern __shared__ __attribute__((aligned(16))) char smem[];
  const int tid = threadIdx.x, lane = tid & 63;
  const int wv = __builtin_amdgcn_readfirstlane(tid >> 6);
  const int w = wv & 3;
  const bool prod = wv < 4;
  const int bid = blockIdx.x;
  const int xcd = bid & 7, bi = bid >> 3;
  const int cb = bi & 7, rsel = bi >> 3;
  const int g = lane >> 4, l15 = lane & 15;
  const unsigned lds0 = lds_off(smem);
  const float* mod = (const float*)(smem + L_MOD);
  const int t3 = (xcd * 4 + rsel) * 64;
  const int grp3 = xcd * 2 + (rsel >> 1);

  {
    const int isim = rsel >> 1;
    const int grp2 = xcd * 2 + (rsel & 1);
    const int t0 = grp2 * 128;
    if (prod) {
      const char* rbase = (const char*)(isim ? wim : wre) + (size_t)t0 * (S_ * 2);
      const char* cbase = ws + WS_HT + (size_t)cb * 32 * 10240;
      unsigned roff[4];
#pragma unroll
      for (int j = 0; j < 4; ++j) {
        const int row = 32 * w + 8 * j + (lane >> 3);
        roff[j] = row * (S_ * 2) + 16 * ((lane & 7) ^ ((row >> 1) & 7));
      }
      const unsigned coff = (20 * w) * 128 + lane * 16;
      producer<P2_NS, 32, 7>([&](int ks, int buf) {
        const unsigned lb = lds0 + buf * P2_STAGE;
        {
#pragma unroll
          for (int j = 0; j < 4; ++j) dma16(roff[j], rbase + ks * 128, lb + (32 * w + 8 * j) * 128);
        }
        dma16(coff, cbase + ks * 10240, lb + 16384 + (20 * w) * 128);
        dma16(coff, cbase + ks * 10240 + 1024, lb + 16384 + (20 * w + 8) * 128);
        dma16h(coff, cbase + ks * 10240 + 2048, lb + 16384 + (20 * w + 16) * 128);
      });
      bar();
    } else {
      if (w < 2) {
        int L = 64 * w + lane; if (L > 99) L = 99;
        const int vec = L / 20, part = vec + (vec >= 3);
        dma16(part * 2560 + cb * 320 + (L - vec * 20) * 16, ws + WS_MOD, lds0 + L_MOD + w * 1024);
      }
      dma_res_tile((const char*)x + (size_t)t3 * (D_ * 2) + cb * 160, lds0 + L_RES, w, lane);
      const int wm = w & 1, wk = w >> 1;
      const int fsw = ((4 * wk + g) ^ (l15 >> 1)) * 16;
      const int ra = (64 * wm + l15) * 128 + fsw;
      const int ca_ = 16384 + (2 * wk + (g >> 1)) * 2560 + l15 * 32 + (g & 1) * 16;
      bf16x8 rf[2][4], cf[2][5];
      f32x4 acc[6][4];
#pragma unroll
      for (int j = 0; j < 6; ++j)
#pragma unroll
        for (int i = 0; i < 4; ++i) acc[j][i] = (f32x4){0.f, 0.f, 0.f, 0.f};
      const s16x8 ones_s = {0x3F80, 0x3F80, 0x3F80, 0x3F80, 0x3F80, 0x3F80, 0x3F80, 0x3F80};
      const bf16x8 ones = __builtin_bit_cast(bf16x8, ones_s);
      auto ldf = [&](auto par, auto ic, int buf) {
        constexpr int PP = decltype(par)::value, n = decltype(ic)::value;
        const char* lb = smem + buf * P2_STAGE;
        if constexpr (n == 0) rf[PP][0] = lds_frag(lb + ra);
        else if constexpr (n < 6) cf[PP][n - 1] = lds_frag(lb + ca_ + (n - 1) * 512);
        else rf[PP][n - 5] = lds_frag(lb + ra + (n - 5) * 2048);
      };
      auto body = [&](auto par, auto dl, int rd) {
        constexpr int PP = decltype(par)::value, DL = decltype(dl)::value;
        auto fm = [&](auto mc) {
          constexpr int m = decltype(mc)::value, i = m / 6, j = m % 6;
          if constexpr (j < 5) acc[j][i] = MFMA16(cf[PP][j], rf[PP][i], acc[j][i]);
          else acc[5][i] = MFMA16(ones, rf[PP][i], acc[5][i]);
        };
        auto fr = [&](auto rc) { ldf(IC<PP ^ 1>{}, rc, rd); };
        run_sched<SchedP2, 0, 0, 0, DL>(fm, fr);
      };
      auto load0 = [&] { static_for<9>([&](auto ic) { ldf(IC<0>{}, ic, 0); }); };
      consumer<P2_NS, 32>(load0, body);
      STAMP(1);
      auto finish = [&](auto keepc) {
        constexpr int KP = decltype(keepc)::value;
        char* dst = smem + P2_X + w * 12288 + lane * 16;
#pragma unroll
        for (int j = 0; j < 5; ++j) {
          *(f32x4*)(dst + (2 * j) * 1024) = acc[j][2 * (KP ^ 1)];
          *(f32x4*)(dst + (2 * j + 1) * 1024) = acc[j][2 * (KP ^ 1) + 1];
        }
        *(f32x2*)(dst + 10240) = (f32x2){acc[5][2 * (KP ^ 1)][0], acc[5][2 * (KP ^ 1) + 1][0]};
        wait_vm<0>();
        wait_lgkm0();
        bar();
        const char* srcp = smem + P2_X + (w ^ 2) * 12288 + lane * 16;
        const float sgn = isim ? -1.0f : 1.0f;
        const f32x2 rp = *(const f32x2*)(srcp + 10240);
        const float rs0 = (acc[5][2 * KP][0] + rp[0]) * sgn, rs1 = (acc[5][2 * KP + 1][0] + rp[1]) * sgn;
        char* stg = smem + P2_STG + (64 * wm + 32 * KP + l15) * 176 + g * 8;
#pragma unroll
        for (int j = 0; j < 5; ++j) {
          const f32x4 sa = *(const f32x4*)(mod + 16 * j + 4 * g);
          const f32x4 c1 = *(const f32x4*)(mod + 80 + 16 * j + 4 * g) * sgn;
          const f32x4 v0 = c1 * (acc[j][2 * KP] + *(const f32x4*)(srcp + (2 * j) * 1024)) + sa * rs0;
          const f32x4 v1 = c1 * (acc[j][2 * KP + 1] + *(const f32x4*)(srcp + (2 * j + 1) * 1024)) + sa * rs1;
          *(u32x2*)(stg + j * 32) = (u32x2){pk2(v0[0], v0[1]), pk2(v0[2], v0[3])};
          *(u32x2*)(stg + 16 * 176 + j * 32) = (u32x2){pk2(v1[0], v1[1]), pk2(v1[2], v1[3])};
        }
      };
      if (wk == 0) finish(IC<0>{}); else finish(IC<1>{});
    }
    wait_lgkm0();
    bar();
    {
      char* pq = ws + WS_PQ + ((size_t)(grp2 * 2) * 80 + isim * 40 + cb * 5) * 2048;
      for (int id = tid; id < 1280; id += 512) {
        const int rbh = id >= 640, r_ = id - 640 * rbh;
        const int gq = r_ >> 7, row = rbh * 64 + ((r_ & 127) >> 1), c = 2 * gq + (r_ & 1);
        st16_wt(pq + (size_t)rbh * (80 * 2048) + r_ * 16, *(const u32x4*)(smem + P2_STG + row * 176 + c * 16));
      }
    }
    wait_lgkm0();
    bar();
    STAMP(2);
    const char* wdt = ws + WS_WDT + (size_t)cb * 204800;
    if (prod) {
#pragma unroll
      for (int s = 0; s < PFD; ++s)
#pragma unroll
        for (int j = 0; j < 5; ++j)
          dma16(lane * 16, wdt + s * 20480 + w * 5120 + j * 1024, lds0 + s * P3_STAGE + 16384 + w * 5120 + j * 1024);
      wait_vm<5 * PFD>();
    } else {
      wait_vm<0>();
    }
    bar();
    STAMP(3);
    if (wv == 4) {
      if (lane == 0) add_count(ws, grp2);
      wait_count(ws, grp3, 16);
    }
    bar();
  }

  {
    const char* wdt = ws + WS_WDT + (size_t)cb * 204800;
    if (prod) {
      const char* rbase = ws + WS_PQ + (size_t)(t3 >> 6) * (80 * 2048) + w * 4096;
      STAMP(4);
      producer_seam<10, 4, 5>([&](int ks, int buf) {
#pragma unroll
        for (int j = 0; j < 4; ++j) dma16s(lane * 16, rbase + ks * 16384 + j * 1024, lds0 + buf * P3_STAGE + w * 4096 + j * 1024);
      }, [&](int ks, int buf) {
#pragma unroll
        for (int j = 0; j < 5; ++j)
          dma16(lane * 16, wdt + ks * 20480 + w * 5120 + j * 1024, lds0 + buf * P3_STAGE + 16384 + w * 5120 + j * 1024);
      }, [] {});
    } else {
      const int sw0 = (g ^ (l15 >> 1)) * 16, sw1 = ((4 + g) ^ (l15 >> 1)) * 16;
      const int ra = (g >> 1) * 2048 + (16 * w + l15) * 32 + (g & 1) * 16, ca_ = 16384 + l15 * 128;
      bf16x8 rf[2][2], cf[2][2][5];
      f32x4 acc[5];
#pragma unroll
      for (int j = 0; j < 5; ++j) acc[j] = (f32x4){0.f, 0.f, 0.f, 0.f};
      auto ldf = [&](auto hc, auto ic, int buf) {
        constexpr int H = decltype(hc)::value, n = decltype(ic)::value, kk = n / 6, m = n % 6;
        const char* lb = smem + buf * P3_STAGE;
        if constexpr (m == 0) rf[H][kk] = lds_frag(lb + ra + H * 8192 + kk * 4096);
        else cf[H][kk][m - 1] = lds_frag(lb + (kk ? sw1 : sw0) + ca_ + H * 10240 + (m - 1) * 2048);
      };
      auto half = [&](auto par, auto dl, int buf) {
        constexpr int PP = decltype(par)::value, DL = decltype(dl)::value;
        auto fm = [&](auto mc) {
          constexpr int m = decltype(mc)::value, kk = m / 5, j = m % 5;
          acc[j] = MFMA16(cf[PP][kk][j], rf[PP][kk], acc[j]);
        };
        auto fr = [&](auto rc) { ldf(IC<PP ^ 1>{}, rc, buf); };
        run_sched<SchedP3, 0, 0, 0, DL>(fm, fr);
      };
      auto load0 = [&] { STAMP(4); static_for<12>([&](auto ic) { ldf(IC<0>{}, ic, 0); }); };
      consumer_half<10>(load0, half);
      STAMP(5);
      char* res = smem + L_RES + (16 * w + l15) * 160 + g * 8;
#pragma unroll
      for (int j = 0; j < 5; ++j) {
        const u32x2 xr = *(const u32x2*)(res + j * 32);
        const f32x4 ga = *(const f32x4*)(mod + 160 + 16 * j + 4 * g);
        const f32x4 y = acc[j];
        *(u32x2*)(res + j * 32) = (u32x2){pk2(lo16(xr[0]) + ga[0] * y[0], hi16(xr[0]) + ga[1] * y[1]),
                                          pk2(lo16(xr[1]) + ga[2] * y[2], hi16(xr[1]) + ga[3] * y[3])};
      }
    }
    wait_lgkm0();
    bar();
    {
      const int row = tid >> 3, c0 = tid & 7;
      char* xc = ws + WS_XC + ((size_t)(t3 >> 6) * 40 + cb * 5) * 2048 + row * 32;
      float xf[2][8];
      float s = 0.f;
#pragma unroll
      for (int it = 0; it < 2; ++it) {
        const int c = c0 + 8 * it;
        if (c < 10) {
          const u32x4 xr = *(const u32x4*)(smem + L_RES + row * 160 + c * 16);
          const f32x4 c10 = *(const f32x4*)(mod + 240 + c * 8), c11 = *(const f32x4*)(mod + 244 + c * 8);
#pragma unroll
          for (int e = 0; e < 4; ++e) { xf[it][2 * e] = lo16(xr[e]); xf[it][2 * e + 1] = hi16(xr[e]); s += xf[it][2 * e] + xf[it][2 * e + 1]; }
          const u32x4 oc = {pk2(xf[it][0] * c10[0], xf[it][1] * c10[1]), pk2(xf[it][2] * c10[2], xf[it][3] * c10[3]),
                            pk2(xf[it][4] * c11[0], xf[it][5] * c11[1]), pk2(xf[it][6] * c11[2], xf[it][7] * c11[3])};
          st16_wt(xc + (c >> 1) * 2048 + (c & 1) * 16, oc);
        }
      }
#pragma unroll
      for (int o = 1; o < 8; o <<= 1) s += __shfl_xor(s, o, 64);
      const float ml = s * (1.0f / 80.0f);
      float m2 = 0.f;
#pragma unroll
      for (int e = 0; e < 8; ++e) { float d = xf[0][e] - ml; m2 += d * d; }
      if (c0 < 2) {
#pragma unroll
        for (int e = 0; e < 8; ++e) { float d = xf[1][e] - ml; m2 += d * d; }
      }
#pragma unroll
      for (int o = 1; o < 8; o <<= 1) m2 += __shfl_xor(m2, o, 64);
      if (c0 == 0) st8_wt(ws + WS_ST + ((size_t)(t3 >> 6) * 8 + cb) * 512 + row * 8, (f32x2){s, m2});
    }
    STAMP(6);
    const char* w1t = ws + WS_W1T + (size_t)(bi & 15) * 122880;
    if (prod) {
      dma16(w * 1024 + lane * 16, ws + WS_E, lds0 + P4_E + w * 1024);
#pragma unroll
      for (int s = 0; s < PFD; ++s)
#pragma unroll
        for (int j = 0; j < 3; ++j)
          dma16(lane * 16, w1t + s * 12288 + w * 3072 + j * 1024, lds0 + s * P4_STAGE + 16384 + w * 3072 + j * 1024);
      wait_vm<1 + 3 * PFD>();
    } else {
      wait_vm<0>();
    }
    bar();
    STAMP(7);
    if (wv == 4) {
      if (lane == 0) add_count(ws, 16 + grp3);
      wait_count(ws, 16 + xcd * 2 + (bi >> 4), 16);
    }
    bar();
  }

  {
    const int cb4 = bi & 15, grp4 = xcd * 2 + (bi >> 4);
    const int t0 = grp4 * 128;
    const char* w1t = ws + WS_W1T + (size_t)cb4 * 122880;
    if (prod) {
      const char* rbase = ws + WS_XC + (size_t)(grp4 * 2 + (w >> 1)) * (40 * 2048) + (w & 1) * 4096;
      STAMP(8);
      dma16s(w * 2048 + lane * 16, ws + WS_ST + (size_t)t0 * 64, lds0 + P4_ST + w * 2048);
      dma16s(w * 2048 + 1024 + lane * 16, ws + WS_ST + (size_t)t0 * 64, lds0 + P4_ST + w * 2048 + 1024);
      producer_seam<10, 4, 3>([&](int ks, int buf) {
#pragma unroll
        for (int j = 0; j < 4; ++j) dma16s(lane * 16, rbase + ks * 8192 + j * 1024, lds0 + buf * P4_STAGE + w * 4096 + j * 1024);
      }, [&](int ks, int buf) {
#pragma unroll
        for (int j = 0; j < 3; ++j)
          dma16(lane * 16, w1t + ks * 12288 + w * 3072 + j * 1024, lds0 + buf * P4_STAGE + 16384 + w * 3072 + j * 1024);
      }, [&] {
        if (tid < 128) {
          const char* sp = smem + P4_ST + (tid >> 6) * 4096 + (tid & 63) * 8;
          f32x2 pp[8];
#pragma unroll
          for (int e = 0; e < 8; ++e) pp[e] = *(const f32x2*)(sp + e * 512);
          const float mean = (((pp[0][0] + pp[1][0]) + (pp[2][0] + pp[3][0])) + ((pp[4][0] + pp[5][0]) + (pp[6][0] + pp[7][0]))) * (1.0f / D_);
          float m2 = ((pp[0][1] + pp[1][1]) + (pp[2][1] + pp[3][1])) + ((pp[4][1] + pp[5][1]) + (pp[6][1] + pp[7][1]));
          float dd = 0.f;
#pragma unroll
          for (int e = 0; e < 8; ++e) { const float d = pp[e][0] * (1.0f / 80.0f) - mean; dd += d * d; }
          m2 += 80.0f * dd;
          const float rden = 1.0f / bfr(sqrtf(bfr(bfr(m2 * (1.0f / D_)) + 1e-5f)));
          *(f32x2*)(smem + P4_RS + tid * 8) = (f32x2){bfr(mean), rden};
        }
        wait_lgkm0();
      });
    } else {
      const int q = l15 >> 2, p4 = lane & 3;
      const int wm = w & 1, wn = w >> 1;
      const int rpos = 16 * (g >> 1) + 4 * (g & 1) + q;
      const int xs = (rpos >> 2) & 1;
      int cadr[3];
#pragma unroll
      for (int j = 0; j < 3; ++j) {
        const int jj = 3 * wn + j;
        cadr[j] = 16384 + rpos * 192 + (2 * (jj ^ xs) + (p4 >> 1)) * 16 + (p4 & 1) * 8;
      }
      const int ra = wm * 8192 + (g >> 1) * 2048 + l15 * 32 + (g & 1) * 16;
      int ea = P4_E + (l15 < 2 ? l15 : 2) * 1280 + g * 16;
      bf16x8 rf[2][2][5], cf[2][2][3];
      f32x4 acc[3][5];
#pragma unroll
      for (int j = 0; j < 3; ++j)
#pragma unroll
        for (int i = 0; i < 5; ++i) acc[j][i] = (f32x4){0.f, 0.f, 0.f, 0.f};
      auto ldf = [&](auto par, auto ic, int buf) {
        constexpr int PP = decltype(par)::value, n = decltype(ic)::value, kk = n / 8, m = n % 8;
        const char* lb = smem + buf * P4_STAGE;
        if constexpr (m == 0) rf[PP][kk][0] = lds_frag(lb + ra + kk * 4096);
        else if constexpr (m < 4) cf[PP][kk][m - 1] = cat_frag(lds_tr(lb + cadr[m - 1] + kk * 6144), lds_tr(lb + cadr[m - 1] + kk * 6144 + 1536));
        else if constexpr (m < 7) rf[PP][kk][m - 3] = lds_frag(lb + ra + kk * 4096 + (m - 3) * 512);
        else {
          rf[PP][kk][4] = lds_frag(smem + ea + kk * 64);
          if constexpr (kk == 1) ea += 128;
        }
      };
      auto body = [&](auto par, auto dl, int rd) {
        constexpr int PP = decltype(par)::value, DL = decltype(dl)::value;
        auto fm = [&](auto mc) {
          constexpr int m = decltype(mc)::value, kk = m / 15, i = (m % 15) / 3, j = m % 3;
          acc[j][i] = MFMA16(cf[PP][kk][j], rf[PP][kk][i], acc[j][i]);
        };
        auto fr = [&](auto rc) { ldf(IC<PP ^ 1>{}, rc, rd); };
        run_sched<SchedP4, 0, 0, 0, DL>(fm, fr);
      };
      auto load0 = [&] { STAMP(8); static_for<16>([&](auto ic) { ldf(IC<0>{}, ic, 0); }); };
      consumer<4, 10>(load0, body);
      STAMP(9);
      f32x4 u[3], v[3];
#pragma unroll
      for (int j = 0; j < 3; ++j)
#pragma unroll
        for (int e = 0; e < 4; ++e) {
          const int a = __float_as_int(acc[j][4][e]);
          u[j][e] = __int_as_float(__builtin_amdgcn_update_dpp(0, a, 0x150, 0xf, 0xf, false));
          v[j][e] = __int_as_float(__builtin_amdgcn_update_dpp(0, a, 0x151, 0xf, 0xf, false));
        }
      char* stg = smem + P4_STG + (64 * wm + l15) * 208 + (48 * wn + 4 * g) * 2;
#pragma unroll
      for (int i = 0; i < 4; ++i) {
        const f32x2 rs = *(const f32x2*)(smem + P4_RS + (64 * wm + 16 * i + l15) * 8);
#pragma unroll
        for (int j = 0; j < 3; ++j) {
          const f32x4 hv = (acc[j][i] - u[j] * rs[0]) * rs[1] + v[j];
          *(u32x2*)(stg + i * 16 * 208 + j * 32) =
              (u32x2){pk2(fmaxf(hv[0], 0.f), fmaxf(hv[1], 0.f)), pk2(fmaxf(hv[2], 0.f), fmaxf(hv[3], 0.f))};
        }
      }
    }
    wait_lgkm0();
    bar();
    {
      char* h1 = ws + WS_H1 + ((size_t)(grp4 * 2) * 96 + cb4 * 6) * 2048;
#pragma unroll
      for (int it = 0; it < 3; ++it) {
        const int id = tid + 512 * it;
        const int rbh = id >= 768, r_ = id - 768 * rbh;
        const int gq = r_ >> 7, row = rbh * 64 + ((r_ & 127) >> 1), c = 2 * gq + (r_ & 1);
        st16_wt(h1 + (size_t)rbh * (96 * 2048) + r_ * 16, *(const u32x4*)(smem + P4_STG + row * 208 + c * 16));
      }
    }
    wait_lgkm0();
    bar();
    STAMP(10);
    const char* w2t = ws + WS_W2T + (size_t)cb * F_ * 160;
    if (prod) {
#pragma unroll
      for (int s = 0; s < PFD; ++s)
#pragma unroll
        for (int j = 0; j < 5; ++j)
          dma16(lane * 16, w2t + s * 20480 + w * 5120 + j * 1024, lds0 + s * P3_STAGE + 16384 + w * 5120 + j * 1024);
      wait_vm<5 * PFD>();
    } else {
      wait_vm<0>();
    }
    bar();
    STAMP(11);
    if (wv == 4) {
      if (lane == 0) add_count(ws, 32 + grp4);
      wait_count(ws, 32 + grp3, 16);
    }
    bar();
  }

  {
    const char* w2t = ws + WS_W2T + (size_t)cb * F_ * 160;
    if (prod) {
      const char* rbase = ws + WS_H1 + (size_t)(t3 >> 6) * (96 * 2048) + w * 4096;
      STAMP(12);
      producer_seam<12, 4, 5>([&](int ks, int buf) {
#pragma unroll
        for (int j = 0; j < 4; ++j) dma16s(lane * 16, rbase + ks * 16384 + j * 1024, lds0 + buf * P3_STAGE + w * 4096 + j * 1024);
      }, [&](int ks, int buf) {
#pragma unroll
        for (int j = 0; j < 5; ++j)
          dma16(lane * 16, w2t + ks * 20480 + w * 5120 + j * 1024, lds0 + buf * P3_STAGE + 16384 + w * 5120 + j * 1024);
      }, [] {});
    } else {
      const int q = l15 >> 2, p4 = lane & 3;
      const int ra = (g >> 1) * 2048 + (16 * w + l15) * 32 + (g & 1) * 16;
      const int ca_ = 16384 + (16 * (g >> 1) + 4 * (g & 1) + q) * 160 + p4 * 8;
      bf16x8 rf[2][2], cf[2][2][5];
      f32x4 acc[5];
#pragma unroll
      for (int j = 0; j < 5; ++j) acc[j] = (f32x4){0.f, 0.f, 0.f, 0.f};
      auto ldf = [&](auto hc, auto ic, int buf) {
        constexpr int H = decltype(hc)::value, n = decltype(ic)::value, kk = n / 6, m = n % 6;
        const char* lb = smem + buf * P3_STAGE;
        if constexpr (m == 0) rf[H][kk] = lds_frag(lb + ra + H * 8192 + kk * 4096);
        else cf[H][kk][m - 1] = cat_frag(lds_tr(lb + ca_ + H * 10240 + kk * 5120 + (m - 1) * 32),
                                         lds_tr(lb + ca_ + H * 10240 + kk * 5120 + (m - 1) * 32 + 1280));
      };
      auto half = [&](auto par, auto dl, int buf) {
        constexpr int PP = decltype(par)::value, DL = decltype(dl)::value;
        auto fm = [&](auto mc) {
          constexpr int m = decltype(mc)::value, kk = m / 5, j = m % 5;
          acc[j] = MFMA16(cf[PP][kk][j], rf[PP][kk], acc[j]);
        };
        auto fr = [&](auto rc) { ldf(IC<PP ^ 1>{}, rc, buf); };
        run_sched<SchedP3, 0, 0, 0, DL>(fm, fr);
      };
      auto load0 = [&] { STAMP(12); static_for<12>([&](auto ic) { ldf(IC<0>{}, ic, 0); }); };
      consumer_half<12>(load0, half);
      STAMP(13);
      char* res = smem + L_RES + (16 * w + l15) * 160 + g * 8;
#pragma unroll
      for (int j = 0; j < 5; ++j) {
        const u32x2 xr = *(const u32x2*)(res + j * 32);
        const f32x4 gb = *(const f32x4*)(mod + 320 + 16 * j + 4 * g);
        const f32x4 y = acc[j];
        *(u32x2*)(res + j * 32) = (u32x2){pk2(lo16(xr[0]) + gb[0] * y[0], hi16(xr[0]) + gb[1] * y[1]),
                                          pk2(lo16(xr[1]) + gb[2] * y[2], hi16(xr[1]) + gb[3] * y[3])};
      }
      wait_vm<0>();
    }
    wait_lgkm0();
    bar();
    for (int id = tid; id < 640; id += 512) {
      const int row = id / 10, c = id - row * 10;
      *(u32x4*)((char*)out + (size_t)(t3 + row) * (D_ * 2) + cb * 160 + c * 16) = *(const u32x4*)(smem + L_RES + row * 160 + c * 16);
    }
  }
  STAMP(14);
  STAMP_OUT();
}

extern "C" void kernel_launch(void* const* d_in, const int* in_sizes, int n_in,
                              void* d_out, int out_size, void* d_ws, size_t ws_size,
                              hipStream_t stream) {
  const unsigned short* x    = (const unsigned short*)d_in[0];
  const unsigned short* wsre = (const unsigned short*)d_in[1];
  const unsigned short* wsim = (const unsigned short*)d_in[2];
  const unsigned short* wdre = (const unsigned short*)d_in[3];
  const unsigned short* wdim = (const unsigned short*)d_in[4];
  const unsigned short* w1   = (const unsigned short*)d_in[6];
  const unsigned short* w2   = (const unsigned short*)d_in[7];
  const unsigned short* c    = (const unsigned short*)d_in[8];
  const unsigned short* wmod = (const unsigned short*)d_in[9];
  char* ws = (char*)d_ws;
  unsigned short* out = (unsigned short*)d_out;
  (void)hipFuncSetAttribute((const void*)mega, hipFuncAttributeMaxDynamicSharedMemorySize, L_TOTAL);
  k1_ln_mod<<<640 + 64 * WPF_KB, 256, 0, stream>>>(x, c, wmod, wsre, wsim, w2, wdre, wdim, w1, ws);
  mega<<<256, 512, L_TOTAL, stream>>>(wsre, wsim, x, ws, out);
}
```

```cpp
#include <hip/hip_runtime.h>
#include <hip/hip_bf16.h>
#include <stdint.h>

typedef __attribute__((ext_vector_type(8))) __bf16 bf16x8;
typedef __attribute__((ext_vector_type(4))) __bf16 bf16x4;
typedef __attribute__((ext_vector_type(2))) __bf16 bf16x2;
typedef __attribute__((ext_vector_type(4))) float f32x4;
typedef __attribute__((ext_vector_type(2))) float f32x2;
typedef __attribute__((ext_vector_type(4))) short s16x4;
typedef __attribute__((ext_vector_type(8))) short s16x8;
typedef __attribute__((ext_vector_type(4))) unsigned u32x4;
typedef __attribute__((ext_vector_type(2))) unsigned u32x2;
typedef __attribute__((address_space(3))) s16x4* lds_s16x4_ptr;
typedef __attribute__((address_space(3))) void* lds_vptr;

constexpr int S_ = 2048, D_ = 640, F_ = 1536, MODN = 3840;

constexpr size_t WS_MOD = 0;
constexpr size_t WS_E   = 16384;
constexpr size_t WS_CNT = 20480;
constexpr size_t WS_ST  = 32768;
constexpr size_t WS_HT  = WS_ST + (size_t)S_ * 64;
constexpr size_t WS_PQ  = WS_HT + (size_t)D_ * S_ * 2;
constexpr size_t WS_XC  = WS_PQ + (size_t)S_ * 1280 * 2;
constexpr size_t WS_H1  = WS_XC + (size_t)S_ * D_ * 2;
constexpr size_t WS_W2T = WS_H1 + (size_t)S_ * F_ * 2;
constexpr size_t WS_W1T = WS_W2T + (size_t)F_ * D_ * 2;
constexpr size_t WS_WDT = WS_W1T + (size_t)F_ * D_ * 2;

#define STAMP_DECL
#define STAMP(i)
#define STAMP_OUT()
template <int V> struct IC { static constexpr int value = V; };
template <int N, int I = 0, class F> __device__ __forceinline__ void static_for(F f) {
  if constexpr (I < N) { f(IC<I>{}); static_for<N, I + 1>(f); }
}

__device__ __forceinline__ float bf2f(unsigned short u) { return __uint_as_float(((unsigned)u) << 16); }
__device__ __forceinline__ float bfr(float f) { return (float)(__bf16)f; }
__device__ __forceinline__ unsigned pk2(float a, float b) {
  bf16x2 v = __builtin_convertvector((f32x2){a, b}, bf16x2);
  return __builtin_bit_cast(unsigned, v);
}
__device__ __forceinline__ float lo16(unsigned u) { return __uint_as_float(u << 16); }
__device__ __forceinline__ float hi16(unsigned u) { return __uint_as_float(u & 0xffff0000u); }

__device__ __forceinline__ void dma16(unsigned voff, const char* sbase, unsigned lds) {
  asm volatile("s_mov_b32 m0, %2\n\ts_nop 0\n\tglobal_load_lds_dwordx4 %0, %1"
               ::"v"(voff), "s"(sbase), "s"(lds) : "memory");
}
__device__ __forceinline__ void dma16nt(unsigned voff, const char* sbase, unsigned lds) {
  asm volatile("s_mov_b32 m0, %2\n\ts_nop 0\n\tglobal_load_lds_dwordx4 %0, %1 nt"
               ::"v"(voff), "s"(sbase), "s"(lds) : "memory");
}
__device__ __forceinline__ void dma16s(unsigned voff, const char* sbase, unsigned lds) {
  asm volatile("s_mov_b32 m0, %2\n\ts_nop 0\n\tglobal_load_lds_dwordx4 %0, %1 sc1"
               ::"v"(voff), "s"(sbase), "s"(lds) : "memory");
}
#define LD1(p) __builtin_nontemporal_load(p)
__device__ __forceinline__ void dma16h(unsigned voff, const char* sbase, unsigned lds) {
  asm volatile("s_mov_b32 m0, %2\n\ts_mov_b32 exec_hi, 0\n\tglobal_load_lds_dwordx4 %0, %1\n\ts_mov_b32 exec_hi, -1"
               ::"v"(voff), "s"(sbase), "s"(lds) : "memory");
}
__device__ __forceinline__ unsigned lds_off(const void* p) {
  return (unsigned)(uintptr_t)(__attribute__((address_space(3))) const char*)p;
}
#define SB0() __builtin_amdgcn_sched_barrier(0)
template <int N> __device__ __forceinline__ void wait_vm_lgkm0() {
  asm volatile("s_waitcnt vmcnt(%0) lgkmcnt(0)" ::"n"(N) : "memory");
}
__device__ __forceinline__ void bar() {
  asm volatile("" ::: "memory");
  __builtin_amdgcn_s_barrier();
  asm volatile("" ::: "memory");
}
__device__ __forceinline__ bf16x8 lds_frag(const char* p) { return *(const bf16x8*)p; }
__device__ __forceinline__ s16x4 lds_tr(const char* p) {
  return __builtin_amdgcn_ds_read_tr16_b64_v4i16((lds_s16x4_ptr)p);
}
__device__ __forceinline__ bf16x8 cat_frag(s16x4 a, s16x4 b) {
  s16x8 v = __builtin_shufflevector(a, b, 0, 1, 2, 3, 4, 5, 6, 7);
  return __builtin_bit_cast(bf16x8, v);
}
#define MFMA16(a, b, c) __builtin_amdgcn_mfma_f32_16x16x32_bf16((a), (b), (c), 0, 0, 0)

template <class Sc, int POS, int NM, int NR, int DL, class FM, class FR>
__device__ __forceinline__ void run_sched(FM& fm, FR& fr) {
  if constexpr (Sc::s[POS] != 0) {
    constexpr char c = Sc::s[POS];
    if constexpr (c == 'M') {
      fm(IC<NM>{}); SB0();
      run_sched<Sc, POS + 1, NM + 1, NR, DL>(fm, fr);
    } else {
      if constexpr (DL) { fr(IC<NR>{}); SB0(); }
      run_sched<Sc, POS + 1, NM, NR + 1, DL>(fm, fr);
    }
  }
}
template <int N> __device__ __forceinline__ void wait_vm() { asm volatile("s_waitcnt vmcnt(%0)" ::"n"(N) : "memory"); }
__device__ __forceinline__ void wait_lgkm0() { asm volatile("s_waitcnt lgkmcnt(0)" ::: "memory"); }

template <int NS, int P, int TI>
__device__ __forceinline__ void prod_tail() {
  if constexpr (TI < NS - 1) {
    wait_vm<P*(NS - 2 - TI)>();
    bar();
    prod_tail<NS, P, TI + 1>();
  }
}
constexpr int ramp_A(int j, int NS) {
  int a = 2;
  for (int i = 1; i <= j; ++i) { int lim = (i == 1) ? NS : (i - 1) + NS; a = (a + 2 < lim) ? a + 2 : lim; }
  return a;
}
constexpr int ramp_len(int NS) { int j = 1; while (ramp_A(j, NS) != ((j == 1) ? NS : (j - 1) + NS) || j < 2) ++j; return j; }
template <int NS, int P, int J, int R, class Issue>
__device__ __forceinline__ void prod_ramp(Issue& issue) {
  if constexpr (J < R) {
    constexpr int a0 = (J == 0) ? 0 : ramp_A(J - 1, NS), a1 = ramp_A(J, NS);
#pragma unroll
    for (int s = a0; s < a1; ++s) issue(s, s % NS);
    wait_vm<P*(a1 - (J + 1))>();
    bar();
    prod_ramp<NS, P, J + 1, R>(issue);
  }
}
template <int NS, int NK, int P, class Issue>
__device__ __forceinline__ void producer(Issue issue) {
  constexpr int R = ramp_len(NS);
  static_assert(R - 1 + NS <= NK, "ramp too long");
  prod_ramp<NS, P, 0, R>(issue);
#pragma unroll
  for (int s = ramp_A(R - 1, NS); s < R - 1 + NS; ++s) issue(s, s % NS);
  int wr = (R - 1) % NS;
#pragma nounroll
  for (int i = R - 1; i < NK - NS; ++i) {
    wait_vm<P*(NS - 2)>();
    bar();
    issue(i + NS, wr);
    wr = (wr + 1 == NS) ? 0 : wr + 1;
  }
  prod_tail<NS, P, 0>();
}
#define PFD 2
template <int NK, int PR, int PC, class IR, class ICC, class Mid>
__device__ __forceinline__ void producer_seam(IR issueR, ICC issueC, Mid mid) {
  constexpr int P = PR + PC;
  issueR(0, 0); issueR(1, 1);
  wait_vm<PR>(); bar();
  issueR(2, 2); issueC(2, 2); issueR(3, 3); issueC(3, 3);
  wait_vm<2 * P>(); bar();
  mid();
  constexpr int J0 = 2;
#pragma nounroll
  for (int j = J0; j <= NK - 3; ++j) {
    issueR(j + 2, (j + 2) & 3); issueC(j + 2, (j + 2) & 3);
    wait_vm<2 * P>(); bar();
  }
  wait_vm<P>(); bar();
  wait_vm<0>(); bar();
}
template <int NS, int NK, class Load0, class Body>
__device__ __forceinline__ void consumer(Load0 load0, Body body) {
  static_assert((NK & 1) == 0, "even number of stages");
  bar();
  load0();
  int rd = 1;
#pragma nounroll
  for (int i = 0; i < NK - 2; i += 2) {
    wait_lgkm0();
    bar();
    SB0();
    body(IC<0>{}, IC<1>{}, rd);
    SB0();
    rd = (rd + 1 == NS) ? 0 : rd + 1;
    wait_lgkm0();
    bar();
    SB0();
    body(IC<1>{}, IC<1>{}, rd);
    SB0();
    rd = (rd + 1 == NS) ? 0 : rd + 1;
  }
  wait_lgkm0();
  bar();
  SB0();
  body(IC<0>{}, IC<1>{}, rd);
  SB0();
  body(IC<1>{}, IC<0>{}, 0);
  SB0();
}
template <int NK, class Load0, class Half>
__device__ __forceinline__ void consumer_half(Load0 load0, Half half) {
  bar();
  load0();
#pragma nounroll
  for (int s = 0; s < NK - 1; ++s) {
    SB0();
    half(IC<0>{}, IC<1>{}, s & 3);
    SB0();
    wait_lgkm0();
    bar();
    SB0();
    half(IC<1>{}, IC<1>{}, (s + 1) & 3);
    SB0();
  }
  half(IC<0>{}, IC<1>{}, (NK - 1) & 3);
  SB0();
  half(IC<1>{}, IC<0>{}, 0);
  SB0();
}
typedef __attribute__((address_space(1))) unsigned gu32;
__device__ __forceinline__ void st16_wt(void* p, u32x4 v) {
  asm volatile("global_store_dwordx4 %0, %1, off sc1\n\ts_nop 1" ::"v"(p), "v"(v) : "memory");
}
__device__ __forceinline__ void st8_wt(void* p, f32x2 v) {
  asm volatile("global_store_dwordx2 %0, %1, off sc1\n\ts_nop 1" ::"v"(p), "v"(v) : "memory");
}
__device__ __forceinline__ void wait_count(char* ws, int idx, unsigned need) {
  gu32* cnt = (gu32*)(ws + WS_CNT + idx * 256);
#pragma nounroll
  for (unsigned spins = 0; spins < (1u << 22); ++spins) {
    if ((unsigned)__builtin_amdgcn_readfirstlane(__hip_atomic_load(cnt, __ATOMIC_RELAXED, __HIP_MEMORY_SCOPE_AGENT)) >= need) break;
    __builtin_amdgcn_s_sleep(1);
  }
  asm volatile("" ::: "memory");
}
__device__ __forceinline__ void add_count(char* ws, int idx) {
  __hip_atomic_fetch_add((gu32*)(ws + WS_CNT + idx * 256), 1u, __ATOMIC_RELAXED, __HIP_MEMORY_SCOPE_AGENT);
}
__device__ __forceinline__ int swap23(int r) { return (r & ~12) | ((r & 4) << 1) | ((r & 8) >> 1); }

#define WPF_KB 4
__global__ __launch_bounds__(256) void k1_ln_mod(const unsigned short* __restrict__ x,
                                                 const unsigned short* __restrict__ c,
                                                 const unsigned short* __restrict__ wmod,
                                                 const unsigned short* __restrict__ wre,
                                                 const unsigned short* __restrict__ wim,
                                                 const unsigned short* __restrict__ w2,
                                                 const unsigned short* __restrict__ dre,
                                                 const unsigned short* __restrict__ dim,
                                                 const unsigned short* __restrict__ w1,
                                                 const unsigned short* __restrict__ outp,
                                                 char* __restrict__ ws) {
  __shared__ __attribute__((aligned(16))) char smem[16 * 1296 + 256];
  const int tid = threadIdx.x, lane = tid & 63;
  const int bid = blockIdx.x;
  if (bid < 128) {
    const int xcd = bid & 7, i = bid >> 3;
    const int rg = xcd * 4 + (i >> 2), sub = i & 3;
    const int row0 = rg * 64 + sub * 16;
    const int r = tid >> 4, j = tid & 15;
    const u32x4* src = (const u32x4*)(x + (size_t)(row0 + r) * D_);
    u32x4 v[5];
#pragma unroll
    for (int q = 0; q < 5; ++q) v[q] = LD1(src + j + 16 * q);
    float f[40];
#pragma unroll
    for (int q = 0; q < 5; ++q)
#pragma unroll
      for (int e = 0; e < 4; ++e) { f[q * 8 + 2 * e] = lo16(v[q][e]); f[q * 8 + 2 * e + 1] = hi16(v[q][e]); }
    float s = 0.f;
#pragma unroll
    for (int e = 0; e < 40; ++e) s += f[e];
#pragma unroll
    for (int o = 1; o < 16; o <<= 1) s += __shfl_xor(s, o, 64);
    const float mean = s * (1.0f / D_);
    float s2 = 0.f;
#pragma unroll
    for (int e = 0; e < 40; ++e) { float d = f[e] - mean; s2 += d * d; }
#pragma unroll
    for (int o = 1; o < 16; o <<= 1) s2 += __shfl_xor(s2, o, 64);
    const float mb = bfr(mean);
    const float vb = bfr(s2 * (1.0f / D_));
    const float rden = 1.0f / bfr(sqrtf(bfr(vb + 1e-5f)));
#pragma unroll
    for (int q = 0; q < 5; ++q) {
      u32x4 o;
#pragma unroll
      for (int e = 0; e < 4; ++e) {
        float a = bfr(f[q * 8 + 2 * e] - mb) * rden;
        float b = bfr(f[q * 8 + 2 * e + 1] - mb) * rden;
        o[e] = pk2(a, b);
      }
      *(u32x4*)(smem + r * 1296 + (j + 16 * q) * 16) = o;
    }
    wait_lgkm0();
    bar();
    const int grp = tid >> 4, li = tid & 15, qq = li >> 2, pp = li & 3;
    char* ht = ws + WS_HT + (size_t)(row0 >> 4) * 2560;
#pragma unroll
    for (int it = 0; it < 5; ++it) {
      const int pr = it * 16 + grp;
      const int cblk = pr >> 1, half = pr & 1;
      const char* a0 = smem + (8 * half + qq) * 1296 + (16 * cblk + 4 * pp) * 2;
      s16x4 t0 = lds_tr(a0);
      s16x4 t1 = lds_tr(a0 + 4 * 1296);
      s16x8 o = __builtin_shufflevector(t0, t1, 0, 1, 2, 3, 4, 5, 6, 7);
      const int d = 16 * cblk + li;
      const int cb = d / 80, rr = d - cb * 80;
      *(s16x8*)(ht + (size_t)cb * (128 * 2560) + rr * 32 + half * 16) = o;
    }
    if (bid == 0 && tid < 96) *(u32x4*)(ws + WS_E + 2560 + tid * 16) = (u32x4){0u, 0u, 0u, 0u};
    if (bid == 1 && tid < 48) *(unsigned*)(ws + WS_CNT + tid * 256) = 0u;
  } else if (bid < 368) {
    const int g = bid - 128;
    const int cgp = (g & 7) * 30 + (g >> 3);
    const int l2 = tid & 1, rg = tid >> 1;
    u32x4 v[5];
    unsigned short cv16[5];
#pragma unroll
    for (int q = 0; q < 5; ++q) {
      const int k = q * 128 + rg;
      v[q] = *(const u32x4*)(wmod + (size_t)k * MODN + cgp * 16 + l2 * 8);
      cv16[q] = c[k];
    }
    float acc[8];
#pragma unroll
    for (int e = 0; e < 8; ++e) acc[e] = 0.f;
#pragma unroll
    for (int q = 0; q < 5; ++q) {
      const float cv = bf2f(cv16[q]);
      const float sv = bfr(cv / (1.0f + __expf(-cv)));
#pragma unroll
      for (int e = 0; e < 4; ++e) {
        acc[2 * e] += sv * lo16(v[q][e]);
        acc[2 * e + 1] += sv * hi16(v[q][e]);
      }
    }
#pragma unroll
    for (int o = 2; o < 64; o <<= 1)
#pragma unroll
      for (int e = 0; e < 8; ++e) acc[e] += __shfl_xor(acc[e], o, 64);
    float* red = (float*)smem;
    if (lane < 2) {
#pragma unroll
      for (int e = 0; e < 8; ++e) red[(tid >> 6) * 16 + lane * 8 + e] = acc[e];
    }
    wait_lgkm0();
    bar();
    if (tid < 16) {
      float m = bfr((red[tid] + red[16 + tid]) + (red[32 + tid] + red[48 + tid]));
      const int n = cgp * 16 + tid;
      const int part = n / D_, d = n - part * D_;
      if (part == 1 || part == 4) m = bfr(1.0f + m);
      ((float*)(ws + WS_MOD))[n] = m;
      if (part == 4) ((unsigned short*)(ws + WS_E))[d] = (unsigned short)(__float_as_uint(m) >> 16);
      if (part == 3) ((unsigned short*)(ws + WS_E))[D_ + d] = (unsigned short)(__float_as_uint(m) >> 16);
    }
  } else if (bid < 464) {
    const int k0 = (bid - 368) * 16;
    u32x4 v[5];
#pragma unroll
    for (int q = 0; q < 5; ++q) v[q] = LD1((const u32x4*)((const char*)w2 + (size_t)k0 * (D_ * 2) + (tid + 256 * q) * 16));
#pragma unroll
    for (int q = 0; q < 5; ++q) {
      const int id = tid + 256 * q;
      const int r = id / 80, ch = id - r * 80;
      const int cb = ch / 10, cc = ch - cb * 10;
      *(u32x4*)(ws + WS_W2T + ((size_t)cb * F_ + k0 + swap23(r)) * 160 + cc * 16) = v[q];
    }
  } else if (bid < 560) {
    const int c0 = (bid - 464) * 1280;
    u32x4 v[5];
#pragma unroll
    for (int q = 0; q < 5; ++q) v[q] = LD1((const u32x4*)((const char*)w1 + (size_t)(c0 + tid + 256 * q) * 16));
#pragma unroll
    for (int q = 0; q < 5; ++q) {
      const int id = c0 + tid + 256 * q;
      const int k = id / 192, ch = id - k * 192;
      const int cb = ch / 12, cc = ch - cb * 12;
      const int r = swap23(k);
      *(u32x4*)(ws + WS_W1T + ((size_t)cb * D_ + r) * 192 + (cc ^ (((r >> 2) & 1) << 1)) * 16) = v[q];
    }
  } else if (bid < 640) {
    const int m = bid >= 600, c0 = (bid - 560 - 40 * m) * 1280;
    u32x4 v[5];
#pragma unroll
    for (int q = 0; q < 5; ++q) v[q] = LD1((const u32x4*)((const char*)(m ? dim : dre) + (size_t)(c0 + tid + 256 * q) * 16));
#pragma unroll
    for (int q = 0; q < 5; ++q) {
      const int id = c0 + tid + 256 * q;
      const int e = id / 80, ch = id - e * 80;
      const int cb = e / 80, row = e - cb * 80;
      *(u32x4*)(ws + WS_WDT + ((size_t)(cb * 20 + m * 10 + (ch >> 3)) * 80 + row) * 128 + ((ch & 7) ^ ((row >> 1) & 7)) * 16) = v[q];
    }
  } else if (bid >= 640 + 64 * WPF_KB) {
    const int t = ((bid - 640 - 64 * WPF_KB) >> 3) * 256 + tid;
    const char* a;
    if (t < 384) a = ws + (size_t)t * 65536;
    else if (t < 512) a = (const char*)wre + (size_t)(t - 384) * 65536;
    else if (t < 640) a = (const char*)wim + (size_t)(t - 512) * 65536;
    else if (t < 680) a = (const char*)x + (size_t)(t - 640) * 65536;
    else if (t < 720) a = (const char*)outp + (size_t)(t - 680) * 65536;
    else a = ws;
    unsigned v;
    asm volatile("global_load_dword %0, %1, off" : "=v"(v) : "v"(a) : "memory");
    wait_vm<0>();
    asm volatile("" ::"v"(v));
  } else {
    const int pw = (bid - 640) * 4 + __builtin_amdgcn_readfirstlane(tid >> 6);
    constexpr int RPW = 16 / WPF_KB;
    const int row0 = pw * RPW;
    const char* src = (const char*)(row0 < 2048 ? wre : wim) + (size_t)(row0 & 2047) * 4096;
    u32x4 sink[16];
#pragma unroll
    for (int i = 0; i < RPW; ++i)
#pragma unroll
      for (int q = 0; q < WPF_KB; ++q)
        asm volatile("global_load_dwordx4 %0, %1, %2 offset:%3" : "=v"(sink[WPF_KB * i + q]) : "v"(lane * 16), "s"(src + i * 4096), "n"(q * 1024) : "memory");
    wait_vm<0>();
#pragma unroll
    for (int i = 0; i < 16; ++i) asm volatile("" ::"v"(sink[i]));
  }
}

__device__ __forceinline__ void dma_res_tile(const char* base  , unsigned lds, int w, int lane) {
#pragma unroll
  for (int j = 0; j < 3; ++j) {
    int L = 160 * w + 64 * j + lane;
    if (L > 639) L = 639;
    const int r = L / 10, ch = L - r * 10;
    if (j < 2) dma16(r * (D_ * 2) + ch * 16, base, lds + (160 * w + 64 * j) * 16);
    else dma16h(r * (D_ * 2) + ch * 16, base, lds + (160 * w + 128) * 16);
  }
}
template <int N>
__device__ __forceinline__ void l2_warm(const char* base, unsigned dummy_lds, int lane) {
#pragma unroll
  for (int i = 0; i < N; ++i) dma16(lane * 16, base + i * 1024, dummy_lds);
}

struct SchedP2 { static constexpr char s[] = "MRMRMMRMRMMRMRMMRMRMMRMMMMMMMMMMM"; };
struct SchedP3 { static constexpr char s[] = "RRMRMRRMRMRRMRMRRMRMMM"; };
struct SchedP4 { static constexpr char s[] = "MRMRMRMMRMRMRMMRMRMMRMRMRMMRMRMRMMRMRMMMMMMMMM"; };
constexpr int cnt_ch(const char* s, char c) { int n = 0; for (; *s; ++s) n += (*s == c); return n; }
static_assert(cnt_ch(SchedP2::s, 'M') == 24 && cnt_ch(SchedP2::s, 'R') == 9, "");
static_assert(cnt_ch(SchedP3::s, 'M') == 10 && cnt_ch(SchedP3::s, 'R') == 12, "");
static_assert(cnt_ch(SchedP4::s, 'M') == 30 && cnt_ch(SchedP4::s, 'R') == 16, "");
constexpr int L_RES = 147456, L_MOD = 159744, L_DUM = L_MOD + 2048, L_TOTAL = L_DUM + 1024;
#define P2NS 5
constexpr int P2_NS = P2NS, P2_STAGE = 26624, P2_X = 2 * P2_STAGE, P2_STG = P2_X + 49152;
constexpr int P3_STAGE = 36864;
constexpr int P4_STAGE = 28672, P4_E = 4 * P4_STAGE, P4_ST = P4_E + 4096, P4_RS = P4_ST + 8192, P4_STG = 2 * P4_STAGE;

__global__ __launch_bounds__(512, 2) void mega(const unsigned short* __restrict__ wre,
                                               const unsigned short* __restrict__ wim,
                                               const unsigned short* __restrict__ x,
                                               char* __restrict__ ws,
                                               unsigned short* __restrict__ out) {
  STAMP_DECL
  STAMP(0);
  extern __shared__ __attribute__((aligned(16))) char smem[];
  const int tid = threadIdx.x, lane = tid & 63;
  const int wv = __builtin_amdgcn_readfirstlane(tid >> 6);
  const int w = wv & 3;
  const bool prod = wv < 4;
  const int bid = blockIdx.x;
  const int xcd = bid & 7, bi = bid >> 3;
  const int cb = bi & 7, rsel = bi >> 3;
  const int g = lane >> 4, l15 = lane & 15;
  const unsigned lds0 = lds_off(smem);
  const float* mod = (const float*)(smem + L_MOD);
  const int t3 = (xcd * 4 + rsel) * 64;
  const int grp3 = xcd * 2 + (rsel >> 1);

  {
    const int isim = rsel >> 1;
    const int grp2 = xcd * 2 + (rsel & 1);
    const int t0 = grp2 * 128;
    if (prod) {
      const char* rbase = (const char*)(isim ? wim : wre) + (size_t)t0 * (S_ * 2);
      const char* cbase = ws + WS_HT + (size_t)cb * 32 * 10240;
      unsigned roff[4];
#pragma unroll
      for (int j = 0; j < 4; ++j) {
        const int row = 32 * w + 8 * j + (lane >> 3);
        roff[j] = row * (S_ * 2) + 16 * ((lane & 7) ^ ((row >> 1) & 7));
      }
      const unsigned coff = (20 * w) * 128 + lane * 16;
      producer<P2_NS, 32, 7>([&](int ks, int buf) {
        const unsigned lb = lds0 + buf * P2_STAGE;
        {
#pragma unroll
          for (int j = 0; j < 4; ++j) dma16(roff[j], rbase + ks * 128, lb + (32 * w + 8 * j) * 128);
        }
        dma16(coff, cbase + ks * 10240, lb + 16384 + (20 * w) * 128);
        dma16(coff, cbase + ks * 10240 + 1024, lb + 16384 + (20 * w + 8) * 128);
        dma16h(coff, cbase + ks * 10240 + 2048, lb + 16384 + (20 * w + 16) * 128);
      });
      bar();
    } else {
      if (w < 2) {
        int L = 64 * w + lane; if (L > 99) L = 99;
        const int vec = L / 20, part = vec + (vec >= 3);
        dma16(part * 2560 + cb * 320 + (L - vec * 20) * 16, ws + WS_MOD, lds0 + L_MOD + w * 1024);
      }
      dma_res_tile((const char*)x + (size_t)t3 * (D_ * 2) + cb * 160, lds0 + L_RES, w, lane);
      const int wm = w & 1, wk = w >> 1;
      const int fsw = ((4 * wk + g) ^ (l15 >> 1)) * 16;
      const int ra = (64 * wm + l15) * 128 + fsw;
      const int ca_ = 16384 + (2 * wk + (g >> 1)) * 2560 + l15 * 32 + (g & 1) * 16;
      bf16x8 rf[2][4], cf[2][5];
      f32x4 acc[6][4];
#pragma unroll
      for (int j = 0; j < 6; ++j)
#pragma unroll
        for (int i = 0; i < 4; ++i) acc[j][i] = (f32x4){0.f, 0.f, 0.f, 0.f};
      const s16x8 ones_s = {0x3F80, 0x3F80, 0x3F80, 0x3F80, 0x3F80, 0x3F80, 0x3F80, 0x3F80};
      const bf16x8 ones = __builtin_bit_cast(bf16x8, ones_s);
      auto ldf = [&](auto par, auto ic, int buf) {
        constexpr int PP = decltype(par)::value, n = decltype(ic)::value;
        const char* lb = smem + buf * P2_STAGE;
        if constexpr (n == 0) rf[PP][0] = lds_frag(lb + ra);
        else if constexpr (n < 6) cf[PP][n - 1] = lds_frag(lb + ca_ + (n - 1) * 512);
        else rf[PP][n - 5] = lds_frag(lb + ra + (n - 5) * 2048);
      };
      auto body = [&](auto par, auto dl, int rd) {
        constexpr int PP = decltype(par)::value, DL = decltype(dl)::value;
        auto fm = [&](auto mc) {
          constexpr int m = decltype(mc)::value, i = m / 6, j = m % 6;
          if constexpr (j < 5) acc[j][i] = MFMA16(cf[PP][j], rf[PP][i], acc[j][i]);
          else acc[5][i] = MFMA16(ones, rf[PP][i], acc[5][i]);
        };
        auto fr = [&](auto rc) { ldf(IC<PP ^ 1>{}, rc, rd); };
        run_sched<SchedP2, 0, 0, 0, DL>(fm, fr);
      };
      auto load0 = [&] { static_for<9>([&](auto ic) { ldf(IC<0>{}, ic, 0); }); };
      consumer<P2_NS, 32>(load0, body);
      STAMP(1);
      auto finish = [&](auto keepc) {
        constexpr int KP = decltype(keepc)::value;
        char* dst = smem + P2_X + w * 12288 + lane * 16;
#pragma unroll
        for (int j = 0; j < 5; ++j) {
          *(f32x4*)(dst + (2 * j) * 1024) = acc[j][2 * (KP ^ 1)];
          *(f32x4*)(dst + (2 * j + 1) * 1024) = acc[j][2 * (KP ^ 1) + 1];
        }
        *(f32x2*)(dst + 10240) = (f32x2){acc[5][2 * (KP ^ 1)][0], acc[5][2 * (KP ^ 1) + 1][0]};
        wait_vm<0>();
        wait_lgkm0();
        bar();
        const char* srcp = smem + P2_X + (w ^ 2) * 12288 + lane * 16;
        const float sgn = isim ? -1.0f : 1.0f;
        const f32x2 rp = *(const f32x2*)(srcp + 10240);
        const float rs0 = (acc[5][2 * KP][0] + rp[0]) * sgn, rs1 = (acc[5][2 * KP + 1][0] + rp[1]) * sgn;
        char* stg = smem + P2_STG + (64 * wm + 32 * KP + l15) * 176 + g * 8;
#pragma unroll
        for (int j = 0; j < 5; ++j) {
          const f32x4 sa = *(const f32x4*)(mod + 16 * j + 4 * g);
          const f32x4 c1 = *(const f32x4*)(mod + 80 + 16 * j + 4 * g) * sgn;
          const f32x4 v0 = c1 * (acc[j][2 * KP] + *(const f32x4*)(srcp + (2 * j) * 1024)) + sa * rs0;
          const f32x4 v1 = c1 * (acc[j][2 * KP + 1] + *(const f32x4*)(srcp + (2 * j + 1) * 1024)) + sa * rs1;
          *(u32x2*)(stg + j * 32) = (u32x2){pk2(v0[0], v0[1]), pk2(v0[2], v0[3])};
          *(u32x2*)(stg + 16 * 176 + j * 32) = (u32x2){pk2(v1[0], v1[1]), pk2(v1[2], v1[3])};
        }
      };
      if (wk == 0) finish(IC<0>{}); else finish(IC<1>{});
    }
    wait_lgkm0();
    bar();
    {
      char* pq = ws + WS_PQ + ((size_t)(grp2 * 2) * 80 + isim * 40 + cb * 5) * 2048;
      for (int id = tid; id < 1280; id += 512) {
        const int rbh = id >= 640, r_ = id - 640 * rbh;
        const int gq = r_ >> 7, row = rbh * 64 + ((r_ & 127) >> 1), c = 2 * gq + (r_ & 1);
        st16_wt(pq + (size_t)rbh * (80 * 2048) + r_ * 16, *(const u32x4*)(smem + P2_STG + row * 176 + c * 16));
      }
    }
    wait_lgkm0();
    bar();
    STAMP(2);
    const char* wdt = ws + WS_WDT + (size_t)cb * 204800;
    if (prod) {
#pragma unroll
      for (int s = 0; s < PFD; ++s)
#pragma unroll
        for (int j = 0; j < 5; ++j)
          dma16(lane * 16, wdt + s * 20480 + w * 5120 + j * 1024, lds0 + s * P3_STAGE + 16384 + w * 5120 + j * 1024);
      wait_vm<5 * PFD>();
    } else {
      wait_vm<0>();
    }
    bar();
    STAMP(3);
    if (wv == 4) {
      if (lane == 0) add_count(ws, grp2);
      wait_count(ws, grp3, 16);
    }
    bar();
  }

  {
    const char* wdt = ws + WS_WDT + (size_t)cb * 204800;
    if (prod) {
      const char* rbase = ws + WS_PQ + (size_t)(t3 >> 6) * (80 * 2048) + w * 4096;
      STAMP(4);
      producer_seam<10, 4, 5>([&](int ks, int buf) {
#pragma unroll
        for (int j = 0; j < 4; ++j) dma16s(lane * 16, rbase + ks * 16384 + j * 1024, lds0 + buf * P3_STAGE + w * 4096 + j * 1024);
      }, [&](int ks, int buf) {
#pragma unroll
        for (int j = 0; j < 5; ++j)
          dma16(lane * 16, wdt + ks * 20480 + w * 5120 + j * 1024, lds0 + buf * P3_STAGE + 16384 + w * 5120 + j * 1024);
      }, [] {});
    } else {
      const int sw0 = (g ^ (l15 >> 1)) * 16, sw1 = ((4 + g) ^ (l15 >> 1)) * 16;
      const int ra = (g >> 1) * 2048 + (16 * w + l15) * 32 + (g & 1) * 16, ca_ = 16384 + l15 * 128;
      bf16x8 rf[2][2], cf[2][2][5];
      f32x4 acc[5];
#pragma unroll
      for (int j = 0; j < 5; ++j) acc[j] = (f32x4){0.f, 0.f, 0.f, 0.f};
      auto ldf = [&](auto hc, auto ic, int buf) {
        constexpr int H = decltype(hc)::value, n = decltype(ic)::value, kk = n / 6, m = n % 6;
        const char* lb = smem + buf * P3_STAGE;
        if constexpr (m == 0) rf[H][kk] = lds_frag(lb + ra + H * 8192 + kk * 4096);
        else cf[H][kk][m - 1] = lds_frag(lb + (kk ? sw1 : sw0) + ca_ + H * 10240 + (m - 1) * 2048);
      };
      auto half = [&](auto par, auto dl, int buf) {
        constexpr int PP = decltype(par)::value, DL = decltype(dl)::value;
        auto fm = [&](auto mc) {
          constexpr int m = decltype(mc)::value, kk = m / 5, j = m % 5;
          acc[j] = MFMA16(cf[PP][kk][j], rf[PP][kk], acc[j]);
        };
        auto fr = [&](auto rc) { ldf(IC<PP ^ 1>{}, rc, buf); };
        run_sched<SchedP3, 0, 0, 0, DL>(fm, fr);
      };
      auto load0 = [&] { STAMP(4); static_for<12>([&](auto ic) { ldf(IC<0>{}, ic, 0); }); };
      consumer_half<10>(load0, half);
      STAMP(5);
      char* res = smem + L_RES + (16 * w + l15) * 160 + g * 8;
#pragma unroll
      for (int j = 0; j < 5; ++j) {
        const u32x2 xr = *(const u32x2*)(res + j * 32);
        const f32x4 ga = *(const f32x4*)(mod + 160 + 16 * j + 4 * g);
        const f32x4 y = acc[j];
        *(u32x2*)(res + j * 32) = (u32x2){pk2(lo16(xr[0]) + ga[0] * y[0], hi16(xr[0]) + ga[1] * y[1]),
                                          pk2(lo16(xr[1]) + ga[2] * y[2], hi16(xr[1]) + ga[3] * y[3])};
      }
    }
    wait_lgkm0();
    bar();
    {
      const int row = tid >> 3, c0 = tid & 7;
      char* xc = ws + WS_XC + ((size_t)(t3 >> 6) * 40 + cb * 5) * 2048 + row * 32;
      float xf[2][8];
      float s = 0.f;
#pragma unroll
      for (int it = 0; it < 2; ++it) {
        const int c = c0 + 8 * it;
        if (c < 10) {
          const u32x4 xr = *(const u32x4*)(smem + L_RES + row * 160 + c * 16);
          const f32x4 c10 = *(const f32x4*)(mod + 240 + c * 8), c11 = *(const f32x4*)(mod + 244 + c * 8);
#pragma unroll
          for (int e = 0; e < 4; ++e) { xf[it][2 * e] = lo16(xr[e]); xf[it][2 * e + 1] = hi16(xr[e]); s += xf[it][2 * e] + xf[it][2 * e + 1]; }
          const u32x4 oc = {pk2(xf[it][0] * c10[0], xf[it][1] * c10[1]), pk2(xf[it][2] * c10[2], xf[it][3] * c10[3]),
                            pk2(xf[it][4] * c11[0], xf[it][5] * c11[1]), pk2(xf[it][6] * c11[2], xf[it][7] * c11[3])};
          st16_wt(xc + (c >> 1) * 2048 + (c & 1) * 16, oc);
        }
      }
#pragma unroll
      for (int o = 1; o < 8; o <<= 1) s += __shfl_xor(s, o, 64);
      const float ml = s * (1.0f / 80.0f);
      float m2 = 0.f;
#pragma unroll
      for (int e = 0; e < 8; ++e) { float d = xf[0][e] - ml; m2 += d * d; }
      if (c0 < 2) {
#pragma unroll
        for (int e = 0; e < 8; ++e) { float d = xf[1][e] - ml; m2 += d * d; }
      }
#pragma unroll
      for (int o = 1; o < 8; o <<= 1) m2 += __shfl_xor(m2, o, 64);
      if (c0 == 0) st8_wt(ws + WS_ST + ((size_t)(t3 >> 6) * 8 + cb) * 512 + row * 8, (f32x2){s, m2});
    }
    STAMP(6);
    const char* w1t = ws + WS_W1T + (size_t)(bi & 15) * 122880;
    if (prod) {
      dma16(w * 1024 + lane * 16, ws + WS_E, lds0 + P4_E + w * 1024);
#pragma unroll
      for (int s = 0; s < PFD; ++s)
#pragma unroll
        for (int j = 0; j < 3; ++j)
          dma16(lane * 16, w1t + s * 12288 + w * 3072 + j * 1024, lds0 + s * P4_STAGE + 16384 + w * 3072 + j * 1024);
      wait_vm<1 + 3 * PFD>();
    } else {
      wait_vm<0>();
    }
    bar();
    STAMP(7);
    if (wv == 4) {
      if (lane == 0) add_count(ws, 16 + grp3);
      wait_count(ws, 16 + xcd * 2 + (bi >> 4), 16);
    }
    bar();
  }

  {
    const int cb4 = bi & 15, grp4 = xcd * 2 + (bi >> 4);
    const int t0 = grp4 * 128;
    const char* w1t = ws + WS_W1T + (size_t)cb4 * 122880;
    if (prod) {
      const char* rbase = ws + WS_XC + (size_t)(grp4 * 2 + (w >> 1)) * (40 * 2048) + (w & 1) * 4096;
      STAMP(8);
      dma16s(w * 2048 + lane * 16, ws + WS_ST + (size_t)t0 * 64, lds0 + P4_ST + w * 2048);
      dma16s(w * 2048 + 1024 + lane * 16, ws + WS_ST + (size_t)t0 * 64, lds0 + P4_ST + w * 2048 + 1024);
      producer_seam<10, 4, 3>([&](int ks, int buf) {
#pragma unroll
        for (int j = 0; j < 4; ++j) dma16s(lane * 16, rbase + ks * 8192 + j * 1024, lds0 + buf * P4_STAGE + w * 4096 + j * 1024);
      }, [&](int ks, int buf) {
#pragma unroll
        for (int j = 0; j < 3; ++j)
          dma16(lane * 16, w1t + ks * 12288 + w * 3072 + j * 1024, lds0 + buf * P4_STAGE + 16384 + w * 3072 + j * 1024);
      }, [&] {
        if (tid < 128) {
          const char* sp = smem + P4_ST + (tid >> 6) * 4096 + (tid & 63) * 8;
          f32x2 pp[8];
#pragma unroll
          for (int e = 0; e < 8; ++e) pp[e] = *(const f32x2*)(sp + e * 512);
          const float mean = (((pp[0][0] + pp[1][0]) + (pp[2][0] + pp[3][0])) + ((pp[4][0] + pp[5][0]) + (pp[6][0] + pp[7][0]))) * (1.0f / D_);
          float m2 = ((pp[0][1] + pp[1][1]) + (pp[2][1] + pp[3][1])) + ((pp[4][1] + pp[5][1]) + (pp[6][1] + pp[7][1]));
          float dd = 0.f;
#pragma unroll
          for (int e = 0; e < 8; ++e) { const float d = pp[e][0] * (1.0f / 80.0f) - mean; dd += d * d; }
          m2 += 80.0f * dd;
          const float rden = 1.0f / bfr(sqrtf(bfr(bfr(m2 * (1.0f / D_)) + 1e-5f)));
          *(f32x2*)(smem + P4_RS + tid * 8) = (f32x2){bfr(mean), rden};
        }
        wait_lgkm0();
      });
    } else {
      const int q = l15 >> 2, p4 = lane & 3;
      const int wm = w & 1, wn = w >> 1;
      const int rpos = 16 * (g >> 1) + 4 * (g & 1) + q;
      const int xs = (rpos >> 2) & 1;
      int cadr[3];
#pragma unroll
      for (int j = 0; j < 3; ++j) {
        const int jj = 3 * wn + j;
        cadr[j] = 16384 + rpos * 192 + (2 * (jj ^ xs) + (p4 >> 1)) * 16 + (p4 & 1) * 8;
      }
      const int ra = wm * 8192 + (g >> 1) * 2048 + l15 * 32 + (g & 1) * 16;
      int ea = P4_E + (l15 < 2 ? l15 : 2) * 1280 + g * 16;
      bf16x8 rf[2][2][5], cf[2][2][3];
      f32x4 acc[3][5];
#pragma unroll
      for (int j = 0; j < 3; ++j)
#pragma unroll
        for (int i = 0; i < 5; ++i) acc[j][i] = (f32x4){0.f, 0.f, 0.f, 0.f};
      auto ldf = [&](auto par, auto ic, int buf) {
        constexpr int PP = decltype(par)::value, n = decltype(ic)::value, kk = n / 8, m = n % 8;
        const char* lb = smem + buf * P4_STAGE;
        if constexpr (m == 0) rf[PP][kk][0] = lds_frag(lb + ra + kk * 4096);
        else if constexpr (m < 4) cf[PP][kk][m - 1] = cat_frag(lds_tr(lb + cadr[m - 1] + kk * 6144), lds_tr(lb + cadr[m - 1] + kk * 6144 + 1536));
        else if constexpr (m < 7) rf[PP][kk][m - 3] = lds_frag(lb + ra + kk * 4096 + (m - 3) * 512);
        else {
          rf[PP][kk][4] = lds_frag(smem + ea + kk * 64);
          if constexpr (kk == 1) ea += 128;
        }
      };
      auto body = [&](auto par, auto dl, int rd) {
        constexpr int PP = decltype(par)::value, DL = decltype(dl)::value;
        auto fm = [&](auto mc) {
          constexpr int m = decltype(mc)::value, kk = m / 15, i = (m % 15) / 3, j = m % 3;
          acc[j][i] = MFMA16(cf[PP][kk][j], rf[PP][kk][i], acc[j][i]);
        };
        auto fr = [&](auto rc) { ldf(IC<PP ^ 1>{}, rc, rd); };
        run_sched<SchedP4, 0, 0, 0, DL>(fm, fr);
      };
      auto load0 = [&] { STAMP(8); static_for<16>([&](auto ic) { ldf(IC<0>{}, ic, 0); }); };
      consumer<4, 10>(load0, body);
      STAMP(9);
      f32x4 u[3], v[3];
#pragma unroll
      for (int j = 0; j < 3; ++j)
#pragma unroll
        for (int e = 0; e < 4; ++e) {
          const int a = __float_as_int(acc[j][4][e]);
          u[j][e] = __int_as_float(__builtin_amdgcn_update_dpp(0, a, 0x150, 0xf, 0xf, false));
          v[j][e] = __int_as_float(__builtin_amdgcn_update_dpp(0, a, 0x151, 0xf, 0xf, false));
        }
      char* stg = smem + P4_STG + (64 * wm + l15) * 208 + (48 * wn + 4 * g) * 2;
#pragma unroll
      for (int i = 0; i < 4; ++i) {
        const f32x2 rs = *(const f32x2*)(smem + P4_RS + (64 * wm + 16 * i + l15) * 8);
#pragma unroll
        for (int j = 0; j < 3; ++j) {
          const f32x4 hv = (acc[j][i] - u[j] * rs[0]) * rs[1] + v[j];
          *(u32x2*)(stg + i * 16 * 208 + j * 32) =
              (u32x2){pk2(fmaxf(hv[0], 0.f), fmaxf(hv[1], 0.f)), pk2(fmaxf(hv[2], 0.f), fmaxf(hv[3], 0.f))};
        }
      }
    }
    wait_lgkm0();
    bar();
    {
      char* h1 = ws + WS_H1 + ((size_t)(grp4 * 2) * 96 + cb4 * 6) * 2048;
#pragma unroll
      for (int it = 0; it < 3; ++it) {
        const int id = tid + 512 * it;
        const int rbh = id >= 768, r_ = id - 768 * rbh;
        const int gq = r_ >> 7, row = rbh * 64 + ((r_ & 127) >> 1), c = 2 * gq + (r_ & 1);
        st16_wt(h1 + (size_t)rbh * (96 * 2048) + r_ * 16, *(const u32x4*)(smem + P4_STG + row * 208 + c * 16));
      }
    }
    wait_lgkm0();
    bar();
    STAMP(10);
    const char* w2t = ws + WS_W2T + (size_t)cb * F_ * 160;
    if (prod) {
#pragma unroll
      for (int s = 0; s < PFD; ++s)
#pragma unroll
        for (int j = 0; j < 5; ++j)
          dma16(lane * 16, w2t + s * 20480 + w * 5120 + j * 1024, lds0 + s * P3_STAGE + 16384 + w * 5120 + j * 1024);
      wait_vm<5 * PFD>();
    } else {
      wait_vm<0>();
    }
    bar();
    STAMP(11);
    if (wv == 4) {
      if (lane == 0) add_count(ws, 32 + grp4);
      wait_count(ws, 32 + grp3, 16);
    }
    bar();
  }

  {
    const char* w2t = ws + WS_W2T + (size_t)cb * F_ * 160;
    if (prod) {
      const char* rbase = ws + WS_H1 + (size_t)(t3 >> 6) * (96 * 2048) + w * 4096;
      STAMP(12);
      producer_seam<12, 4, 5>([&](int ks, int buf) {
#pragma unroll
        for (int j = 0; j < 4; ++j) dma16s(lane * 16, rbase + ks * 16384 + j * 1024, lds0 + buf * P3_STAGE + w * 4096 + j * 1024);
      }, [&](int ks, int buf) {
#pragma unroll
        for (int j = 0; j < 5; ++j)
          dma16(lane * 16, w2t + ks * 20480 + w * 5120 + j * 1024, lds0 + buf * P3_STAGE + 16384 + w * 5120 + j * 1024);
      }, [] {});
    } else {
      const int q = l15 >> 2, p4 = lane & 3;
      const int ra = (g >> 1) * 2048 + (16 * w + l15) * 32 + (g & 1) * 16;
      const int ca_ = 16384 + (16 * (g >> 1) + 4 * (g & 1) + q) * 160 + p4 * 8;
      bf16x8 rf[2][2], cf[2][2][5];
      f32x4 acc[5];
#pragma unroll
      for (int j = 0; j < 5; ++j) acc[j] = (f32x4){0.f, 0.f, 0.f, 0.f};
      auto ldf = [&](auto hc, auto ic, int buf) {
        constexpr int H = decltype(hc)::value, n = decltype(ic)::value, kk = n / 6, m = n % 6;
        const char* lb = smem + buf * P3_STAGE;
        if constexpr (m == 0) rf[H][kk] = lds_frag(lb + ra + H * 8192 + kk * 4096);
        else cf[H][kk][m - 1] = cat_frag(lds_tr(lb + ca_ + H * 10240 + kk * 5120 + (m - 1) * 32),
                                         lds_tr(lb + ca_ + H * 10240 + kk * 5120 + (m - 1) * 32 + 1280));
      };
      auto half = [&](auto par, auto dl, int buf) {
        constexpr int PP = decltype(par)::value, DL = decltype(dl)::value;
        auto fm = [&](auto mc) {
          constexpr int m = decltype(mc)::value, kk = m / 5, j = m % 5;
          acc[j] = MFMA16(cf[PP][kk][j], rf[PP][kk], acc[j]);
        };
        auto fr = [&](auto rc) { ldf(IC<PP ^ 1>{}, rc, buf); };
        run_sched<SchedP3, 0, 0, 0, DL>(fm, fr);
      };
      auto load0 = [&] { STAMP(12); static_for<12>([&](auto ic) { ldf(IC<0>{}, ic, 0); }); };
      consumer_half<12>(load0, half);
      STAMP(13);
      char* res = smem + L_RES + (16 * w + l15) * 160 + g * 8;
#pragma unroll
      for (int j = 0; j < 5; ++j) {
        const u32x2 xr = *(const u32x2*)(res + j * 32);
        const f32x4 gb = *(const f32x4*)(mod + 320 + 16 * j + 4 * g);
        const f32x4 y = acc[j];
        *(u32x2*)(res + j * 32) = (u32x2){pk2(lo16(xr[0]) + gb[0] * y[0], hi16(xr[0]) + gb[1] * y[1]),
                                          pk2(lo16(xr[1]) + gb[2] * y[2], hi16(xr[1]) + gb[3] * y[3])};
      }
      wait_vm<0>();
    }
    wait_lgkm0();
    bar();
    for (int id = tid; id < 640; id += 512) {
      const int row = id / 10, c = id - row * 10;
      *(u32x4*)((char*)out + (size_t)(t3 + row) * (D_ * 2) + cb * 160 + c * 16) = *(const u32x4*)(smem + L_RES + row * 160 + c * 16);
    }
  }
  STAMP(14);
  STAMP_OUT();
}

extern "C" void kernel_launch(void* const* d_in, const int* in_sizes, int n_in,
                              void* d_out, int out_size, void* d_ws, size_t ws_size,
                              hipStream_t stream) {
  const unsigned short* x    = (const unsigned short*)d_in[0];
  const unsigned short* wsre = (const unsigned short*)d_in[1];
  const unsigned short* wsim = (const unsigned short*)d_in[2];
  const unsigned short* wdre = (const unsigned short*)d_in[3];
  const unsigned short* wdim = (const unsigned short*)d_in[4];
  const unsigned short* w1   = (const unsigned short*)d_in[6];
  const unsigned short* w2   = (const unsigned short*)d_in[7];
  const unsigned short* c    = (const unsigned short*)d_in[8];
  const unsigned short* wmod = (const unsigned short*)d_in[9];
  char* ws = (char*)d_ws;
  unsigned short* out = (unsigned short*)d_out;
  (void)hipFuncSetAttribute((const void*)mega, hipFuncAttributeMaxDynamicSharedMemorySize, L_TOTAL);
  k1_ln_mod<<<640 + 64 * WPF_KB + 24, 256, 0, stream>>>(x, c, wmod, wsre, wsim, w2, wdre, wdim, w1, out, ws);
  mega<<<256, 512, L_TOTAL, stream>>>(wsre, wsim, x, ws, out);
}
```

```cpp
#include <hip/hip_runtime.h>
#include <hip/hip_bf16.h>
#include <stdint.h>

typedef __attribute__((ext_vector_type(8))) __bf16 bf16x8;
typedef __attribute__((ext_vector_type(4))) __bf16 bf16x4;
typedef __attribute__((ext_vector_type(2))) __bf16 bf16x2;
typedef __attribute__((ext_vector_type(4))) float f32x4;
typedef __attribute__((ext_vector_type(2))) float f32x2;
typedef __attribute__((ext_vector_type(4))) short s16x4;
typedef __attribute__((ext_vector_type(8))) short s16x8;
typedef __attribute__((ext_vector_type(4))) unsigned u32x4;
typedef __attribute__((ext_vector_type(2))) unsigned u32x2;
typedef __attribute__((address_space(3))) s16x4* lds_s16x4_ptr;
typedef __attribute__((address_space(3))) void* lds_vptr;

constexpr int S_ = 2048, D_ = 640, F_ = 1536, MODN = 3840;

constexpr size_t WS_MOD = 0;
constexpr size_t WS_E   = 16384;
constexpr size_t WS_CNT = 20480;
constexpr size_t WS_ST  = 32768;
constexpr size_t WS_HT  = WS_ST + (size_t)S_ * 64;
constexpr size_t WS_PQ  = WS_HT + (size_t)D_ * S_ * 2;
constexpr size_t WS_XC  = WS_PQ + (size_t)S_ * 1280 * 2;
constexpr size_t WS_H1  = WS_XC + (size_t)S_ * D_ * 2;
constexpr size_t WS_W2T = WS_H1 + (size_t)S_ * F_ * 2;
constexpr size_t WS_W1T = WS_W2T + (size_t)F_ * D_ * 2;
constexpr size_t WS_WDT = WS_W1T + (size_t)F_ * D_ * 2;

#define STAMP_DECL
#define STAMP(i)
#define STAMP_OUT()
template <int V> struct IC { static constexpr int value = V; };
template <int N, int I = 0, class F> __device__ __forceinline__ void static_for(F f) {
  if constexpr (I < N) { f(IC<I>{}); static_for<N, I + 1>(f); }
}

__device__ __forceinline__ float bf2f(unsigned short u) { return __uint_as_float(((unsigned)u) << 16); }
__device__ __forceinline__ float bfr(float f) { return (float)(__bf16)f; }
__device__ __forceinline__ unsigned pk2(float a, float b) {
  bf16x2 v = __builtin_convertvector((f32x2){a, b}, bf16x2);
  return __builtin_bit_cast(unsigned, v);
}
__device__ __forceinline__ float lo16(unsigned u) { return __uint_as_float(u << 16); }
__device__ __forceinline__ float hi16(unsigned u) { return __uint_as_float(u & 0xffff0000u); }

__device__ __forceinline__ void dma16(unsigned voff, const char* sbase, unsigned lds) {
  asm volatile("s_mov_b32 m0, %2\n\ts_nop 0\n\tglobal_load_lds_dwordx4 %0, %1"
               ::"v"(voff), "s"(sbase), "s"(lds) : "memory");
}
__device__ __forceinline__ void dma16nt(unsigned voff, const char* sbase, unsigned lds) {
  asm volatile("s_mov_b32 m0, %2\n\ts_nop 0\n\tglobal_load_lds_dwordx4 %0, %1 nt"
               ::"v"(voff), "s"(sbase), "s"(lds) : "memory");
}
__device__ __forceinline__ void dma16s(unsigned voff, const char* sbase, unsigned lds) {
  asm volatile("s_mov_b32 m0, %2\n\ts_nop 0\n\tglobal_load_lds_dwordx4 %0, %1 sc1"
               ::"v"(voff), "s"(sbase), "s"(lds) : "memory");
}
#define LD1(p) __builtin_nontemporal_load(p)
__device__ __forceinline__ void dma16h(unsigned voff, const char* sbase, unsigned lds) {
  asm volatile("s_mov_b32 m0, %2\n\ts_mov_b32 exec_hi, 0\n\tglobal_load_lds_dwordx4 %0, %1\n\ts_mov_b32 exec_hi, -1"
               ::"v"(voff), "s"(sbase), "s"(lds) : "memory");
}
__device__ __forceinline__ unsigned lds_off(const void* p) {
  return (unsigned)(uintptr_t)(__attribute__((address_space(3))) const char*)p;
}
#define SB0() __builtin_amdgcn_sched_barrier(0)
template <int N> __device__ __forceinline__ void wait_vm_lgkm0() {
  asm volatile("s_waitcnt vmcnt(%0) lgkmcnt(0)" ::"n"(N) : "memory");
}
__device__ __forceinline__ void bar() {
  asm volatile("" ::: "memory");
  __builtin_amdgcn_s_barrier();
  asm volatile("" ::: "memory");
}
__device__ __forceinline__ bf16x8 lds_frag(const char* p) { return *(const bf16x8*)p; }
__device__ __forceinline__ s16x4 lds_tr(const char* p) {
  return __builtin_amdgcn_ds_read_tr16_b64_v4i16((lds_s16x4_ptr)p);
}
__device__ __forceinline__ bf16x8 cat_frag(s16x4 a, s16x4 b) {
  s16x8 v = __builtin_shufflevector(a, b, 0, 1, 2, 3, 4, 5, 6, 7);
  return __builtin_bit_cast(bf16x8, v);
}
#define MFMA16(a, b, c) __builtin_amdgcn_mfma_f32_16x16x32_bf16((a), (b), (c), 0, 0, 0)

template <class Sc, int POS, int NM, int NR, int DL, class FM, class FR>
__device__ __forceinline__ void run_sched(FM& fm, FR& fr) {
  if constexpr (Sc::s[POS] != 0) {
    constexpr char c = Sc::s[POS];
    if constexpr (c == 'M') {
      fm(IC<NM>{}); SB0();
      run_sched<Sc, POS + 1, NM + 1, NR, DL>(fm, fr);
    } else {
      if constexpr (DL) { fr(IC<NR>{}); SB0(); }
      run_sched<Sc, POS + 1, NM, NR + 1, DL>(fm, fr);
    }
  }
}
template <int N> __device__ __forceinline__ void wait_vm() { asm volatile("s_waitcnt vmcnt(%0)" ::"n"(N) : "memory"); }
__device__ __forceinline__ void wait_lgkm0() { asm volatile("s_waitcnt lgkmcnt(0)" ::: "memory"); }

template <int NS, int P, int TI>
__device__ __forceinline__ void prod_tail() {
  if constexpr (TI < NS - 1) {
    wait_vm<P*(NS - 2 - TI)>();
    bar();
    prod_tail<NS, P, TI + 1>();
  }
}
constexpr int ramp_A(int j, int NS) {
  int a = 2;
  for (int i = 1; i <= j; ++i) { int lim = (i == 1) ? NS : (i - 1) + NS; a = (a + 2 < lim) ? a + 2 : lim; }
  return a;
}
constexpr int ramp_len(int NS) { int j = 1; while (ramp_A(j, NS) != ((j == 1) ? NS : (j - 1) + NS) || j < 2) ++j; return j; }
template <int NS, int P, int J, int R, class Issue>
__device__ __forceinline__ void prod_ramp(Issue& issue) {
  if constexpr (J < R) {
    constexpr int a0 = (J == 0) ? 0 : ramp_A(J - 1, NS), a1 = ramp_A(J, NS);
#pragma unroll
    for (int s = a0; s < a1; ++s) issue(s, s % NS);
    wait_vm<P*(a1 - (J + 1))>();
    bar();
    prod_ramp<NS, P, J + 1, R>(issue);
  }
}
template <int NS, int NK, int P, class Issue>
__device__ __forceinline__ void producer(Issue issue) {
  constexpr int R = ramp_len(NS);
  static_assert(R - 1 + NS <= NK, "ramp too long");
  prod_ramp<NS, P, 0, R>(issue);
#pragma unroll
  for (int s = ramp_A(R - 1, NS); s < R - 1 + NS; ++s) issue(s, s % NS);
  int wr = (R - 1) % NS;
#pragma nounroll
  for (int i = R - 1; i < NK - NS; ++i) {
    wait_vm<P*(NS - 2)>();
    bar();
    issue(i + NS, wr);
    wr = (wr + 1 == NS) ? 0 : wr + 1;
  }
  prod_tail<NS, P, 0>();
}
#define PFD 2
template <int NK, int PR, int PC, class IR, class ICC, class Mid>
__device__ __forceinline__ void producer_seam(IR issueR, ICC issueC, Mid mid) {
  constexpr int P = PR + PC;
  issueR(0, 0); issueR(1, 1);
  wait_vm<PR>(); bar();
  issueR(2, 2); issueC(2, 2); issueR(3, 3); issueC(3, 3);
  wait_vm<2 * P>(); bar();
  mid();
  constexpr int J0 = 2;
#pragma nounroll
  for (int j = J0; j <= NK - 3; ++j) {
    issueR(j + 2, (j + 2) & 3); issueC(j + 2, (j + 2) & 3);
    wait_vm<2 * P>(); bar();
  }
  wait_vm<P>(); bar();
  wait_vm<0>(); bar();
}
template <int NS, int NK, class Load0, class Body>
__device__ __forceinline__ void consumer(Load0 load0, Body body) {
  static_assert((NK & 1) == 0, "even number of stages");
  bar();
  load0();
  int rd = 1;
#pragma nounroll
  for (int i = 0; i < NK - 2; i += 2) {
    wait_lgkm0();
    bar();
    SB0();
    body(IC<0>{}, IC<1>{}, rd);
    SB0();
    rd = (rd + 1 == NS) ? 0 : rd + 1;
    wait_lgkm0();
    bar();
    SB0();
    body(IC<1>{}, IC<1>{}, rd);
    SB0();
    rd = (rd + 1 == NS) ? 0 : rd + 1;
  }
  wait_lgkm0();
  bar();
  SB0();
  body(IC<0>{}, IC<1>{}, rd);
  SB0();
  body(IC<1>{}, IC<0>{}, 0);
  SB0();
}
template <int NK, class Load0, class Half>
__device__ __forceinline__ void consumer_half(Load0 load0, Half half) {
  bar();
  load0();
#pragma nounroll
  for (int s = 0; s < NK - 1; ++s) {
    SB0();
    half(IC<0>{}, IC<1>{}, s & 3);
    SB0();
    wait_lgkm0();
    bar();
    SB0();
    half(IC<1>{}, IC<1>{}, (s + 1) & 3);
    SB0();
  }
  half(IC<0>{}, IC<1>{}, (NK - 1) & 3);
  SB0();
  half(IC<1>{}, IC<0>{}, 0);
  SB0();
}
typedef __attribute__((address_space(1))) unsigned gu32;
__device__ __forceinline__ void st16_wt(void* p, u32x4 v) {
  asm volatile("global_store_dwordx4 %0, %1, off sc1\n\ts_nop 1" ::"v"(p), "v"(v) : "memory");
}
__device__ __forceinline__ void st8u_wt(void* p, u32x2 v) {
  asm volatile("global_store_dwordx2 %0, %1, off sc1\n\ts_nop 1" ::"v"(p), "v"(v) : "memory");
}
__device__ __forceinline__ void st8_wt(void* p, f32x2 v) {
  asm volatile("global_store_dwordx2 %0, %1, off sc1\n\ts_nop 1" ::"v"(p), "v"(v) : "memory");
}
__device__ __forceinline__ void wait_count(char* ws, int idx, unsigned need) {
  gu32* cnt = (gu32*)(ws + WS_CNT + idx * 256);
#pragma nounroll
  for (unsigned spins = 0; spins < (1u << 22); ++spins) {
    if ((unsigned)__builtin_amdgcn_readfirstlane(__hip_atomic_load(cnt, __ATOMIC_RELAXED, __HIP_MEMORY_SCOPE_AGENT)) >= need) break;
    __builtin_amdgcn_s_sleep(1);
  }
  asm volatile("" ::: "memory");
}
__device__ __forceinline__ void add_count(char* ws, int idx) {
  __hip_atomic_fetch_add((gu32*)(ws + WS_CNT + idx * 256), 1u, __ATOMIC_RELAXED, __HIP_MEMORY_SCOPE_AGENT);
}
__device__ __forceinline__ int swap23(int r) { return (r & ~12) | ((r & 4) << 1) | ((r & 8) >> 1); }

#define WPF_KB 4
__global__ __launch_bounds__(256) void k1_ln_mod(const unsigned short* __restrict__ x,
                                                 const unsigned short* __restrict__ c,
                                                 const unsigned short* __restrict__ wmod,
                                                 const unsigned short* __restrict__ wre,
                                                 const unsigned short* __restrict__ wim,
                                                 const unsigned short* __restrict__ w2,
                                                 const unsigned short* __restrict__ dre,
                                                 const unsigned short* __restrict__ dim,
                                                 const unsigned short* __restrict__ w1,
                                                 const unsigned short* __restrict__ outp,
                                                 char* __restrict__ ws) {
  __shared__ __attribute__((aligned(16))) char smem[16 * 1296 + 256];
  const int tid = threadIdx.x, lane = tid & 63;
  const int bid = blockIdx.x;
  if (bid < 128) {
    const int xcd = bid & 7, i = bid >> 3;
    const int rg = xcd * 4 + (i >> 2), sub = i & 3;
    const int row0 = rg * 64 + sub * 16;
    const int r = tid >> 4, j = tid & 15;
    const u32x4* src = (const u32x4*)(x + (size_t)(row0 + r) * D_);
    u32x4 v[5];
#pragma unroll
    for (int q = 0; q < 5; ++q) v[q] = LD1(src + j + 16 * q);
    float f[40];
#pragma unroll
    for (int q = 0; q < 5; ++q)
#pragma unroll
      for (int e = 0; e < 4; ++e) { f[q * 8 + 2 * e] = lo16(v[q][e]); f[q * 8 + 2 * e + 1] = hi16(v[q][e]); }
    float s = 0.f;
#pragma unroll
    for (int e = 0; e < 40; ++e) s += f[e];
#pragma unroll
    for (int o = 1; o < 16; o <<= 1) s += __shfl_xor(s, o, 64);
    const float mean = s * (1.0f / D_);
    float s2 = 0.f;
#pragma unroll
    for (int e = 0; e < 40; ++e) { float d = f[e] - mean; s2 += d * d; }
#pragma unroll
    for (int o = 1; o < 16; o <<= 1) s2 += __shfl_xor(s2, o, 64);
    const float mb = bfr(mean);
    const float vb = bfr(s2 * (1.0f / D_));
    const float rden = 1.0f / bfr(sqrtf(bfr(vb + 1e-5f)));
#pragma unroll
    for (int q = 0; q < 5; ++q) {
      u32x4 o;
#pragma unroll
      for (int e = 0; e < 4; ++e) {
        float a = bfr(f[q * 8 + 2 * e] - mb) * rden;
        float b = bfr(f[q * 8 + 2 * e + 1] - mb) * rden;
        o[e] = pk2(a, b);
      }
      *(u32x4*)(smem + r * 1296 + (j + 16 * q) * 16) = o;
    }
    wait_lgkm0();
    bar();
    const int grp = tid >> 4, li = tid & 15, qq = li >> 2, pp = li & 3;
    char* ht = ws + WS_HT + (size_t)(row0 >> 4) * 2560;
#pragma unroll
    for (int it = 0; it < 5; ++it) {
      const int pr = it * 16 + grp;
      const int cblk = pr >> 1, half = pr & 1;
      const char* a0 = smem + (8 * half + qq) * 1296 + (16 * cblk + 4 * pp) * 2;
      s16x4 t0 = lds_tr(a0);
      s16x4 t1 = lds_tr(a0 + 4 * 1296);
      s16x8 o = __builtin_shufflevector(t0, t1, 0, 1, 2, 3, 4, 5, 6, 7);
      const int d = 16 * cblk + li;
      const int cb = d / 80, rr = d - cb * 80;
      *(s16x8*)(ht + (size_t)cb * (128 * 2560) + rr * 32 + half * 16) = o;
    }
    if (bid == 0 && tid < 96) *(u32x4*)(ws + WS_E + 2560 + tid * 16) = (u32x4){0u, 0u, 0u, 0u};
    if (bid == 1 && tid < 48) *(unsigned*)(ws + WS_CNT + tid * 256) = 0u;
  } else if (bid < 368) {
    const int g = bid - 128;
    const int cgp = (g & 7) * 30 + (g >> 3);
    const int l2 = tid & 1, rg = tid >> 1;
    u32x4 v[5];
    unsigned short cv16[5];
#pragma unroll
    for (int q = 0; q < 5; ++q) {
      const int k = q * 128 + rg;
      v[q] = *(const u32x4*)(wmod + (size_t)k * MODN + cgp * 16 + l2 * 8);
      cv16[q] = c[k];
    }
    float acc[8];
#pragma unroll
    for (int e = 0; e < 8; ++e) acc[e] = 0.f;
#pragma unroll
    for (int q = 0; q < 5; ++q) {
      const float cv = bf2f(cv16[q]);
      const float sv = bfr(cv / (1.0f + __expf(-cv)));
#pragma unroll
      for (int e = 0; e < 4; ++e) {
        acc[2 * e] += sv * lo16(v[q][e]);
        acc[2 * e + 1] += sv * hi16(v[q][e]);
      }
    }
#pragma unroll
    for (int o = 2; o < 64; o <<= 1)
#pragma unroll
      for (int e = 0; e < 8; ++e) acc[e] += __shfl_xor(acc[e], o, 64);
    float* red = (float*)smem;
    if (lane < 2) {
#pragma unroll
      for (int e = 0; e < 8; ++e) red[(tid >> 6) * 16 + lane * 8 + e] = acc[e];
    }
    wait_lgkm0();
    bar();
    if (tid < 16) {
      float m = bfr((red[tid] + red[16 + tid]) + (red[32 + tid] + red[48 + tid]));
      const int n = cgp * 16 + tid;
      const int part = n / D_, d = n - part * D_;
      if (part == 1 || part == 4) m = bfr(1.0f + m);
      ((float*)(ws + WS_MOD))[n] = m;
      if (part == 4) ((unsigned short*)(ws + WS_E))[d] = (unsigned short)(__float_as_uint(m) >> 16);
      if (part == 3) ((unsigned short*)(ws + WS_E))[D_ + d] = (unsigned short)(__float_as_uint(m) >> 16);
    }
  } else if (bid < 464) {
    const int k0 = (bid - 368) * 16;
    u32x4 v[5];
#pragma unroll
    for (int q = 0; q < 5; ++q) v[q] = LD1((const u32x4*)((const char*)w2 + (size_t)k0 * (D_ * 2) + (tid + 256 * q) * 16));
#pragma unroll
    for (int q = 0; q < 5; ++q) {
      const int id = tid + 256 * q;
      const int r = id / 80, ch = id - r * 80;
      const int cb = ch / 10, cc = ch - cb * 10;
      *(u32x4*)(ws + WS_W2T + ((size_t)cb * F_ + k0 + swap23(r)) * 160 + cc * 16) = v[q];
    }
  } else if (bid < 560) {
    const int c0 = (bid - 464) * 1280;
    u32x4 v[5];
#pragma unroll
    for (int q = 0; q < 5; ++q) v[q] = LD1((const u32x4*)((const char*)w1 + (size_t)(c0 + tid + 256 * q) * 16));
#pragma unroll
    for (int q = 0; q < 5; ++q) {
      const int id = c0 + tid + 256 * q;
      const int k = id / 192, ch = id - k * 192;
      const int cb = ch / 12, cc = ch - cb * 12;
      const int r = swap23(k);
      *(u32x4*)(ws + WS_W1T + ((size_t)cb * D_ + r) * 192 + (cc ^ (((r >> 2) & 1) << 1)) * 16) = v[q];
    }
  } else if (bid < 640) {
    const int m = bid >= 600, c0 = (bid - 560 - 40 * m) * 1280;
    u32x4 v[5];
#pragma unroll
    for (int q = 0; q < 5; ++q) v[q] = LD1((const u32x4*)((const char*)(m ? dim : dre) + (size_t)(c0 + tid + 256 * q) * 16));
#pragma unroll
    for (int q = 0; q < 5; ++q) {
      const int id = c0 + tid + 256 * q;
      const int e = id / 80, ch = id - e * 80;
      const int cb = e / 80, row = e - cb * 80;
      *(u32x4*)(ws + WS_WDT + ((size_t)(cb * 20 + m * 10 + (ch >> 3)) * 80 + row) * 128 + ((ch & 7) ^ ((row >> 1) & 7)) * 16) = v[q];
    }
  } else if (bid >= 640 + 64 * WPF_KB) {
    const int t = ((bid - 640 - 64 * WPF_KB) >> 3) * 256 + tid;
    const char* a;
    if (t < 384) a = ws + (size_t)t * 65536;
    else if (t < 512) a = (const char*)wre + (size_t)(t - 384) * 65536;
    else if (t < 640) a = (const char*)wim + (size_t)(t - 512) * 65536;
    else if (t < 680) a = (const char*)x + (size_t)(t - 640) * 65536;
    else if (t < 720) a = (const char*)outp + (size_t)(t - 680) * 65536;
    else a = ws;
    unsigned v;
    asm volatile("global_load_dword %0, %1, off" : "=v"(v) : "v"(a) : "memory");
    wait_vm<0>();
    asm volatile("" ::"v"(v));
  } else {
    const int pw = (bid - 640) * 4 + __builtin_amdgcn_readfirstlane(tid >> 6);
    constexpr int RPW = 16 / WPF_KB;
    const int row0 = pw * RPW;
    const char* src = (const char*)(row0 < 2048 ? wre : wim) + (size_t)(row0 & 2047) * 4096;
    u32x4 sink[16];
#pragma unroll
    for (int i = 0; i < RPW; ++i)
#pragma unroll
      for (int q = 0; q < WPF_KB; ++q)
        asm volatile("global_load_dwordx4 %0, %1, %2 offset:%3" : "=v"(sink[WPF_KB * i + q]) : "v"(lane * 16), "s"(src + i * 4096), "n"(q * 1024) : "memory");
    wait_vm<0>();
#pragma unroll
    for (int i = 0; i < 16; ++i) asm volatile("" ::"v"(sink[i]));
  }
}

__device__ __forceinline__ void dma_res_tile(const char* base  , unsigned lds, int w, int lane) {
#pragma unroll
  for (int j = 0; j < 3; ++j) {
    int L = 160 * w + 64 * j + lane;
    if (L > 639) L = 639;
    const int r = L / 10, ch = L - r * 10;
    if (j < 2) dma16(r * (D_ * 2) + ch * 16, base, lds + (160 * w + 64 * j) * 16);
    else dma16h(r * (D_ * 2) + ch * 16, base, lds + (160 * w + 128) * 16);
  }
}
template <int N>
__device__ __forceinline__ void l2_warm(const char* base, unsigned dummy_lds, int lane) {
#pragma unroll
  for (int i = 0; i < N; ++i) dma16(lane * 16, base + i * 1024, dummy_lds);
}

struct SchedP2 { static constexpr char s[] = "MRMRMMRMRMMRMRMMRMRMMRMMMMMMMMMMM"; };
struct SchedP3 { static constexpr char s[] = "RRMRMRRMRMRRMRMRRMRMMM"; };
struct SchedP4 { static constexpr char s[] = "MRMRMRMMRMRMRMMRMRMMRMRMRMMRMRMRMMRMRMMMMMMMMM"; };
constexpr int cnt_ch(const char* s, char c) { int n = 0; for (; *s; ++s) n += (*s == c); return n; }
static_assert(cnt_ch(SchedP2::s, 'M') == 24 && cnt_ch(SchedP2::s, 'R') == 9, "");
static_assert(cnt_ch(SchedP3::s, 'M') == 10 && cnt_ch(SchedP3::s, 'R') == 12, "");
static_assert(cnt_ch(SchedP4::s, 'M') == 30 && cnt_ch(SchedP4::s, 'R') == 16, "");
constexpr int L_RES = 147456, L_MOD = 159744, L_DUM = L_MOD + 2048, L_TOTAL = L_DUM + 1024;
#define P2NS 5
constexpr int P2_NS = P2NS, P2_STAGE = 26624, P2_X = 73728;
constexpr int P3_STAGE = 36864;
constexpr int P4_STAGE = 28672, P4_E = 4 * P4_STAGE, P4_ST = P4_E + 4096, P4_RS = P4_ST + 8192, P4_STG = 2 * P4_STAGE;

__global__ __launch_bounds__(512, 2) void mega(const unsigned short* __restrict__ wre,
                                               const unsigned short* __restrict__ wim,
                                               const unsigned short* __restrict__ x,
                                               char* __restrict__ ws,
                                               unsigned short* __restrict__ out) {
  STAMP_DECL
  STAMP(0);
  extern __shared__ __attribute__((aligned(16))) char smem[];
  const int tid = threadIdx.x, lane = tid & 63;
  const int wv = __builtin_amdgcn_readfirstlane(tid >> 6);
  const int w = wv & 3;
  const bool prod = wv < 4;
  const int bid = blockIdx.x;
  const int xcd = bid & 7, bi = bid >> 3;
  const int cb = bi & 7, rsel = bi >> 3;
  const int g = lane >> 4, l15 = lane & 15;
  const unsigned lds0 = lds_off(smem);
  const float* mod = (const float*)(smem + L_MOD);
  const int t3 = (xcd * 4 + rsel) * 64;
  const int grp3 = xcd * 2 + (rsel >> 1);

  {
    const int isim = rsel >> 1;
    const int grp2 = xcd * 2 + (rsel & 1);
    const int t0 = grp2 * 128;
    if (prod) {
      const char* rbase = (const char*)(isim ? wim : wre) + (size_t)t0 * (S_ * 2);
      const char* cbase = ws + WS_HT + (size_t)cb * 32 * 10240;
      unsigned roff[4];
#pragma unroll
      for (int j = 0; j < 4; ++j) {
        const int row = 32 * w + 8 * j + (lane >> 3);
        roff[j] = row * (S_ * 2) + 16 * ((lane & 7) ^ ((row >> 1) & 7));
      }
      const unsigned coff = (20 * w) * 128 + lane * 16;
      producer<P2_NS, 32, 7>([&](int ks, int buf) {
        const unsigned lb = lds0 + buf * P2_STAGE;
        {
#pragma unroll
          for (int j = 0; j < 4; ++j) dma16(roff[j], rbase + ks * 128, lb + (32 * w + 8 * j) * 128);
        }
        dma16(coff, cbase + ks * 10240, lb + 16384 + (20 * w) * 128);
        dma16(coff, cbase + ks * 10240 + 1024, lb + 16384 + (20 * w + 8) * 128);
        dma16h(coff, cbase + ks * 10240 + 2048, lb + 16384 + (20 * w + 16) * 128);
      });
      bar();
      const char* wdt = ws + WS_WDT + (size_t)cb * 204800;
#pragma unroll
      for (int s = 0; s < PFD; ++s)
#pragma unroll
        for (int j = 0; j < 5; ++j)
          dma16(lane * 16, wdt + s * 20480 + w * 5120 + j * 1024, lds0 + s * P3_STAGE + 16384 + w * 5120 + j * 1024);
    } else {
      if (w < 2) {
        int L = 64 * w + lane; if (L > 99) L = 99;
        const int vec = L / 20, part = vec + (vec >= 3);
        dma16(part * 2560 + cb * 320 + (L - vec * 20) * 16, ws + WS_MOD, lds0 + L_MOD + w * 1024);
      }
      dma_res_tile((const char*)x + (size_t)t3 * (D_ * 2) + cb * 160, lds0 + L_RES, w, lane);
      const int wm = w & 1, wk = w >> 1;
      const int fsw = ((4 * wk + g) ^ (l15 >> 1)) * 16;
      const int ra = (64 * wm + l15) * 128 + fsw;
      const int ca_ = 16384 + (2 * wk + (g >> 1)) * 2560 + l15 * 32 + (g & 1) * 16;
      bf16x8 rf[2][4], cf[2][5];
      f32x4 acc[6][4];
#pragma unroll
      for (int j = 0; j < 6; ++j)
#pragma unroll
        for (int i = 0; i < 4; ++i) acc[j][i] = (f32x4){0.f, 0.f, 0.f, 0.f};
      const s16x8 ones_s = {0x3F80, 0x3F80, 0x3F80, 0x3F80, 0x3F80, 0x3F80, 0x3F80, 0x3F80};
      const bf16x8 ones = __builtin_bit_cast(bf16x8, ones_s);
      auto ldf = [&](auto par, auto ic, int buf) {
        constexpr int PP = decltype(par)::value, n = decltype(ic)::value;
        const char* lb = smem + buf * P2_STAGE;
        if constexpr (n == 0) rf[PP][0] = lds_frag(lb + ra);
        else if constexpr (n < 6) cf[PP][n - 1] = lds_frag(lb + ca_ + (n - 1) * 512);
        else rf[PP][n - 5] = lds_frag(lb + ra + (n - 5) * 2048);
      };
      auto body = [&](auto par, auto dl, int rd) {
        constexpr int PP = decltype(par)::value, DL = decltype(dl)::value;
        auto fm = [&](auto mc) {
          constexpr int m = decltype(mc)::value, i = m / 6, j = m % 6;
          if constexpr (j < 5) acc[j][i] = MFMA16(cf[PP][j], rf[PP][i], acc[j][i]);
          else acc[5][i] = MFMA16(ones, rf[PP][i], acc[5][i]);
        };
        auto fr = [&](auto rc) { ldf(IC<PP ^ 1>{}, rc, rd); };
        run_sched<SchedP2, 0, 0, 0, DL>(fm, fr);
      };
      auto load0 = [&] { static_for<9>([&](auto ic) { ldf(IC<0>{}, ic, 0); }); };
      consumer<P2_NS, 32>(load0, body);
      STAMP(1);
      auto finish = [&](auto keepc) {
        constexpr int KP = decltype(keepc)::value;
        char* dst = smem + P2_X + w * 10752 + lane * 16;
#pragma unroll
        for (int j = 0; j < 5; ++j) {
          *(f32x4*)(dst + (2 * j) * 1024) = acc[j][2 * (KP ^ 1)];
          *(f32x4*)(dst + (2 * j + 1) * 1024) = acc[j][2 * (KP ^ 1) + 1];
        }
        *(f32x2*)(dst + 10240 - lane * 8) = (f32x2){acc[5][2 * (KP ^ 1)][0], acc[5][2 * (KP ^ 1) + 1][0]};
        wait_vm<0>();
        wait_lgkm0();
        bar();
        const char* srcp = smem + P2_X + (w ^ 2) * 10752 + lane * 16;
        const float sgn = isim ? -1.0f : 1.0f;
        const f32x2 rp = *(const f32x2*)(srcp + 10240 - lane * 8);
        const float rs0 = (acc[5][2 * KP][0] + rp[0]) * sgn, rs1 = (acc[5][2 * KP + 1][0] + rp[1]) * sgn;
        char* pq = ws + WS_PQ + ((size_t)(grp2 * 2 + wm) * 80 + isim * 40 + cb * 5) * 2048 + (32 * KP + l15) * 32 + g * 8;
#pragma unroll
        for (int j = 0; j < 5; ++j) {
          const f32x4 sa = *(const f32x4*)(mod + 16 * j + 4 * g);
          const f32x4 c1 = *(const f32x4*)(mod + 80 + 16 * j + 4 * g) * sgn;
          const f32x4 v0 = c1 * (acc[j][2 * KP] + *(const f32x4*)(srcp + (2 * j) * 1024)) + sa * rs0;
          const f32x4 v1 = c1 * (acc[j][2 * KP + 1] + *(const f32x4*)(srcp + (2 * j + 1) * 1024)) + sa * rs1;
          st8u_wt(pq + j * 2048, (u32x2){pk2(v0[0], v0[1]), pk2(v0[2], v0[3])});
          st8u_wt(pq + j * 2048 + 512, (u32x2){pk2(v1[0], v1[1]), pk2(v1[2], v1[3])});
        }
      };
      if (wk == 0) finish(IC<0>{}); else finish(IC<1>{});
    }
    STAMP(2);
    if (!prod) wait_vm<0>();
    bar();
    STAMP(3);
    if (wv == 4) {
      if (lane == 0) add_count(ws, grp2);
      wait_count(ws, grp3, 16);
    }
    bar();
  }

  {
    const char* wdt = ws + WS_WDT + (size_t)cb * 204800;
    if (prod) {
      const char* rbase = ws + WS_PQ + (size_t)(t3 >> 6) * (80 * 2048) + w * 4096;
      STAMP(4);
      producer_seam<10, 4, 5>([&](int ks, int buf) {
#pragma unroll
        for (int j = 0; j < 4; ++j) dma16s(lane * 16, rbase + ks * 16384 + j * 1024, lds0 + buf * P3_STAGE + w * 4096 + j * 1024);
      }, [&](int ks, int buf) {
#pragma unroll
        for (int j = 0; j < 5; ++j)
          dma16(lane * 16, wdt + ks * 20480 + w * 5120 + j * 1024, lds0 + buf * P3_STAGE + 16384 + w * 5120 + j * 1024);
      }, [] {});
      bar();
      const char* w1t = ws + WS_W1T + (size_t)(bi & 15) * 122880;
      dma16(w * 1024 + lane * 16, ws + WS_E, lds0 + P4_E + w * 1024);
#pragma unroll
      for (int s = 0; s < PFD; ++s)
#pragma unroll
        for (int j = 0; j < 3; ++j)
          dma16(lane * 16, w1t + s * 12288 + w * 3072 + j * 1024, lds0 + s * P4_STAGE + 16384 + w * 3072 + j * 1024);
    } else {
      const int sw0 = (g ^ (l15 >> 1)) * 16, sw1 = ((4 + g) ^ (l15 >> 1)) * 16;
      const int ra = (g >> 1) * 2048 + (16 * w + l15) * 32 + (g & 1) * 16, ca_ = 16384 + l15 * 128;
      bf16x8 rf[2][2], cf[2][2][5];
      f32x4 acc[5];
#pragma unroll
      for (int j = 0; j < 5; ++j) acc[j] = (f32x4){0.f, 0.f, 0.f, 0.f};
      auto ldf = [&](auto hc, auto ic, int buf) {
        constexpr int H = decltype(hc)::value, n = decltype(ic)::value, kk = n / 6, m = n % 6;
        const char* lb = smem + buf * P3_STAGE;
        if constexpr (m == 0) rf[H][kk] = lds_frag(lb + ra + H * 8192 + kk * 4096);
        else cf[H][kk][m - 1] = lds_frag(lb + (kk ? sw1 : sw0) + ca_ + H * 10240 + (m - 1) * 2048);
      };
      auto half = [&](auto par, auto dl, int buf) {
        constexpr int PP = decltype(par)::value, DL = decltype(dl)::value;
        auto fm = [&](auto mc) {
          constexpr int m = decltype(mc)::value, kk = m / 5, j = m % 5;
          acc[j] = MFMA16(cf[PP][kk][j], rf[PP][kk], acc[j]);
        };
        auto fr = [&](auto rc) { ldf(IC<PP ^ 1>{}, rc, buf); };
        run_sched<SchedP3, 0, 0, 0, DL>(fm, fr);
      };
      auto load0 = [&] { STAMP(4); static_for<12>([&](auto ic) { ldf(IC<0>{}, ic, 0); }); };
      consumer_half<10>(load0, half);
      STAMP(5);
      bar();
      char* res = smem + L_RES + (16 * w + l15) * 160 + g * 8;
      char* xc = ws + WS_XC + ((size_t)(t3 >> 6) * 40 + cb * 5) * 2048 + (16 * w + l15) * 32 + g * 8;
      float xf[20];
      float s = 0.f;
#pragma unroll
      for (int j = 0; j < 5; ++j) {
        const u32x2 xr = *(const u32x2*)(res + j * 32);
        const f32x4 ga = *(const f32x4*)(mod + 160 + 16 * j + 4 * g);
        const f32x4 c1 = *(const f32x4*)(mod + 240 + 16 * j + 4 * g);
        const f32x4 y = acc[j];
        const u32x2 o = {pk2(lo16(xr[0]) + ga[0] * y[0], hi16(xr[0]) + ga[1] * y[1]),
                         pk2(lo16(xr[1]) + ga[2] * y[2], hi16(xr[1]) + ga[3] * y[3])};
        *(u32x2*)(res + j * 32) = o;
        xf[4 * j] = lo16(o[0]); xf[4 * j + 1] = hi16(o[0]); xf[4 * j + 2] = lo16(o[1]); xf[4 * j + 3] = hi16(o[1]);
        st8u_wt(xc + j * 2048, (u32x2){pk2(xf[4 * j] * c1[0], xf[4 * j + 1] * c1[1]), pk2(xf[4 * j + 2] * c1[2], xf[4 * j + 3] * c1[3])});
        s += (xf[4 * j] + xf[4 * j + 1]) + (xf[4 * j + 2] + xf[4 * j + 3]);
      }
      s += __shfl_xor(s, 16, 64);
      s += __shfl_xor(s, 32, 64);
      const float ml = s * (1.0f / 80.0f);
      float m2 = 0.f;
#pragma unroll
      for (int e = 0; e < 20; ++e) { float d = xf[e] - ml; m2 += d * d; }
      m2 += __shfl_xor(m2, 16, 64);
      m2 += __shfl_xor(m2, 32, 64);
      if (lane < 16) st8_wt(ws + WS_ST + ((size_t)(t3 >> 6) * 8 + cb) * 512 + (16 * w + l15) * 8, (f32x2){s, m2});
      STAMP(6);
      wait_vm<0>();
    }
    bar();
    STAMP(7);
    if (wv == 4) {
      if (lane == 0) add_count(ws, 16 + grp3);
      wait_count(ws, 16 + xcd * 2 + (bi >> 4), 16);
    }
    bar();
  }

  {
    const int cb4 = bi & 15, grp4 = xcd * 2 + (bi >> 4);
    const int t0 = grp4 * 128;
    const char* w1t = ws + WS_W1T + (size_t)cb4 * 122880;
    if (prod) {
      const char* rbase = ws + WS_XC + (size_t)(grp4 * 2 + (w >> 1)) * (40 * 2048) + (w & 1) * 4096;
      STAMP(8);
      dma16s(w * 2048 + lane * 16, ws + WS_ST + (size_t)t0 * 64, lds0 + P4_ST + w * 2048);
      dma16s(w * 2048 + 1024 + lane * 16, ws + WS_ST + (size_t)t0 * 64, lds0 + P4_ST + w * 2048 + 1024);
      producer_seam<10, 4, 3>([&](int ks, int buf) {
#pragma unroll
        for (int j = 0; j < 4; ++j) dma16s(lane * 16, rbase + ks * 8192 + j * 1024, lds0 + buf * P4_STAGE + w * 4096 + j * 1024);
      }, [&](int ks, int buf) {
#pragma unroll
        for (int j = 0; j < 3; ++j)
          dma16(lane * 16, w1t + ks * 12288 + w * 3072 + j * 1024, lds0 + buf * P4_STAGE + 16384 + w * 3072 + j * 1024);
      }, [&] {
        if (tid < 128) {
          const char* sp = smem + P4_ST + (tid >> 6) * 4096 + (tid & 63) * 8;
          f32x2 pp[8];
#pragma unroll
          for (int e = 0; e < 8; ++e) pp[e] = *(const f32x2*)(sp + e * 512);
          const float mean = (((pp[0][0] + pp[1][0]) + (pp[2][0] + pp[3][0])) + ((pp[4][0] + pp[5][0]) + (pp[6][0] + pp[7][0]))) * (1.0f / D_);
          float m2 = ((pp[0][1] + pp[1][1]) + (pp[2][1] + pp[3][1])) + ((pp[4][1] + pp[5][1]) + (pp[6][1] + pp[7][1]));
          float dd = 0.f;
#pragma unroll
          for (int e = 0; e < 8; ++e) { const float d = pp[e][0] * (1.0f / 80.0f) - mean; dd += d * d; }
          m2 += 80.0f * dd;
          const float rden = 1.0f / bfr(sqrtf(bfr(bfr(m2 * (1.0f / D_)) + 1e-5f)));
          *(f32x2*)(smem + P4_RS + tid * 8) = (f32x2){bfr(mean), rden};
        }
        wait_lgkm0();
      });
      bar();
      const char* w2t = ws + WS_W2T + (size_t)cb * F_ * 160;
#pragma unroll
      for (int s = 0; s < PFD; ++s)
#pragma unroll
        for (int j = 0; j < 5; ++j)
          dma16(lane * 16, w2t + s * 20480 + w * 5120 + j * 1024, lds0 + s * P3_STAGE + 16384 + w * 5120 + j * 1024);
    } else {
      const int q = l15 >> 2, p4 = lane & 3;
      const int wm = w & 1, wn = w >> 1;
      const int rpos = 16 * (g >> 1) + 4 * (g & 1) + q;
      const int xs = (rpos >> 2) & 1;
      int cadr[3];
#pragma unroll
      for (int j = 0; j < 3; ++j) {
        const int jj = 3 * wn + j;
        cadr[j] = 16384 + rpos * 192 + (2 * (jj ^ xs) + (p4 >> 1)) * 16 + (p4 & 1) * 8;
      }
      const int ra = wm * 8192 + (g >> 1) * 2048 + l15 * 32 + (g & 1) * 16;
      int ea = P4_E + (l15 < 2 ? l15 : 2) * 1280 + g * 16;
      bf16x8 rf[2][2][5], cf[2][2][3];
      f32x4 acc[3][5];
#pragma unroll
      for (int j = 0; j < 3; ++j)
#pragma unroll
        for (int i = 0; i < 5; ++i) acc[j][i] = (f32x4){0.f, 0.f, 0.f, 0.f};
      auto ldf = [&](auto par, auto ic, int buf) {
        constexpr int PP = decltype(par)::value, n = decltype(ic)::value, kk = n / 8, m = n % 8;
        const char* lb = smem + buf * P4_STAGE;
        if constexpr (m == 0) rf[PP][kk][0] = lds_frag(lb + ra + kk * 4096);
        else if constexpr (m < 4) cf[PP][kk][m - 1] = cat_frag(lds_tr(lb + cadr[m - 1] + kk * 6144), lds_tr(lb + cadr[m - 1] + kk * 6144 + 1536));
        else if constexpr (m < 7) rf[PP][kk][m - 3] = lds_frag(lb + ra + kk * 4096 + (m - 3) * 512);
        else {
          rf[PP][kk][4] = lds_frag(smem + ea + kk * 64);
          if constexpr (kk == 1) ea += 128;
        }
      };
      auto body = [&](auto par, auto dl, int rd) {
        constexpr int PP = decltype(par)::value, DL = decltype(dl)::value;
        auto fm = [&](auto mc) {
          constexpr int m = decltype(mc)::value, kk = m / 15, i = (m % 15) / 3, j = m % 3;
          acc[j][i] = MFMA16(cf[PP][kk][j], rf[PP][kk][i], acc[j][i]);
        };
        auto fr = [&](auto rc) { ldf(IC<PP ^ 1>{}, rc, rd); };
        run_sched<SchedP4, 0, 0, 0, DL>(fm, fr);
      };
      auto load0 = [&] { STAMP(8); static_for<16>([&](auto ic) { ldf(IC<0>{}, ic, 0); }); };
      consumer<4, 10>(load0, body);
      STAMP(9);
      bar();
      f32x4 u[3], v[3];
#pragma unroll
      for (int j = 0; j < 3; ++j)
#pragma unroll
        for (int e = 0; e < 4; ++e) {
          const int a = __float_as_int(acc[j][4][e]);
          u[j][e] = __int_as_float(__builtin_amdgcn_update_dpp(0, a, 0x150, 0xf, 0xf, false));
          v[j][e] = __int_as_float(__builtin_amdgcn_update_dpp(0, a, 0x151, 0xf, 0xf, false));
        }
      char* h1 = ws + WS_H1 + ((size_t)(grp4 * 2 + wm) * 96 + cb4 * 6 + 3 * wn) * 2048 + l15 * 32 + g * 8;
#pragma unroll
      for (int i = 0; i < 4; ++i) {
        const f32x2 rs = *(const f32x2*)(smem + P4_RS + (64 * wm + 16 * i + l15) * 8);
#pragma unroll
        for (int j = 0; j < 3; ++j) {
          const f32x4 hv = (acc[j][i] - u[j] * rs[0]) * rs[1] + v[j];
          st8u_wt(h1 + j * 2048 + i * 512,
                  (u32x2){pk2(fmaxf(hv[0], 0.f), fmaxf(hv[1], 0.f)), pk2(fmaxf(hv[2], 0.f), fmaxf(hv[3], 0.f))});
        }
      }
      STAMP(10);
      wait_vm<0>();
    }
    bar();
    STAMP(11);
    if (wv == 4) {
      if (lane == 0) add_count(ws, 32 + grp4);
      wait_count(ws, 32 + grp3, 16);
    }
    bar();
  }

  {
    const char* w2t = ws + WS_W2T + (size_t)cb * F_ * 160;
    if (prod) {
      const char* rbase = ws + WS_H1 + (size_t)(t3 >> 6) * (96 * 2048) + w * 4096;
      STAMP(12);
      producer_seam<12, 4, 5>([&](int ks, int buf) {
#pragma unroll
        for (int j = 0; j < 4; ++j) dma16s(lane * 16, rbase + ks * 16384 + j * 1024, lds0 + buf * P3_STAGE + w * 4096 + j * 1024);
      }, [&](int ks, int buf) {
#pragma unroll
        for (int j = 0; j < 5; ++j)
          dma16(lane * 16, w2t + ks * 20480 + w * 5120 + j * 1024, lds0 + buf * P3_STAGE + 16384 + w * 5120 + j * 1024);
      }, [] {});
    } else {
      const int q = l15 >> 2, p4 = lane & 3;
      const int ra = (g >> 1) * 2048 + (16 * w + l15) * 32 + (g & 1) * 16;
      const int ca_ = 16384 + (16 * (g >> 1) + 4 * (g & 1) + q) * 160 + p4 * 8;
      bf16x8 rf[2][2], cf[2][2][5];
      f32x4 acc[5];
#pragma unroll
      for (int j = 0; j < 5; ++j) acc[j] = (f32x4){0.f, 0.f, 0.f, 0.f};
      auto ldf = [&](auto hc, auto ic, int buf) {
        constexpr int H = decltype(hc)::value, n = decltype(ic)::value, kk = n / 6, m = n % 6;
        const char* lb = smem + buf * P3_STAGE;
        if constexpr (m == 0) rf[H][kk] = lds_frag(lb + ra + H * 8192 + kk * 4096);
        else cf[H][kk][m - 1] = cat_frag(lds_tr(lb + ca_ + H * 10240 + kk * 5120 + (m - 1) * 32),
                                         lds_tr(lb + ca_ + H * 10240 + kk * 5120 + (m - 1) * 32 + 1280));
      };
      auto half = [&](auto par, auto dl, int buf) {
        constexpr int PP = decltype(par)::value, DL = decltype(dl)::value;
        auto fm = [&](auto mc) {
          constexpr int m = decltype(mc)::value, kk = m / 5, j = m % 5;
          acc[j] = MFMA16(cf[PP][kk][j], rf[PP][kk], acc[j]);
        };
        auto fr = [&](auto rc) { ldf(IC<PP ^ 1>{}, rc, buf); };
        run_sched<SchedP3, 0, 0, 0, DL>(fm, fr);
      };
      auto load0 = [&] { STAMP(12); static_for<12>([&](auto ic) { ldf(IC<0>{}, ic, 0); }); };
      consumer_half<12>(load0, half);
      STAMP(13);
      char* res = smem + L_RES + (16 * w + l15) * 160 + g * 8;
#pragma unroll
      for (int j = 0; j < 5; ++j) {
        const u32x2 xr = *(const u32x2*)(res + j * 32);
        const f32x4 gb = *(const f32x4*)(mod + 320 + 16 * j + 4 * g);
        const f32x4 y = acc[j];
        *(u32x2*)(res + j * 32) = (u32x2){pk2(lo16(xr[0]) + gb[0] * y[0], hi16(xr[0]) + gb[1] * y[1]),
                                          pk2(lo16(xr[1]) + gb[2] * y[2], hi16(xr[1]) + gb[3] * y[3])};
      }
      wait_vm<0>();
    }
    wait_lgkm0();
    bar();
    for (int id = tid; id < 640; id += 512) {
      const int row = id / 10, c = id - row * 10;
      *(u32x4*)((char*)out + (size_t)(t3 + row) * (D_ * 2) + cb * 160 + c * 16) = *(const u32x4*)(smem + L_RES + row * 160 + c * 16);
    }
  }
  STAMP(14);
  STAMP_OUT();
}

extern "C" void kernel_launch(void* const* d_in, const int* in_sizes, int n_in,
                              void* d_out, int out_size, void* d_ws, size_t ws_size,
                              hipStream_t stream) {
  const unsigned short* x    = (const unsigned short*)d_in[0];
  const unsigned short* wsre = (const unsigned short*)d_in[1];
  const unsigned short* wsim = (const unsigned short*)d_in[2];
  const unsigned short* wdre = (const unsigned short*)d_in[3];
  const unsigned short* wdim = (const unsigned short*)d_in[4];
  const unsigned short* w1   = (const unsigned short*)d_in[6];
  const unsigned short* w2   = (const unsigned short*)d_in[7];
  const unsigned short* c    = (const unsigned short*)d_in[8];
  const unsigned short* wmod = (const unsigned short*)d_in[9];
  char* ws = (char*)d_ws;
  unsigned short* out = (unsigned short*)d_out;
  (void)hipFuncSetAttribute((const void*)mega, hipFuncAttributeMaxDynamicSharedMemorySize, L_TOTAL);
  k1_ln_mod<<<640 + 64 * WPF_KB + 24, 256, 0, stream>>>(x, c, wmod, wsre, wsim, w2, wdre, wdim, w1, out, ws);
  mega<<<256, 512, L_TOTAL, stream>>>(wsre, wsim, x, ws, out);
}
```

```cpp
#include <hip/hip_runtime.h>
#include <hip/hip_bf16.h>
#include <stdint.h>

typedef __attribute__((ext_vector_type(8))) __bf16 bf16x8;
typedef __attribute__((ext_vector_type(4))) __bf16 bf16x4;
typedef __attribute__((ext_vector_type(2))) __bf16 bf16x2;
typedef __attribute__((ext_vector_type(4))) float f32x4;
typedef __attribute__((ext_vector_type(2))) float f32x2;
typedef __attribute__((ext_vector_type(4))) short s16x4;
typedef __attribute__((ext_vector_type(8))) short s16x8;
typedef __attribute__((ext_vector_type(4))) unsigned u32x4;
typedef __attribute__((ext_vector_type(2))) unsigned u32x2;
typedef __attribute__((address_space(3))) s16x4* lds_s16x4_ptr;
typedef __attribute__((address_space(3))) void* lds_vptr;

constexpr int S_ = 2048, D_ = 640, F_ = 1536, MODN = 3840;

constexpr size_t WS_MOD = 0;
constexpr size_t WS_E   = 16384;
constexpr size_t WS_CNT = 20480;
constexpr size_t WS_ST  = 32768;
constexpr size_t WS_RSUM = WS_ST + (size_t)S_ * 64;
constexpr size_t WS_HT  = WS_RSUM + 16384;
constexpr size_t WS_PQ  = WS_HT + (size_t)D_ * S_ * 2;
constexpr size_t WS_XC  = WS_PQ + (size_t)S_ * 1280 * 2;
constexpr size_t WS_H1  = WS_XC + (size_t)S_ * D_ * 2;
constexpr size_t WS_W2T = WS_H1 + (size_t)S_ * F_ * 2;
constexpr size_t WS_W1T = WS_W2T + (size_t)F_ * D_ * 2;
constexpr size_t WS_WDT = WS_W1T + (size_t)F_ * D_ * 2;

#define STAMP_DECL
#define STAMP(i)
#define STAMP_OUT()
template <int V> struct IC { static constexpr int value = V; };
template <int N, int I = 0, class F> __device__ __forceinline__ void static_for(F f) {
  if constexpr (I < N) { f(IC<I>{}); static_for<N, I + 1>(f); }
}

__device__ __forceinline__ float bf2f(unsigned short u) { return __uint_as_float(((unsigned)u) << 16); }
__device__ __forceinline__ float bfr(float f) { return (float)(__bf16)f; }
__device__ __forceinline__ unsigned pk2(float a, float b) {
  bf16x2 v = __builtin_convertvector((f32x2){a, b}, bf16x2);
  return __builtin_bit_cast(unsigned, v);
}
__device__ __forceinline__ float lo16(unsigned u) { return __uint_as_float(u << 16); }
__device__ __forceinline__ float hi16(unsigned u) { return __uint_as_float(u & 0xffff0000u); }

__device__ __forceinline__ void dma16(unsigned voff, const char* sbase, unsigned lds) {
  asm volatile("s_mov_b32 m0, %2\n\ts_nop 0\n\tglobal_load_lds_dwordx4 %0, %1"
               ::"v"(voff), "s"(sbase), "s"(lds) : "memory");
}
__device__ __forceinline__ void dma16nt(unsigned voff, const char* sbase, unsigned lds) {
  asm volatile("s_mov_b32 m0, %2\n\ts_nop 0\n\tglobal_load_lds_dwordx4 %0, %1 nt"
               ::"v"(voff), "s"(sbase), "s"(lds) : "memory");
}
__device__ __forceinline__ void dma16s(unsigned voff, const char* sbase, unsigned lds) {
  asm volatile("s_mov_b32 m0, %2\n\ts_nop 0\n\tglobal_load_lds_dwordx4 %0, %1 sc1"
               ::"v"(voff), "s"(sbase), "s"(lds) : "memory");
}
#define LD1(p) __builtin_nontemporal_load(p)
__device__ __forceinline__ void dma16h(unsigned voff, const char* sbase, unsigned lds) {
  asm volatile("s_mov_b32 m0, %2\n\ts_mov_b32 exec_hi, 0\n\tglobal_load_lds_dwordx4 %0, %1\n\ts_mov_b32 exec_hi, -1"
               ::"v"(voff), "s"(sbase), "s"(lds) : "memory");
}
__device__ __forceinline__ unsigned lds_off(const void* p) {
  return (unsigned)(uintptr_t)(__attribute__((address_space(3))) const char*)p;
}
#define SB0() __builtin_amdgcn_sched_barrier(0)
template <int N> __device__ __forceinline__ void wait_vm_lgkm0() {
  asm volatile("s_waitcnt vmcnt(%0) lgkmcnt(0)" ::"n"(N) : "memory");
}
__device__ __forceinline__ void bar() {
  asm volatile("" ::: "memory");
  __builtin_amdgcn_s_barrier();
  asm volatile("" ::: "memory");
}
__device__ __forceinline__ bf16x8 lds_frag(const char* p) { return *(const bf16x8*)p; }
__device__ __forceinline__ s16x4 lds_tr(const char* p) {
  return __builtin_amdgcn_ds_read_tr16_b64_v4i16((lds_s16x4_ptr)p);
}
__device__ __forceinline__ bf16x8 cat_frag(s16x4 a, s16x4 b) {
  s16x8 v = __builtin_shufflevector(a, b, 0, 1, 2, 3, 4, 5, 6, 7);
  return __builtin_bit_cast(bf16x8, v);
}
#define MFMA16(a, b, c) __builtin_amdgcn_mfma_f32_16x16x32_bf16((a), (b), (c), 0, 0, 0)

template <class Sc, int POS, int NM, int NR, int DL, class FM, class FR>
__device__ __forceinline__ void run_sched(FM& fm, FR& fr) {
  if constexpr (Sc::s[POS] != 0) {
    constexpr char c = Sc::s[POS];
    if constexpr (c == 'M') {
      fm(IC<NM>{}); SB0();
      run_sched<Sc, POS + 1, NM + 1, NR, DL>(fm, fr);
    } else {
      if constexpr (DL) { fr(IC<NR>{}); SB0(); }
      run_sched<Sc, POS + 1, NM, NR + 1, DL>(fm, fr);
    }
  }
}
template <int N> __device__ __forceinline__ void wait_vm() { asm volatile("s_waitcnt vmcnt(%0)" ::"n"(N) : "memory"); }
__device__ __forceinline__ void wait_lgkm0() { asm volatile("s_waitcnt lgkmcnt(0)" ::: "memory"); }

template <int NS, int P, int TI>
__device__ __forceinline__ void prod_tail() {
  if constexpr (TI < NS - 1) {
    wait_vm<P*(NS - 2 - TI)>();
    bar();
    prod_tail<NS, P, TI + 1>();
  }
}
constexpr int ramp_A(int j, int NS) {
  int a = 2;
  for (int i = 1; i <= j; ++i) { int lim = (i == 1) ? NS : (i - 1) + NS; a = (a + 2 < lim) ? a + 2 : lim; }
  return a;
}
constexpr int ramp_len(int NS) { int j = 1; while (ramp_A(j, NS) != ((j == 1) ? NS : (j - 1) + NS) || j < 2) ++j; return j; }
template <int NS, int P, int J, int R, class Issue>
__device__ __forceinline__ void prod_ramp(Issue& issue) {
  if constexpr (J < R) {
    constexpr int a0 = (J == 0) ? 0 : ramp_A(J - 1, NS), a1 = ramp_A(J, NS);
#pragma unroll
    for (int s = a0; s < a1; ++s) issue(s, s % NS);
    wait_vm<P*(a1 - (J + 1))>();
    bar();
    prod_ramp<NS, P, J + 1, R>(issue);
  }
}
template <int NS, int NK, int P, class Issue>
__device__ __forceinline__ void producer(Issue issue) {
  constexpr int R = ramp_len(NS);
  static_assert(R - 1 + NS <= NK, "ramp too long");
  prod_ramp<NS, P, 0, R>(issue);
#pragma unroll
  for (int s = ramp_A(R - 1, NS); s < R - 1 + NS; ++s) issue(s, s % NS);
  int wr = (R - 1) % NS;
#pragma nounroll
  for (int i = R - 1; i < NK - NS; ++i) {
    wait_vm<P*(NS - 2)>();
    bar();
    issue(i + NS, wr);
    wr = (wr + 1 == NS) ? 0 : wr + 1;
  }
  prod_tail<NS, P, 0>();
}
#define NO_SBAR
#define MAP84
#define PFD 2
template <int NK, int PR, int PC, class IR, class ICC, class Mid>
__device__ __forceinline__ void producer_seam(IR issueR, ICC issueC, Mid mid) {
  constexpr int P = PR + PC;
  issueR(0, 0); issueR(1, 1);
  wait_vm<PR>(); bar();
  issueR(2, 2); issueC(2, 2); issueR(3, 3); issueC(3, 3);
  wait_vm<2 * P>(); bar();
  mid();
  constexpr int J0 = 2;
#pragma nounroll
  for (int j = J0; j <= NK - 3; ++j) {
    issueR(j + 2, (j + 2) & 3); issueC(j + 2, (j + 2) & 3);
    wait_vm<2 * P>(); bar();
  }
  wait_vm<P>(); bar();
  wait_vm<0>(); bar();
}
template <int NS, int NK, class Load0, class Body>
__device__ __forceinline__ void consumer(Load0 load0, Body body) {
  static_assert((NK & 1) == 0, "even number of stages");
  bar();
  load0();
  int rd = 1;
#pragma nounroll
  for (int i = 0; i < NK - 2; i += 2) {
    wait_lgkm0();
    bar();
    SB0();
    body(IC<0>{}, IC<1>{}, rd);
    SB0();
    rd = (rd + 1 == NS) ? 0 : rd + 1;
    wait_lgkm0();
    bar();
    SB0();
    body(IC<1>{}, IC<1>{}, rd);
    SB0();
    rd = (rd + 1 == NS) ? 0 : rd + 1;
  }
  wait_lgkm0();
  bar();
  SB0();
  body(IC<0>{}, IC<1>{}, rd);
  SB0();
  body(IC<1>{}, IC<0>{}, 0);
  SB0();
}
template <int NK, class Load0, class Half>
__device__ __forceinline__ void consumer_half(Load0 load0, Half half) {
  bar();
  load0();
#pragma nounroll
  for (int s = 0; s < NK - 1; ++s) {
    SB0();
    half(IC<0>{}, IC<1>{}, s & 3);
    SB0();
    wait_lgkm0();
    bar();
    SB0();
    half(IC<1>{}, IC<1>{}, (s + 1) & 3);
    SB0();
  }
  half(IC<0>{}, IC<1>{}, (NK - 1) & 3);
  SB0();
  half(IC<1>{}, IC<0>{}, 0);
  SB0();
}
typedef __attribute__((address_space(1))) unsigned gu32;
__device__ __forceinline__ void st16_wt(void* p, u32x4 v) {
  asm volatile("global_store_dwordx4 %0, %1, off sc1\n\ts_nop 1" ::"v"(p), "v"(v) : "memory");
}
__device__ __forceinline__ void st8u_wt(void* p, u32x2 v) {
  asm volatile("global_store_dwordx2 %0, %1, off sc1\n\ts_nop 1" ::"v"(p), "v"(v) : "memory");
}
__device__ __forceinline__ void st8_wt(void* p, f32x2 v) {
  asm volatile("global_store_dwordx2 %0, %1, off sc1\n\ts_nop 1" ::"v"(p), "v"(v) : "memory");
}
__device__ __forceinline__ void wait_count(char* ws, int idx, unsigned need) {
  gu32* cnt = (gu32*)(ws + WS_CNT + idx * 256);
#pragma nounroll
  for (unsigned spins = 0; spins < (1u << 22); ++spins) {
    if ((unsigned)__builtin_amdgcn_readfirstlane(__hip_atomic_load(cnt, __ATOMIC_RELAXED, __HIP_MEMORY_SCOPE_AGENT)) >= need) break;
    __builtin_amdgcn_s_sleep(1);
  }
  asm volatile("" ::: "memory");
}
__device__ __forceinline__ void add_count(char* ws, int idx) {
  __hip_atomic_fetch_add((gu32*)(ws + WS_CNT + idx * 256), 1u, __ATOMIC_RELAXED, __HIP_MEMORY_SCOPE_AGENT);
}
__device__ __forceinline__ int swap23(int r) { return (r & ~12) | ((r & 4) << 1) | ((r & 8) >> 1); }

#define WPF_KB 4
__global__ __launch_bounds__(256) void k1_ln_mod(const unsigned short* __restrict__ x,
                                                 const unsigned short* __restrict__ c,
                                                 const unsigned short* __restrict__ wmod,
                                                 const unsigned short* __restrict__ wre,
                                                 const unsigned short* __restrict__ wim,
                                                 const unsigned short* __restrict__ w2,
                                                 const unsigned short* __restrict__ dre,
                                                 const unsigned short* __restrict__ dim,
                                                 const unsigned short* __restrict__ w1,
                                                 const unsigned short* __restrict__ outp,
                                                 char* __restrict__ ws) {
  __shared__ __attribute__((aligned(16))) char smem[16 * 1296 + 256];
  const int tid = threadIdx.x, lane = tid & 63;
  const int bid = blockIdx.x;
  if (bid < 128) {
    const int xcd = bid & 7, i = bid >> 3;
    const int rg = xcd * 4 + (i >> 2), sub = i & 3;
    const int row0 = rg * 64 + sub * 16;
    const int r = tid >> 4, j = tid & 15;
    const u32x4* src = (const u32x4*)(x + (size_t)(row0 + r) * D_);
    u32x4 v[5];
#pragma unroll
    for (int q = 0; q < 5; ++q) v[q] = LD1(src + j + 16 * q);
    float f[40];
#pragma unroll
    for (int q = 0; q < 5; ++q)
#pragma unroll
      for (int e = 0; e < 4; ++e) { f[q * 8 + 2 * e] = lo16(v[q][e]); f[q * 8 + 2 * e + 1] = hi16(v[q][e]); }
    float s = 0.f;
#pragma unroll
    for (int e = 0; e < 40; ++e) s += f[e];
#pragma unroll
    for (int o = 1; o < 16; o <<= 1) s += __shfl_xor(s, o, 64);
    const float mean = s * (1.0f / D_);
    float s2 = 0.f;
#pragma unroll
    for (int e = 0; e < 40; ++e) { float d = f[e] - mean; s2 += d * d; }
#pragma unroll
    for (int o = 1; o < 16; o <<= 1) s2 += __shfl_xor(s2, o, 64);
    const float mb = bfr(mean);
    const float vb = bfr(s2 * (1.0f / D_));
    const float rden = 1.0f / bfr(sqrtf(bfr(vb + 1e-5f)));
#pragma unroll
    for (int q = 0; q < 5; ++q) {
      u32x4 o;
#pragma unroll
      for (int e = 0; e < 4; ++e) {
        float a = bfr(f[q * 8 + 2 * e] - mb) * rden;
        float b = bfr(f[q * 8 + 2 * e + 1] - mb) * rden;
        o[e] = pk2(a, b);
      }
      *(u32x4*)(smem + r * 1296 + (j + 16 * q) * 16) = o;
    }
    wait_lgkm0();
    bar();
    const int grp = tid >> 4, li = tid & 15, qq = li >> 2, pp = li & 3;
    char* ht = ws + WS_HT + (size_t)(row0 >> 4) * 2560;
#pragma unroll
    for (int it = 0; it < 5; ++it) {
      const int pr = it * 16 + grp;
      const int cblk = pr >> 1, half = pr & 1;
      const char* a0 = smem + (8 * half + qq) * 1296 + (16 * cblk + 4 * pp) * 2;
      s16x4 t0 = lds_tr(a0);
      s16x4 t1 = lds_tr(a0 + 4 * 1296);
      s16x8 o = __builtin_shufflevector(t0, t1, 0, 1, 2, 3, 4, 5, 6, 7);
      const int d = 16 * cblk + li;
      const int cb = d / 80, rr = d - cb * 80;
      *(s16x8*)(ht + (size_t)cb * (128 * 2560) + rr * 32 + half * 16) = o;
    }
    if (bid == 0 && tid < 96) *(u32x4*)(ws + WS_E + 2560 + tid * 16) = (u32x4){0u, 0u, 0u, 0u};
    if (bid == 1 && tid < 48) *(unsigned*)(ws + WS_CNT + tid * 256) = 0u;
  } else if (bid < 368) {
    const int g = bid - 128;
    const int cgp = (g & 7) * 30 + (g >> 3);
    const int l2 = tid & 1, rg = tid >> 1;
    u32x4 v[5];
    unsigned short cv16[5];
#pragma unroll
    for (int q = 0; q < 5; ++q) {
      const int k = q * 128 + rg;
      v[q] = *(const u32x4*)(wmod + (size_t)k * MODN + cgp * 16 + l2 * 8);
      cv16[q] = c[k];
    }
    float acc[8];
#pragma unroll
    for (int e = 0; e < 8; ++e) acc[e] = 0.f;
#pragma unroll
    for (int q = 0; q < 5; ++q) {
      const float cv = bf2f(cv16[q]);
      const float sv = bfr(cv / (1.0f + __expf(-cv)));
#pragma unroll
      for (int e = 0; e < 4; ++e) {
        acc[2 * e] += sv * lo16(v[q][e]);
        acc[2 * e + 1] += sv * hi16(v[q][e]);
      }
    }
#pragma unroll
    for (int o = 2; o < 64; o <<= 1)
#pragma unroll
      for (int e = 0; e < 8; ++e) acc[e] += __shfl_xor(acc[e], o, 64);
    float* red = (float*)smem;
    if (lane < 2) {
#pragma unroll
      for (int e = 0; e < 8; ++e) red[(tid >> 6) * 16 + lane * 8 + e] = acc[e];
    }
    wait_lgkm0();
    bar();
    if (tid < 16) {
      float m = bfr((red[tid] + red[16 + tid]) + (red[32 + tid] + red[48 + tid]));
      const int n = cgp * 16 + tid;
      const int part = n / D_, d = n - part * D_;
      if (part == 1 || part == 4) m = bfr(1.0f + m);
      ((float*)(ws + WS_MOD))[n] = m;
      if (part == 4) ((unsigned short*)(ws + WS_E))[d] = (unsigned short)(__float_as_uint(m) >> 16);
      if (part == 3) ((unsigned short*)(ws + WS_E))[D_ + d] = (unsigned short)(__float_as_uint(m) >> 16);
    }
  } else if (bid < 464) {
    const int k0 = (bid - 368) * 16;
    u32x4 v[5];
#pragma unroll
    for (int q = 0; q < 5; ++q) v[q] = LD1((const u32x4*)((const char*)w2 + (size_t)k0 * (D_ * 2) + (tid + 256 * q) * 16));
#pragma unroll
    for (int q = 0; q < 5; ++q) {
      const int id = tid + 256 * q;
      const int r = id / 80, ch = id - r * 80;
      const int cb = ch / 10, cc = ch - cb * 10;
      *(u32x4*)(ws + WS_W2T + ((size_t)cb * F_ + k0 + swap23(r)) * 160 + cc * 16) = v[q];
    }
  } else if (bid < 560) {
    const int c0 = (bid - 464) * 1280;
    u32x4 v[5];
#pragma unroll
    for (int q = 0; q < 5; ++q) v[q] = LD1((const u32x4*)((const char*)w1 + (size_t)(c0 + tid + 256 * q) * 16));
#pragma unroll
    for (int q = 0; q < 5; ++q) {
      const int id = c0 + tid + 256 * q;
      const int k = id / 192, ch = id - k * 192;
      const int cb = ch / 12, cc = ch - cb * 12;
      const int r = swap23(k);
      *(u32x4*)(ws + WS_W1T + ((size_t)cb * D_ + r) * 192 + (cc ^ (((r >> 2) & 1) << 1)) * 16) = v[q];
    }
  } else if (bid < 640) {
    const int m = bid >= 600, c0 = (bid - 560 - 40 * m) * 1280;
    u32x4 v[5];
#pragma unroll
    for (int q = 0; q < 5; ++q) v[q] = LD1((const u32x4*)((const char*)(m ? dim : dre) + (size_t)(c0 + tid + 256 * q) * 16));
#pragma unroll
    for (int q = 0; q < 5; ++q) {
      const int id = c0 + tid + 256 * q;
      const int e = id / 80, ch = id - e * 80;
      const int cb = e / 80, row = e - cb * 80;
      *(u32x4*)(ws + WS_WDT + ((size_t)(cb * 20 + m * 10 + (ch >> 3)) * 80 + row) * 128 + ((ch & 7) ^ ((row >> 1) & 7)) * 16) = v[q];
    }
  } else if (bid >= 640 + 64 * WPF_KB) {
    const int t = ((bid - 640 - 64 * WPF_KB) >> 3) * 256 + tid;
    const char* a;
    if (t < 384) a = ws + (size_t)t * 65536;
    else if (t < 512) a = (const char*)wre + (size_t)(t - 384) * 65536;
    else if (t < 640) a = (const char*)wim + (size_t)(t - 512) * 65536;
    else if (t < 680) a = (const char*)x + (size_t)(t - 640) * 65536;
    else if (t < 720) a = (const char*)outp + (size_t)(t - 680) * 65536;
    else a = ws;
    unsigned v;
    asm volatile("global_load_dword %0, %1, off" : "=v"(v) : "v"(a) : "memory");
    wait_vm<0>();
    asm volatile("" ::"v"(v));
  } else {
    const int pw = (bid - 640) * 4 + __builtin_amdgcn_readfirstlane(tid >> 6);
    const char* src = (const char*)(pw < 512 ? wre : wim) + (size_t)(pw & 511) * 16384 + lane * 16;
    u32x4 v[16];
#pragma unroll
    for (int i = 0; i < 16; ++i) v[i] = *(const u32x4*)(src + i * 1024);
    float rs[4];
#pragma unroll
    for (int r = 0; r < 4; ++r) {
      float a0 = 0.f, a1 = 0.f;
#pragma unroll
      for (int q = 0; q < 4; ++q)
#pragma unroll
        for (int e = 0; e < 4; ++e) { a0 += lo16(v[4 * r + q][e]); a1 += hi16(v[4 * r + q][e]); }
      rs[r] = a0 + a1;
    }
#pragma unroll
    for (int o = 1; o < 64; o <<= 1)
#pragma unroll
      for (int r = 0; r < 4; ++r) rs[r] += __shfl_xor(rs[r], o, 64);
    if (lane == 0) *(f32x4*)(ws + WS_RSUM + pw * 16) = (f32x4){rs[0], rs[1], rs[2], rs[3]};
  }
}

__device__ __forceinline__ void dma_res_tile(const char* base  , unsigned lds, int w, int lane) {
#pragma unroll
  for (int j = 0; j < 3; ++j) {
    int L = 160 * w + 64 * j + lane;
    if (L > 639) L = 639;
    const int r = L / 10, ch = L - r * 10;
    if (j < 2) dma16(r * (D_ * 2) + ch * 16, base, lds + (160 * w + 64 * j) * 16);
    else dma16h(r * (D_ * 2) + ch * 16, base, lds + (160 * w + 128) * 16);
  }
}
template <int N>
__device__ __forceinline__ void l2_warm(const char* base, unsigned dummy_lds, int lane) {
#pragma unroll
  for (int i = 0; i < N; ++i) dma16(lane * 16, base + i * 1024, dummy_lds);
}

struct SchedP2 { static constexpr char s[] = "MRMRMRMMRMRMRMMRMRMRMMMMMMMMM"; };
struct SchedP3 { static constexpr char s[] = "RRMRMRRMRMRRMRMRRMRMMM"; };
struct SchedP4 { static constexpr char s[] = "MRMRMRMMRMRMRMMRMRMMRMRMRMMRMRMRMMRMRMMMMMMMMM"; };
constexpr int cnt_ch(const char* s, char c) { int n = 0; for (; *s; ++s) n += (*s == c); return n; }
static_assert(cnt_ch(SchedP2::s, 'M') == 20 && cnt_ch(SchedP2::s, 'R') == 9, "");
static_assert(cnt_ch(SchedP3::s, 'M') == 10 && cnt_ch(SchedP3::s, 'R') == 12, "");
static_assert(cnt_ch(SchedP4::s, 'M') == 30 && cnt_ch(SchedP4::s, 'R') == 16, "");
constexpr int L_RES = 147456, L_MOD = 159744, L_DUM = L_MOD + 2048, L_TOTAL = L_DUM + 1024;
#define P2NS 5
constexpr int P2_NS = P2NS, P2_STAGE = 26624, P2_X = 73728;
constexpr int P3_STAGE = 36864;
constexpr int P4_STAGE = 28672, P4_E = 4 * P4_STAGE, P4_ST = P4_E + 4096, P4_RS = P4_ST + 8192, P4_STG = 2 * P4_STAGE;

__global__ __launch_bounds__(512, 2) void mega(const unsigned short* __restrict__ wre,
                                               const unsigned short* __restrict__ wim,
                                               const unsigned short* __restrict__ x,
                                               char* __restrict__ ws,
                                               unsigned short* __restrict__ out) {
  STAMP_DECL
  STAMP(0);
  extern __shared__ __attribute__((aligned(16))) char smem[];
  const int tid = threadIdx.x, lane = tid & 63;
  const int wv = __builtin_amdgcn_readfirstlane(tid >> 6);
  const int w = wv & 3;
  const bool prod = wv < 4;
  const int bid = blockIdx.x;
  const int xcd = bid & 7, bi = bid >> 3;
  const int cb = bi & 7, rsel = bi >> 3;
  const int g = lane >> 4, l15 = lane & 15;
  const unsigned lds0 = lds_off(smem);
  const float* mod = (const float*)(smem + L_MOD);
  const int cb3 = (xcd & 1) * 4 + (bi & 3);
  const int t3 = ((xcd >> 1) * 8 + (bi >> 2)) * 64;
  const int cb4 = (xcd & 1) * 8 + (bi & 7), grp4 = (xcd >> 1) * 4 + (bi >> 3);
  const int grp3 = t3 >> 7;

  {
    const int isim = rsel >> 1;
    const int grp2 = xcd * 2 + (rsel & 1);
    const int t0 = grp2 * 128;
    if (prod) {
      const char* rbase = (const char*)(isim ? wim : wre) + (size_t)t0 * (S_ * 2);
      const char* cbase = ws + WS_HT + (size_t)cb * 32 * 10240;
      unsigned roff[4];
#pragma unroll
      for (int j = 0; j < 4; ++j) {
        const int row = 32 * w + 8 * j + (lane >> 3);
        roff[j] = row * (S_ * 2) + 16 * ((lane & 7) ^ ((row >> 1) & 7));
      }
      auto run = [&](auto ncc) {
        constexpr int NC = decltype(ncc)::value;
        const int c0 = (NC == 3) ? w * 3072 : 6144 + (w - 2) * 2048;
        producer<P2_NS, 32, 4 + NC>([&](int ks, int buf) {
          const unsigned lb = lds0 + buf * P2_STAGE;
#pragma unroll
          for (int j = 0; j < 4; ++j) dma16(roff[j], rbase + ks * 128, lb + (32 * w + 8 * j) * 128);
#pragma unroll
          for (int j = 0; j < NC; ++j) dma16(lane * 16, cbase + ks * 10240 + c0 + j * 1024, lb + 16384 + c0 + j * 1024);
        });
      };
      if (w < 2) run(IC<3>{}); else run(IC<2>{});
      bar();
      const char* wdt = ws + WS_WDT + (size_t)cb3 * 204800;
#pragma unroll
      for (int s = 0; s < PFD; ++s)
#pragma unroll
        for (int j = 0; j < 5; ++j)
          dma16(lane * 16, wdt + s * 20480 + w * 5120 + j * 1024, lds0 + s * P3_STAGE + 16384 + w * 5120 + j * 1024);
    } else {
      if (w < 2) {
        int L = 64 * w + lane; if (L > 99) L = 99;
        const int vec = L / 20, part = vec + (vec >= 3);
        dma16(part * 2560 + (vec < 2 ? cb : cb3) * 320 + (L - vec * 20) * 16, ws + WS_MOD, lds0 + L_MOD + w * 1024);
      }
      dma_res_tile((const char*)x + (size_t)t3 * (D_ * 2) + cb3 * 160, lds0 + L_RES, w, lane);
      const int wm = w & 1, wk = w >> 1;
      const int fsw = ((4 * wk + g) ^ (l15 >> 1)) * 16;
      const int ra = (64 * wm + l15) * 128 + fsw;
      const int ca_ = 16384 + (2 * wk + (g >> 1)) * 2560 + l15 * 32 + (g & 1) * 16;
      bf16x8 rf[2][4], cf[2][5];
      f32x4 acc[5][4];
#pragma unroll
      for (int j = 0; j < 5; ++j)
#pragma unroll
        for (int i = 0; i < 4; ++i) acc[j][i] = (f32x4){0.f, 0.f, 0.f, 0.f};
      const float sgn = isim ? -1.0f : 1.0f;
      const float* rsum = (const float*)(ws + WS_RSUM) + isim * 2048 + t0 + 64 * wm + 32 * wk + l15;
      const float rs0 = rsum[0] * sgn, rs1 = rsum[16] * sgn;
      auto ldf = [&](auto par, auto ic, int buf) {
        constexpr int PP = decltype(par)::value, n = decltype(ic)::value;
        const char* lb = smem + buf * P2_STAGE;
        if constexpr (n == 0) rf[PP][0] = lds_frag(lb + ra);
        else if constexpr (n < 6) cf[PP][n - 1] = lds_frag(lb + ca_ + (n - 1) * 512);
        else rf[PP][n - 5] = lds_frag(lb + ra + (n - 5) * 2048);
      };
      auto body = [&](auto par, auto dl, int rd) {
        constexpr int PP = decltype(par)::value, DL = decltype(dl)::value;
        auto fm = [&](auto mc) {
          constexpr int m = decltype(mc)::value, i = m / 5, j = m % 5;
          acc[j][i] = MFMA16(cf[PP][j], rf[PP][i], acc[j][i]);
        };
        auto fr = [&](auto rc) { ldf(IC<PP ^ 1>{}, rc, rd); };
        run_sched<SchedP2, 0, 0, 0, DL>(fm, fr);
      };
      auto load0 = [&] { static_for<9>([&](auto ic) { ldf(IC<0>{}, ic, 0); }); };
      consumer<P2_NS, 32>(load0, body);
      STAMP(1);
      auto finish = [&](auto keepc) {
        constexpr int KP = decltype(keepc)::value;
        char* dst = smem + P2_X + w * 10752 + lane * 16;
#pragma unroll
        for (int j = 0; j < 5; ++j) {
          *(f32x4*)(dst + (2 * j) * 1024) = acc[j][2 * (KP ^ 1)];
          *(f32x4*)(dst + (2 * j + 1) * 1024) = acc[j][2 * (KP ^ 1) + 1];
        }
        wait_vm<0>();
        wait_lgkm0();
        bar();
        const char* srcp = smem + P2_X + (w ^ 2) * 10752 + lane * 16;
        char* pq = ws + WS_PQ + ((size_t)(grp2 * 2 + wm) * 80 + isim * 40 + cb * 5) * 2048 + (32 * KP + l15) * 32 + g * 8;
        f32x4 p0[5], p1[5], sa[5], c1[5];
#pragma unroll
        for (int j = 0; j < 5; ++j) {
          p0[j] = *(const f32x4*)(srcp + (2 * j) * 1024);
          p1[j] = *(const f32x4*)(srcp + (2 * j + 1) * 1024);
          sa[j] = *(const f32x4*)(mod + 16 * j + 4 * g);
          c1[j] = *(const f32x4*)(mod + 80 + 16 * j + 4 * g);
        }
        u32x2 o0[5], o1[5];
#pragma unroll
        for (int j = 0; j < 5; ++j) {
          const f32x4 cs = c1[j] * sgn;
          const f32x4 v0 = cs * (acc[j][2 * KP] + p0[j]) + sa[j] * rs0;
          const f32x4 v1 = cs * (acc[j][2 * KP + 1] + p1[j]) + sa[j] * rs1;
          o0[j] = (u32x2){pk2(v0[0], v0[1]), pk2(v0[2], v0[3])};
          o1[j] = (u32x2){pk2(v1[0], v1[1]), pk2(v1[2], v1[3])};
        }
#pragma unroll
        for (int j = 0; j < 5; ++j) {
          st8u_wt(pq + j * 2048, o0[j]);
          st8u_wt(pq + j * 2048 + 512, o1[j]);
        }
      };
      if (wk == 0) finish(IC<0>{}); else finish(IC<1>{});
    }
    STAMP(2);
    if (!prod) wait_vm<0>();
    bar();
    STAMP(3);
    if (wv == 4) {
      if (lane == 0) add_count(ws, grp2);
      wait_count(ws, grp3, 16);
    }
    bar();
  }

  {
    const char* wdt = ws + WS_WDT + (size_t)cb3 * 204800;
    if (prod) {
      const char* rbase = ws + WS_PQ + (size_t)(t3 >> 6) * (80 * 2048) + w * 4096;
      STAMP(4);
      producer_seam<10, 4, 5>([&](int ks, int buf) {
#pragma unroll
        for (int j = 0; j < 4; ++j) dma16s(lane * 16, rbase + ks * 16384 + j * 1024, lds0 + buf * P3_STAGE + w * 4096 + j * 1024);
      }, [&](int ks, int buf) {
#pragma unroll
        for (int j = 0; j < 5; ++j)
          dma16(lane * 16, wdt + ks * 20480 + w * 5120 + j * 1024, lds0 + buf * P3_STAGE + 16384 + w * 5120 + j * 1024);
      }, [] {});
      bar();
      const char* w1t = ws + WS_W1T + (size_t)cb4 * 122880;
      dma16(w * 1024 + lane * 16, ws + WS_E, lds0 + P4_E + w * 1024);
#pragma unroll
      for (int s = 0; s < PFD; ++s)
#pragma unroll
        for (int j = 0; j < 3; ++j)
          dma16(lane * 16, w1t + s * 12288 + w * 3072 + j * 1024, lds0 + s * P4_STAGE + 16384 + w * 3072 + j * 1024);
    } else {
      const int sw0 = (g ^ (l15 >> 1)) * 16, sw1 = ((4 + g) ^ (l15 >> 1)) * 16;
      const int ra = (g >> 1) * 2048 + (16 * w + l15) * 32 + (g & 1) * 16, ca_ = 16384 + l15 * 128;
      bf16x8 rf[2][2], cf[2][2][5];
      f32x4 acc[5];
#pragma unroll
      for (int j = 0; j < 5; ++j) acc[j] = (f32x4){0.f, 0.f, 0.f, 0.f};
      auto ldf = [&](auto hc, auto ic, int buf) {
        constexpr int H = decltype(hc)::value, n = decltype(ic)::value, kk = n / 6, m = n % 6;
        const char* lb = smem + buf * P3_STAGE;
        if constexpr (m == 0) rf[H][kk] = lds_frag(lb + ra + H * 8192 + kk * 4096);
        else cf[H][kk][m - 1] = lds_frag(lb + (kk ? sw1 : sw0) + ca_ + H * 10240 + (m - 1) * 2048);
      };
      auto half = [&](auto par, auto dl, int buf) {
        constexpr int PP = decltype(par)::value, DL = decltype(dl)::value;
        auto fm = [&](auto mc) {
          constexpr int m = decltype(mc)::value, kk = m / 5, j = m % 5;
          acc[j] = MFMA16(cf[PP][kk][j], rf[PP][kk], acc[j]);
        };
        auto fr = [&](auto rc) { ldf(IC<PP ^ 1>{}, rc, buf); };
        run_sched<SchedP3, 0, 0, 0, DL>(fm, fr);
      };
      char* res = smem + L_RES + (16 * w + l15) * 160 + g * 8;
      u32x2 xr[5];
      f32x4 ga[5], c1[5];
#pragma unroll
      for (int j = 0; j < 5; ++j) {
        xr[j] = *(const u32x2*)(res + j * 32);
        ga[j] = *(const f32x4*)(mod + 160 + 16 * j + 4 * g);
        c1[j] = *(const f32x4*)(mod + 240 + 16 * j + 4 * g);
      }
      auto load0 = [&] { STAMP(4); static_for<12>([&](auto ic) { ldf(IC<0>{}, ic, 0); }); };
      consumer_half<10>(load0, half);
      STAMP(5);
      bar();
      char* xc = ws + WS_XC + ((size_t)(t3 >> 6) * 40 + cb3 * 5) * 2048 + (16 * w + l15) * 32 + g * 8;
      float xf[20];
      float s = 0.f;
#pragma unroll
      for (int j = 0; j < 5; ++j) {
        const f32x4 y = acc[j];
        const u32x2 o = {pk2(lo16(xr[j][0]) + ga[j][0] * y[0], hi16(xr[j][0]) + ga[j][1] * y[1]),
                         pk2(lo16(xr[j][1]) + ga[j][2] * y[2], hi16(xr[j][1]) + ga[j][3] * y[3])};
        xr[j] = o;
        xf[4 * j] = lo16(o[0]); xf[4 * j + 1] = hi16(o[0]); xf[4 * j + 2] = lo16(o[1]); xf[4 * j + 3] = hi16(o[1]);
        st8u_wt(xc + j * 2048, (u32x2){pk2(xf[4 * j] * c1[j][0], xf[4 * j + 1] * c1[j][1]), pk2(xf[4 * j + 2] * c1[j][2], xf[4 * j + 3] * c1[j][3])});
        s += (xf[4 * j] + xf[4 * j + 1]) + (xf[4 * j + 2] + xf[4 * j + 3]);
      }
      s += __shfl_xor(s, 16, 64);
      s += __shfl_xor(s, 32, 64);
      const float ml = s * (1.0f / 80.0f);
      float m2 = 0.f;
#pragma unroll
      for (int e = 0; e < 20; ++e) { float d = xf[e] - ml; m2 += d * d; }
      m2 += __shfl_xor(m2, 16, 64);
      m2 += __shfl_xor(m2, 32, 64);
      if (lane < 16) st8_wt(ws + WS_ST + ((size_t)(t3 >> 6) * 8 + cb3) * 512 + (16 * w + l15) * 8, (f32x2){s, m2});
#pragma unroll
      for (int j = 0; j < 5; ++j) *(u32x2*)(res + j * 32) = xr[j];
      STAMP(6);
      wait_vm<0>();
    }
    bar();
    STAMP(7);
    if (wv == 4) {
      if (lane == 0) add_count(ws, 16 + grp3);
      wait_count(ws, 16 + grp4, 16);
    }
    bar();
  }

  {
    const int t0 = grp4 * 128;
    const char* w1t = ws + WS_W1T + (size_t)cb4 * 122880;
    if (prod) {
      const char* rbase = ws + WS_XC + (size_t)(grp4 * 2 + (w >> 1)) * (40 * 2048) + (w & 1) * 4096;
      STAMP(8);
      dma16s(w * 2048 + lane * 16, ws + WS_ST + (size_t)t0 * 64, lds0 + P4_ST + w * 2048);
      dma16s(w * 2048 + 1024 + lane * 16, ws + WS_ST + (size_t)t0 * 64, lds0 + P4_ST + w * 2048 + 1024);
      producer_seam<10, 4, 3>([&](int ks, int buf) {
#pragma unroll
        for (int j = 0; j < 4; ++j) dma16s(lane * 16, rbase + ks * 8192 + j * 1024, lds0 + buf * P4_STAGE + w * 4096 + j * 1024);
      }, [&](int ks, int buf) {
#pragma unroll
        for (int j = 0; j < 3; ++j)
          dma16(lane * 16, w1t + ks * 12288 + w * 3072 + j * 1024, lds0 + buf * P4_STAGE + 16384 + w * 3072 + j * 1024);
      }, [&] {
        if (tid < 128) {
          const char* sp = smem + P4_ST + (tid >> 6) * 4096 + (tid & 63) * 8;
          f32x2 pp[8];
#pragma unroll
          for (int e = 0; e < 8; ++e) pp[e] = *(const f32x2*)(sp + e * 512);
          const float mean = (((pp[0][0] + pp[1][0]) + (pp[2][0] + pp[3][0])) + ((pp[4][0] + pp[5][0]) + (pp[6][0] + pp[7][0]))) * (1.0f / D_);
          float m2 = ((pp[0][1] + pp[1][1]) + (pp[2][1] + pp[3][1])) + ((pp[4][1] + pp[5][1]) + (pp[6][1] + pp[7][1]));
          float dd = 0.f;
#pragma unroll
          for (int e = 0; e < 8; ++e) { const float d = pp[e][0] * (1.0f / 80.0f) - mean; dd += d * d; }
          m2 += 80.0f * dd;
          const float rden = 1.0f / bfr(sqrtf(bfr(bfr(m2 * (1.0f / D_)) + 1e-5f)));
          *(f32x2*)(smem + P4_RS + tid * 8) = (f32x2){bfr(mean), rden};
        }
        wait_lgkm0();
      });
      bar();
      const char* w2t = ws + WS_W2T + (size_t)cb3 * F_ * 160;
#pragma unroll
      for (int s = 0; s < PFD; ++s)
#pragma unroll
        for (int j = 0; j < 5; ++j)
          dma16(lane * 16, w2t + s * 20480 + w * 5120 + j * 1024, lds0 + s * P3_STAGE + 16384 + w * 5120 + j * 1024);
    } else {
      const int q = l15 >> 2, p4 = lane & 3;
      const int wm = w & 1, wn = w >> 1;
      const int rpos = 16 * (g >> 1) + 4 * (g & 1) + q;
      const int xs = (rpos >> 2) & 1;
      int cadr[3];
#pragma unroll
      for (int j = 0; j < 3; ++j) {
        const int jj = 3 * wn + j;
        cadr[j] = 16384 + rpos * 192 + (2 * (jj ^ xs) + (p4 >> 1)) * 16 + (p4 & 1) * 8;
      }
      const int ra = wm * 8192 + (g >> 1) * 2048 + l15 * 32 + (g & 1) * 16;
      int ea = P4_E + (l15 < 2 ? l15 : 2) * 1280 + g * 16;
      bf16x8 rf[2][2][5], cf[2][2][3];
      f32x4 acc[3][5];
#pragma unroll
      for (int j = 0; j < 3; ++j)
#pragma unroll
        for (int i = 0; i < 5; ++i) acc[j][i] = (f32x4){0.f, 0.f, 0.f, 0.f};
      auto ldf = [&](auto par, auto ic, int buf) {
        constexpr int PP = decltype(par)::value, n = decltype(ic)::value, kk = n / 8, m = n % 8;
        const char* lb = smem + buf * P4_STAGE;
        if constexpr (m == 0) rf[PP][kk][0] = lds_frag(lb + ra + kk * 4096);
        else if constexpr (m < 4) cf[PP][kk][m - 1] = cat_frag(lds_tr(lb + cadr[m - 1] + kk * 6144), lds_tr(lb + cadr[m - 1] + kk * 6144 + 1536));
        else if constexpr (m < 7) rf[PP][kk][m - 3] = lds_frag(lb + ra + kk * 4096 + (m - 3) * 512);
        else {
          rf[PP][kk][4] = lds_frag(smem + ea + kk * 64);
          if constexpr (kk == 1) ea += 128;
        }
      };
      auto body = [&](auto par, auto dl, int rd) {
        constexpr int PP = decltype(par)::value, DL = decltype(dl)::value;
        auto fm = [&](auto mc) {
          constexpr int m = decltype(mc)::value, kk = m / 15, i = (m % 15) / 3, j = m % 3;
          acc[j][i] = MFMA16(cf[PP][kk][j], rf[PP][kk][i], acc[j][i]);
        };
        auto fr = [&](auto rc) { ldf(IC<PP ^ 1>{}, rc, rd); };
        run_sched<SchedP4, 0, 0, 0, DL>(fm, fr);
      };
      auto load0 = [&] { STAMP(8); static_for<16>([&](auto ic) { ldf(IC<0>{}, ic, 0); }); };
      consumer<4, 10>(load0, body);
      STAMP(9);
      bar();
      f32x4 u[3], v[3];
#pragma unroll
      for (int j = 0; j < 3; ++j)
#pragma unroll
        for (int e = 0; e < 4; ++e) {
          const int a = __float_as_int(acc[j][4][e]);
          u[j][e] = __int_as_float(__builtin_amdgcn_update_dpp(0, a, 0x150, 0xf, 0xf, false));
          v[j][e] = __int_as_float(__builtin_amdgcn_update_dpp(0, a, 0x151, 0xf, 0xf, false));
        }
      char* h1 = ws + WS_H1 + ((size_t)(grp4 * 2 + wm) * 96 + cb4 * 6 + 3 * wn) * 2048 + l15 * 32 + g * 8;
      f32x2 rsv[4];
#pragma unroll
      for (int i = 0; i < 4; ++i) rsv[i] = *(const f32x2*)(smem + P4_RS + (64 * wm + 16 * i + l15) * 8);
#pragma unroll
      for (int i = 0; i < 4; ++i) {
        const f32x2 rs = rsv[i];
#pragma unroll
        for (int j = 0; j < 3; ++j) {
          const f32x4 hv = (acc[j][i] - u[j] * rs[0]) * rs[1] + v[j];
          st8u_wt(h1 + j * 2048 + i * 512,
                  (u32x2){pk2(fmaxf(hv[0], 0.f), fmaxf(hv[1], 0.f)), pk2(fmaxf(hv[2], 0.f), fmaxf(hv[3], 0.f))});
        }
      }
      STAMP(10);
      wait_vm<0>();
    }
    bar();
    STAMP(11);
    if (wv == 4) {
      if (lane == 0) add_count(ws, 32 + grp4);
      wait_count(ws, 32 + grp3, 16);
    }
    bar();
  }

  {
    const char* w2t = ws + WS_W2T + (size_t)cb3 * F_ * 160;
    if (prod) {
      const char* rbase = ws + WS_H1 + (size_t)(t3 >> 6) * (96 * 2048) + w * 4096;
      STAMP(12);
      producer_seam<12, 4, 5>([&](int ks, int buf) {
#pragma unroll
        for (int j = 0; j < 4; ++j) dma16s(lane * 16, rbase + ks * 16384 + j * 1024, lds0 + buf * P3_STAGE + w * 4096 + j * 1024);
      }, [&](int ks, int buf) {
#pragma unroll
        for (int j = 0; j < 5; ++j)
          dma16(lane * 16, w2t + ks * 20480 + w * 5120 + j * 1024, lds0 + buf * P3_STAGE + 16384 + w * 5120 + j * 1024);
      }, [] {});
    } else {
      const int q = l15 >> 2, p4 = lane & 3;
      const int ra = (g >> 1) * 2048 + (16 * w + l15) * 32 + (g & 1) * 16;
      const int ca_ = 16384 + (16 * (g >> 1) + 4 * (g & 1) + q) * 160 + p4 * 8;
      bf16x8 rf[2][2], cf[2][2][5];
      f32x4 acc[5];
#pragma unroll
      for (int j = 0; j < 5; ++j) acc[j] = (f32x4){0.f, 0.f, 0.f, 0.f};
      auto ldf = [&](auto hc, auto ic, int buf) {
        constexpr int H = decltype(hc)::value, n = decltype(ic)::value, kk = n / 6, m = n % 6;
        const char* lb = smem + buf * P3_STAGE;
        if constexpr (m == 0) rf[H][kk] = lds_frag(lb + ra + H * 8192 + kk * 4096);
        else cf[H][kk][m - 1] = cat_frag(lds_tr(lb + ca_ + H * 10240 + kk * 5120 + (m - 1) * 32),
                                         lds_tr(lb + ca_ + H * 10240 + kk * 5120 + (m - 1) * 32 + 1280));
      };
      auto half = [&](auto par, auto dl, int buf) {
        constexpr int PP = decltype(par)::value, DL = decltype(dl)::value;
        auto fm = [&](auto mc) {
          constexpr int m = decltype(mc)::value, kk = m / 5, j = m % 5;
          acc[j] = MFMA16(cf[PP][kk][j], rf[PP][kk], acc[j]);
        };
        auto fr = [&](auto rc) { ldf(IC<PP ^ 1>{}, rc, buf); };
        run_sched<SchedP3, 0, 0, 0, DL>(fm, fr);
      };
      char* res = smem + L_RES + (16 * w + l15) * 160 + g * 8;
      u32x2 xr[5];
      f32x4 gb[5];
#pragma unroll
      for (int j = 0; j < 5; ++j) {
        xr[j] = *(const u32x2*)(res + j * 32);
        gb[j] = *(const f32x4*)(mod + 320 + 16 * j + 4 * g);
      }
      auto load0 = [&] { STAMP(12); static_for<12>([&](auto ic) { ldf(IC<0>{}, ic, 0); }); };
      consumer_half<12>(load0, half);
      STAMP(13);
#pragma unroll
      for (int j = 0; j < 5; ++j) {
        const f32x4 y = acc[j];
        *(u32x2*)(res + j * 32) = (u32x2){pk2(lo16(xr[j][0]) + gb[j][0] * y[0], hi16(xr[j][0]) + gb[j][1] * y[1]),
                                          pk2(lo16(xr[j][1]) + gb[j][2] * y[2], hi16(xr[j][1]) + gb[j][3] * y[3])};
      }
      wait_vm<0>();
    }
    wait_lgkm0();
    bar();
    for (int id = tid; id < 640; id += 512) {
      const int row = id / 10, c = id - row * 10;
      *(u32x4*)((char*)out + (size_t)(t3 + row) * (D_ * 2) + cb3 * 160 + c * 16) = *(const u32x4*)(smem + L_RES + row * 160 + c * 16);
    }
  }
  STAMP(14);
  STAMP_OUT();
}

extern "C" void kernel_launch(void* const* d_in, const int* in_sizes, int n_in,
                              void* d_out, int out_size, void* d_ws, size_t ws_size,
                              hipStream_t stream) {
  const unsigned short* x    = (const unsigned short*)d_in[0];
  const unsigned short* wsre = (const unsigned short*)d_in[1];
  const unsigned short* wsim = (const unsigned short*)d_in[2];
  const unsigned short* wdre = (const unsigned short*)d_in[3];
  const unsigned short* wdim = (const unsigned short*)d_in[4];
  const unsigned short* w1   = (const unsigned short*)d_in[6];
  const unsigned short* w2   = (const unsigned short*)d_in[7];
  const unsigned short* c    = (const unsigned short*)d_in[8];
  const unsigned short* wmod = (const unsigned short*)d_in[9];
  char* ws = (char*)d_ws;
  unsigned short* out = (unsigned short*)d_out;
  (void)hipFuncSetAttribute((const void*)mega, hipFuncAttributeMaxDynamicSharedMemorySize, L_TOTAL);
  k1_ln_mod<<<640 + 64 * WPF_KB + 24, 256, 0, stream>>>(x, c, wmod, wsre, wsim, w2, wdre, wdim, w1, out, ws);
  mega<<<256, 512, L_TOTAL, stream>>>(wsre, wsim, x, ws, out);
}
```

```cpp
#define CODE_PF
#include <hip/hip_runtime.h>
#include <hip/hip_bf16.h>
#include <stdint.h>

typedef __attribute__((ext_vector_type(8))) __bf16 bf16x8;
typedef __attribute__((ext_vector_type(4))) __bf16 bf16x4;
typedef __attribute__((ext_vector_type(2))) __bf16 bf16x2;
typedef __attribute__((ext_vector_type(4))) float f32x4;
typedef __attribute__((ext_vector_type(2))) float f32x2;
typedef __attribute__((ext_vector_type(4))) short s16x4;
typedef __attribute__((ext_vector_type(8))) short s16x8;
typedef __attribute__((ext_vector_type(4))) unsigned u32x4;
typedef __attribute__((ext_vector_type(2))) unsigned u32x2;
typedef __attribute__((address_space(3))) s16x4* lds_s16x4_ptr;
typedef __attribute__((address_space(3))) void* lds_vptr;

constexpr int S_ = 2048, D_ = 640, F_ = 1536, MODN = 3840;

constexpr size_t WS_MOD = 0;
constexpr size_t WS_E   = 16384;
constexpr size_t WS_CNT = 20480;
constexpr size_t WS_ST  = 32768;
constexpr size_t WS_RSUM = WS_ST + (size_t)S_ * 64;
constexpr size_t WS_HT  = WS_RSUM + 16384;
constexpr size_t WS_PQ  = WS_HT + (size_t)D_ * S_ * 2;
constexpr size_t WS_XC  = WS_PQ + (size_t)S_ * 1280 * 2;
constexpr size_t WS_H1  = WS_XC + (size_t)S_ * D_ * 2;
constexpr size_t WS_W2T = WS_H1 + (size_t)S_ * F_ * 2;
constexpr size_t WS_W1T = WS_W2T + (size_t)F_ * D_ * 2;
constexpr size_t WS_WDT = WS_W1T + (size_t)F_ * D_ * 2;

#define STAMP_DECL
#define STAMP(i)
#define STAMP_OUT()
template <int V> struct IC { static constexpr int value = V; };
template <int N, int I = 0, class F> __device__ __forceinline__ void static_for(F f) {
  if constexpr (I < N) { f(IC<I>{}); static_for<N, I + 1>(f); }
}

__device__ __forceinline__ float bf2f(unsigned short u) { return __uint_as_float(((unsigned)u) << 16); }
__device__ __forceinline__ float bfr(float f) { return (float)(__bf16)f; }
__device__ __forceinline__ unsigned pk2(float a, float b) {
  bf16x2 v = __builtin_convertvector((f32x2){a, b}, bf16x2);
  return __builtin_bit_cast(unsigned, v);
}
__device__ __forceinline__ float lo16(unsigned u) { return __uint_as_float(u << 16); }
__device__ __forceinline__ float hi16(unsigned u) { return __uint_as_float(u & 0xffff0000u); }

__device__ __forceinline__ void dma16(unsigned voff, const char* sbase, unsigned lds) {
  asm volatile("s_mov_b32 m0, %2\n\ts_nop 0\n\tglobal_load_lds_dwordx4 %0, %1"
               ::"v"(voff), "s"(sbase), "s"(lds) : "memory");
}
__device__ __forceinline__ void dma16nt(unsigned voff, const char* sbase, unsigned lds) {
  asm volatile("s_mov_b32 m0, %2\n\ts_nop 0\n\tglobal_load_lds_dwordx4 %0, %1 nt"
               ::"v"(voff), "s"(sbase), "s"(lds) : "memory");
}
__device__ __forceinline__ void dma16s(unsigned voff, const char* sbase, unsigned lds) {
  asm volatile("s_mov_b32 m0, %2\n\ts_nop 0\n\tglobal_load_lds_dwordx4 %0, %1 sc1"
               ::"v"(voff), "s"(sbase), "s"(lds) : "memory");
}
#define LD1(p) __builtin_nontemporal_load(p)
__device__ __forceinline__ void dma16h(unsigned voff, const char* sbase, unsigned lds) {
  asm volatile("s_mov_b32 m0, %2\n\ts_mov_b32 exec_hi, 0\n\tglobal_load_lds_dwordx4 %0, %1\n\ts_mov_b32 exec_hi, -1"
               ::"v"(voff), "s"(sbase), "s"(lds) : "memory");
}
__device__ __forceinline__ unsigned lds_off(const void* p) {
  return (unsigned)(uintptr_t)(__attribute__((address_space(3))) const char*)p;
}
#define SB0() __builtin_amdgcn_sched_barrier(0)
template <int N> __device__ __forceinline__ void wait_vm_lgkm0() {
  asm volatile("s_waitcnt vmcnt(%0) lgkmcnt(0)" ::"n"(N) : "memory");
}
__device__ __forceinline__ void bar() {
  asm volatile("" ::: "memory");
  __builtin_amdgcn_s_barrier();
  asm volatile("" ::: "memory");
}
__device__ __forceinline__ bf16x8 lds_frag(const char* p) { return *(const bf16x8*)p; }
__device__ __forceinline__ s16x4 lds_tr(const char* p) {
  return __builtin_amdgcn_ds_read_tr16_b64_v4i16((lds_s16x4_ptr)p);
}
__device__ __forceinline__ bf16x8 cat_frag(s16x4 a, s16x4 b) {
  s16x8 v = __builtin_shufflevector(a, b, 0, 1, 2, 3, 4, 5, 6, 7);
  return __builtin_bit_cast(bf16x8, v);
}
#define MFMA16(a, b, c) __builtin_amdgcn_mfma_f32_16x16x32_bf16((a), (b), (c), 0, 0, 0)

template <class Sc, int POS, int NM, int NR, int DL, class FM, class FR>
__device__ __forceinline__ void run_sched(FM& fm, FR& fr) {
  if constexpr (Sc::s[POS] != 0) {
    constexpr char c = Sc::s[POS];
    if constexpr (c == 'M') {
      fm(IC<NM>{}); SB0();
      run_sched<Sc, POS + 1, NM + 1, NR, DL>(fm, fr);
    } else {
      if constexpr (DL) { fr(IC<NR>{}); SB0(); }
      run_sched<Sc, POS + 1, NM, NR + 1, DL>(fm, fr);
    }
  }
}
template <int N> __device__ __forceinline__ void wait_vm() { asm volatile("s_waitcnt vmcnt(%0)" ::"n"(N) : "memory"); }
__device__ __forceinline__ void wait_lgkm0() { asm volatile("s_waitcnt lgkmcnt(0)" ::: "memory"); }

template <int NS, int P, int TI>
__device__ __forceinline__ void prod_tail() {
  if constexpr (TI < NS - 1) {
    wait_vm<P*(NS - 2 - TI)>();
    bar();
    prod_tail<NS, P, TI + 1>();
  }
}
constexpr int ramp_A(int j, int NS) {
  int a = 2;
  for (int i = 1; i <= j; ++i) { int lim = (i == 1) ? NS : (i - 1) + NS; a = (a + 2 < lim) ? a + 2 : lim; }
  return a;
}
constexpr int ramp_len(int NS) { int j = 1; while (ramp_A(j, NS) != ((j == 1) ? NS : (j - 1) + NS) || j < 2) ++j; return j; }
template <int NS, int P, int J, int R, class Issue>
__device__ __forceinline__ void prod_ramp(Issue& issue) {
  if constexpr (J < R) {
    constexpr int a0 = (J == 0) ? 0 : ramp_A(J - 1, NS), a1 = ramp_A(J, NS);
#pragma unroll
    for (int s = a0; s < a1; ++s) issue(s, s % NS);
    wait_vm<P*(a1 - (J + 1))>();
    bar();
    prod_ramp<NS, P, J + 1, R>(issue);
  }
}
template <int NS, int NK, int P, class Issue>
__device__ __forceinline__ void producer(Issue issue) {
  constexpr int R = ramp_len(NS);
  static_assert(R - 1 + NS <= NK, "ramp too long");
  prod_ramp<NS, P, 0, R>(issue);
#pragma unroll
  for (int s = ramp_A(R - 1, NS); s < R - 1 + NS; ++s) issue(s, s % NS);
  int wr = (R - 1) % NS;
#pragma nounroll
  for (int i = R - 1; i < NK - NS; ++i) {
    wait_vm<P*(NS - 2)>();
    bar();
    issue(i + NS, wr);
    wr = (wr + 1 == NS) ? 0 : wr + 1;
  }
  prod_tail<NS, P, 0>();
}
#define NO_SBAR
#define MAP84
#define PFD 2
template <int NK, int PR, int PC, class IR, class ICC, class Mid>
__device__ __forceinline__ void producer_seam(IR issueR, ICC issueC, Mid mid) {
  constexpr int P = PR + PC;
  issueR(0, 0); issueR(1, 1);
  wait_vm<PR>(); bar();
  issueR(2, 2); issueC(2, 2); issueR(3, 3); issueC(3, 3);
  wait_vm<2 * P>(); bar();
  mid();
  constexpr int J0 = 2;
#pragma nounroll
  for (int j = J0; j <= NK - 3; ++j) {
    issueR(j + 2, (j + 2) & 3); issueC(j + 2, (j + 2) & 3);
    wait_vm<2 * P>(); bar();
  }
  wait_vm<P>(); bar();
  wait_vm<0>(); bar();
}
template <int NS, int NK, class Load0, class Body>
__device__ __forceinline__ void consumer(Load0 load0, Body body) {
  static_assert((NK & 1) == 0, "even number of stages");
  bar();
  load0();
  int rd = 1;
#pragma nounroll
  for (int i = 0; i < NK - 2; i += 2) {
    wait_lgkm0();
    bar();
    SB0();
    body(IC<0>{}, IC<1>{}, rd);
    SB0();
    rd = (rd + 1 == NS) ? 0 : rd + 1;
    wait_lgkm0();
    bar();
    SB0();
    body(IC<1>{}, IC<1>{}, rd);
    SB0();
    rd = (rd + 1 == NS) ? 0 : rd + 1;
  }
  wait_lgkm0();
  bar();
  SB0();
  body(IC<0>{}, IC<1>{}, rd);
  SB0();
  body(IC<1>{}, IC<0>{}, 0);
  SB0();
}
template <int NK, class Load0, class Half>
__device__ __forceinline__ void consumer_half(Load0 load0, Half half) {
  bar();
  load0();
#pragma nounroll
  for (int s = 0; s < NK - 1; ++s) {
    SB0();
    half(IC<0>{}, IC<1>{}, s & 3);
    SB0();
    wait_lgkm0();
    bar();
    SB0();
    half(IC<1>{}, IC<1>{}, (s + 1) & 3);
    SB0();
  }
  half(IC<0>{}, IC<1>{}, (NK - 1) & 3);
  SB0();
  half(IC<1>{}, IC<0>{}, 0);
  SB0();
}
typedef __attribute__((address_space(1))) unsigned gu32;
__device__ __forceinline__ void st16_wt(void* p, u32x4 v) {
  asm volatile("global_store_dwordx4 %0, %1, off sc1\n\ts_nop 1" ::"v"(p), "v"(v) : "memory");
}
__device__ __forceinline__ void st8u_wt(void* p, u32x2 v) {
  asm volatile("global_store_dwordx2 %0, %1, off sc1\n\ts_nop 1" ::"v"(p), "v"(v) : "memory");
}
__device__ __forceinline__ void st8_wt(void* p, f32x2 v) {
  asm volatile("global_store_dwordx2 %0, %1, off sc1\n\ts_nop 1" ::"v"(p), "v"(v) : "memory");
}
__device__ __forceinline__ void wait_count(char* ws, int idx, unsigned need) {
  gu32* cnt = (gu32*)(ws + WS_CNT + idx * 256);
#pragma nounroll
  for (unsigned spins = 0; spins < (1u << 22); ++spins) {
    if ((unsigned)__builtin_amdgcn_readfirstlane(__hip_atomic_load(cnt, __ATOMIC_RELAXED, __HIP_MEMORY_SCOPE_AGENT)) >= need) break;
    __builtin_amdgcn_s_sleep(1);
  }
  asm volatile("" ::: "memory");
}
__device__ __forceinline__ void add_count(char* ws, int idx) {
  __hip_atomic_fetch_add((gu32*)(ws + WS_CNT + idx * 256), 1u, __ATOMIC_RELAXED, __HIP_MEMORY_SCOPE_AGENT);
}
__device__ __forceinline__ int swap23(int r) { return (r & ~12) | ((r & 4) << 1) | ((r & 8) >> 1); }

#define WPF_KB 4
__global__ __launch_bounds__(256) void k1_ln_mod(const unsigned short* __restrict__ x,
                                                 const unsigned short* __restrict__ c,
                                                 const unsigned short* __restrict__ wmod,
                                                 const unsigned short* __restrict__ wre,
                                                 const unsigned short* __restrict__ wim,
                                                 const unsigned short* __restrict__ w2,
                                                 const unsigned short* __restrict__ dre,
                                                 const unsigned short* __restrict__ dim,
                                                 const unsigned short* __restrict__ w1,
                                                 const unsigned short* __restrict__ outp,
                                                 char* __restrict__ ws) {
  __shared__ __attribute__((aligned(16))) char smem[16 * 1296 + 256];
  const int tid = threadIdx.x, lane = tid & 63;
  const int bid = blockIdx.x;
  if (bid < 128) {
    const int xcd = bid & 7, i = bid >> 3;
    const int rg = xcd * 4 + (i >> 2), sub = i & 3;
    const int row0 = rg * 64 + sub * 16;
    const int r = tid >> 4, j = tid & 15;
    const u32x4* src = (const u32x4*)(x + (size_t)(row0 + r) * D_);
    u32x4 v[5];
#pragma unroll
    for (int q = 0; q < 5; ++q) v[q] = LD1(src + j + 16 * q);
    float f[40];
#pragma unroll
    for (int q = 0; q < 5; ++q)
#pragma unroll
      for (int e = 0; e < 4; ++e) { f[q * 8 + 2 * e] = lo16(v[q][e]); f[q * 8 + 2 * e + 1] = hi16(v[q][e]); }
    float s = 0.f;
#pragma unroll
    for (int e = 0; e < 40; ++e) s += f[e];
#pragma unroll
    for (int o = 1; o < 16; o <<= 1) s += __shfl_xor(s, o, 64);
    const float mean = s * (1.0f / D_);
    float s2 = 0.f;
#pragma unroll
    for (int e = 0; e < 40; ++e) { float d = f[e] - mean; s2 += d * d; }
#pragma unroll
    for (int o = 1; o < 16; o <<= 1) s2 += __shfl_xor(s2, o, 64);
    const float mb = bfr(mean);
    const float vb = bfr(s2 * (1.0f / D_));
    const float rden = 1.0f / bfr(sqrtf(bfr(vb + 1e-5f)));
#pragma unroll
    for (int q = 0; q < 5; ++q) {
      u32x4 o;
#pragma unroll
      for (int e = 0; e < 4; ++e) {
        float a = bfr(f[q * 8 + 2 * e] - mb) * rden;
        float b = bfr(f[q * 8 + 2 * e + 1] - mb) * rden;
        o[e] = pk2(a, b);
      }
      *(u32x4*)(smem + r * 1296 + (j + 16 * q) * 16) = o;
    }
    wait_lgkm0();
    bar();
    const int grp = tid >> 4, li = tid & 15, qq = li >> 2, pp = li & 3;
    char* ht = ws + WS_HT + (size_t)(row0 >> 4) * 2560;
#pragma unroll
    for (int it = 0; it < 5; ++it) {
      const int pr = it * 16 + grp;
      const int cblk = pr >> 1, half = pr & 1;
      const char* a0 = smem + (8 * half + qq) * 1296 + (16 * cblk + 4 * pp) * 2;
      s16x4 t0 = lds_tr(a0);
      s16x4 t1 = lds_tr(a0 + 4 * 1296);
      s16x8 o = __builtin_shufflevector(t0, t1, 0, 1, 2, 3, 4, 5, 6, 7);
      const int d = 16 * cblk + li;
      const int cb = d / 80, rr = d - cb * 80;
      *(s16x8*)(ht + (size_t)cb * (128 * 2560) + rr * 32 + half * 16) = o;
    }
    if (bid == 0 && tid < 96) *(u32x4*)(ws + WS_E + 2560 + tid * 16) = (u32x4){0u, 0u, 0u, 0u};
    if (bid == 1 && tid < 48) *(unsigned*)(ws + WS_CNT + tid * 256) = 0u;
  } else if (bid < 368) {
    const int g = bid - 128;
    const int cgp = (g & 7) * 30 + (g >> 3);
    const int l2 = tid & 1, rg = tid >> 1;
    u32x4 v[5];
    unsigned short cv16[5];
#pragma unroll
    for (int q = 0; q < 5; ++q) {
      const int k = q * 128 + rg;
      v[q] = *(const u32x4*)(wmod + (size_t)k * MODN + cgp * 16 + l2 * 8);
      cv16[q] = c[k];
    }
    float acc[8];
#pragma unroll
    for (int e = 0; e < 8; ++e) acc[e] = 0.f;
#pragma unroll
    for (int q = 0; q < 5; ++q) {
      const float cv = bf2f(cv16[q]);
      const float sv = bfr(cv / (1.0f + __expf(-cv)));
#pragma unroll
      for (int e = 0; e < 4; ++e) {
        acc[2 * e] += sv * lo16(v[q][e]);
        acc[2 * e + 1] += sv * hi16(v[q][e]);
      }
    }
#pragma unroll
    for (int o = 2; o < 64; o <<= 1)
#pragma unroll
      for (int e = 0; e < 8; ++e) acc[e] += __shfl_xor(acc[e], o, 64);
    float* red = (float*)smem;
    if (lane < 2) {
#pragma unroll
      for (int e = 0; e < 8; ++e) red[(tid >> 6) * 16 + lane * 8 + e] = acc[e];
    }
    wait_lgkm0();
    bar();
    if (tid < 16) {
      float m = bfr((red[tid] + red[16 + tid]) + (red[32 + tid] + red[48 + tid]));
      const int n = cgp * 16 + tid;
      const int part = n / D_, d = n - part * D_;
      if (part == 1 || part == 4) m = bfr(1.0f + m);
      ((float*)(ws + WS_MOD))[n] = m;
      if (part == 4) ((unsigned short*)(ws + WS_E))[d] = (unsigned short)(__float_as_uint(m) >> 16);
      if (part == 3) ((unsigned short*)(ws + WS_E))[D_ + d] = (unsigned short)(__float_as_uint(m) >> 16);
    }
  } else if (bid < 464) {
    const int k0 = (bid - 368) * 16;
    u32x4 v[5];
#pragma unroll
    for (int q = 0; q < 5; ++q) v[q] = LD1((const u32x4*)((const char*)w2 + (size_t)k0 * (D_ * 2) + (tid + 256 * q) * 16));
#pragma unroll
    for (int q = 0; q < 5; ++q) {
      const int id = tid + 256 * q;
      const int r = id / 80, ch = id - r * 80;
      const int cb = ch / 10, cc = ch - cb * 10;
      *(u32x4*)(ws + WS_W2T + ((size_t)cb * F_ + k0 + swap23(r)) * 160 + cc * 16) = v[q];
    }
  } else if (bid < 560) {
    const int c0 = (bid - 464) * 1280;
    u32x4 v[5];
#pragma unroll
    for (int q = 0; q < 5; ++q) v[q] = LD1((const u32x4*)((const char*)w1 + (size_t)(c0 + tid + 256 * q) * 16));
#pragma unroll
    for (int q = 0; q < 5; ++q) {
      const int id = c0 + tid + 256 * q;
      const int k = id / 192, ch = id - k * 192;
      const int cb = ch / 12, cc = ch - cb * 12;
      const int r = swap23(k);
      *(u32x4*)(ws + WS_W1T + ((size_t)cb * D_ + r) * 192 + (cc ^ (((r >> 2) & 1) << 1)) * 16) = v[q];
    }
  } else if (bid < 640) {
    const int m = bid >= 600, c0 = (bid - 560 - 40 * m) * 1280;
    u32x4 v[5];
#pragma unroll
    for (int q = 0; q < 5; ++q) v[q] = LD1((const u32x4*)((const char*)(m ? dim : dre) + (size_t)(c0 + tid + 256 * q) * 16));
#pragma unroll
    for (int q = 0; q < 5; ++q) {
      const int id = c0 + tid + 256 * q;
      const int e = id / 80, ch = id - e * 80;
      const int cb = e / 80, row = e - cb * 80;
      *(u32x4*)(ws + WS_WDT + ((size_t)(cb * 20 + m * 10 + (ch >> 3)) * 80 + row) * 128 + ((ch & 7) ^ ((row >> 1) & 7)) * 16) = v[q];
    }
  } else if (bid >= 640 + 64 * WPF_KB) {
    const int t = ((bid - 640 - 64 * WPF_KB) >> 3) * 256 + tid;
    const char* a;
    if (t < 384) a = ws + (size_t)t * 65536;
    else if (t < 512) a = (const char*)wre + (size_t)(t - 384) * 65536;
    else if (t < 640) a = (const char*)wim + (size_t)(t - 512) * 65536;
    else if (t < 680) a = (const char*)x + (size_t)(t - 640) * 65536;
    else if (t < 720) a = (const char*)outp + (size_t)(t - 680) * 65536;
    else a = ws;
    unsigned v;
    asm volatile("global_load_dword %0, %1, off" : "=v"(v) : "v"(a) : "memory");
    wait_vm<0>();
    asm volatile("" ::"v"(v));
  } else {
    const int pw = (bid - 640) * 4 + __builtin_amdgcn_readfirstlane(tid >> 6);
    const char* src = (const char*)(pw < 512 ? wre : wim) + (size_t)(pw & 511) * 16384 + lane * 16;
    u32x4 v[16];
#pragma unroll
    for (int i = 0; i < 16; ++i) v[i] = *(const u32x4*)(src + i * 1024);
    float rs[4];
#pragma unroll
    for (int r = 0; r < 4; ++r) {
      float a0 = 0.f, a1 = 0.f;
#pragma unroll
      for (int q = 0; q < 4; ++q)
#pragma unroll
        for (int e = 0; e < 4; ++e) { a0 += lo16(v[4 * r + q][e]); a1 += hi16(v[4 * r + q][e]); }
      rs[r] = a0 + a1;
    }
#pragma unroll
    for (int o = 1; o < 64; o <<= 1)
#pragma unroll
      for (int r = 0; r < 4; ++r) rs[r] += __shfl_xor(rs[r], o, 64);
    if (lane == 0) *(f32x4*)(ws + WS_RSUM + pw * 16) = (f32x4){rs[0], rs[1], rs[2], rs[3]};
  }
}

__device__ __forceinline__ void dma_res_tile(const char* base  , unsigned lds, int w, int lane) {
#pragma unroll
  for (int j = 0; j < 3; ++j) {
    int L = 160 * w + 64 * j + lane;
    if (L > 639) L = 639;
    const int r = L / 10, ch = L - r * 10;
    if (j < 2) dma16(r * (D_ * 2) + ch * 16, base, lds + (160 * w + 64 * j) * 16);
    else dma16h(r * (D_ * 2) + ch * 16, base, lds + (160 * w + 128) * 16);
  }
}
template <int N>
__device__ __forceinline__ void l2_warm(const char* base, unsigned dummy_lds, int lane) {
#pragma unroll
  for (int i = 0; i < N; ++i) dma16(lane * 16, base + i * 1024, dummy_lds);
}

struct SchedP2 { static constexpr char s[] = "MRMRMRMMRMRMRMMRMRMRMMMMMMMMM"; };
struct SchedP3 { static constexpr char s[] = "RRMRMRRMRMRRMRMRRMRMMM"; };
struct SchedP4 { static constexpr char s[] = "MRMRMRMMRMRMRMMRMRMMRMRMRMMRMRMRMMRMRMMMMMMMMM"; };
constexpr int cnt_ch(const char* s, char c) { int n = 0; for (; *s; ++s) n += (*s == c); return n; }
static_assert(cnt_ch(SchedP2::s, 'M') == 20 && cnt_ch(SchedP2::s, 'R') == 9, "");
static_assert(cnt_ch(SchedP3::s, 'M') == 10 && cnt_ch(SchedP3::s, 'R') == 12, "");
static_assert(cnt_ch(SchedP4::s, 'M') == 30 && cnt_ch(SchedP4::s, 'R') == 16, "");
constexpr int L_RES = 147456, L_MOD = 159744, L_DUM = L_MOD + 2048, L_TOTAL = L_DUM + 1024;
#define P2NS 5
constexpr int P2_NS = P2NS, P2_STAGE = 26624, P2_X = 73728;
constexpr int P3_STAGE = 36864;
constexpr int P4_STAGE = 28672, P4_E = 4 * P4_STAGE, P4_ST = P4_E + 4096, P4_RS = P4_ST + 8192, P4_STG = 2 * P4_STAGE;

__global__ __launch_bounds__(512, 2) void mega(const unsigned short* __restrict__ wre,
                                               const unsigned short* __restrict__ wim,
                                               const unsigned short* __restrict__ x,
                                               char* __restrict__ ws,
                                               unsigned short* __restrict__ out) {
  STAMP_DECL
  STAMP(0);
  extern __shared__ __attribute__((aligned(16))) char smem[];
  const int tid = threadIdx.x, lane = tid & 63;
  const int wv = __builtin_amdgcn_readfirstlane(tid >> 6);
  const int w = wv & 3;
  const bool prod = wv < 4;
  const int bid = blockIdx.x;
  const int xcd = bid & 7, bi = bid >> 3;
  const int cb = bi & 7, rsel = bi >> 3;
  const int g = lane >> 4, l15 = lane & 15;
  const unsigned lds0 = lds_off(smem);
  const float* mod = (const float*)(smem + L_MOD);
  const int cb3 = (xcd & 1) * 4 + (bi & 3);
  const int t3 = ((xcd >> 1) * 8 + (bi >> 2)) * 64;
  const int cb4 = (xcd & 1) * 8 + (bi & 7), grp4 = (xcd >> 1) * 4 + (bi >> 3);
  const int grp3 = t3 >> 7;

  {
    const int isim = rsel >> 1;
    const int grp2 = xcd * 2 + (rsel & 1);
    const int t0 = grp2 * 128;
    if (prod) {
      const char* rbase = (const char*)(isim ? wim : wre) + (size_t)t0 * (S_ * 2);
      const char* cbase = ws + WS_HT + (size_t)cb * 32 * 10240;
      unsigned roff[4];
#pragma unroll
      for (int j = 0; j < 4; ++j) {
        const int row = 32 * w + 8 * j + (lane >> 3);
        roff[j] = row * (S_ * 2) + 16 * ((lane & 7) ^ ((row >> 1) & 7));
      }
      auto run = [&](auto ncc) {
        constexpr int NC = decltype(ncc)::value;
        const int c0 = (NC == 3) ? w * 3072 : 6144 + (w - 2) * 2048;
        producer<P2_NS, 32, 4 + NC>([&](int ks, int buf) {
          const unsigned lb = lds0 + buf * P2_STAGE;
#pragma unroll
          for (int j = 0; j < 4; ++j) dma16(roff[j], rbase + ks * 128, lb + (32 * w + 8 * j) * 128);
#pragma unroll
          for (int j = 0; j < NC; ++j) dma16(lane * 16, cbase + ks * 10240 + c0 + j * 1024, lb + 16384 + c0 + j * 1024);
        });
      };
      if (w < 2) run(IC<3>{}); else run(IC<2>{});
      bar();
      const char* wdt = ws + WS_WDT + (size_t)cb3 * 204800;
#pragma unroll
      for (int s = 0; s < PFD; ++s)
#pragma unroll
        for (int j = 0; j < 5; ++j)
          dma16(lane * 16, wdt + s * 20480 + w * 5120 + j * 1024, lds0 + s * P3_STAGE + 16384 + w * 5120 + j * 1024);
    } else {
      {
        const char* pc;
        unsigned len;
        asm volatile("s_getpc_b64 %0\n.Lmega_pf_here:\n\ts_mov_b32 %1, .Lmega_pf_end-.Lmega_pf_here" : "=s"(pc), "=s"(len));
        const unsigned off = (bi * 4 + w) * 1024;
        if (off + 1024 <= len) dma16(lane * 16, (const char*)((uintptr_t)pc & ~(uintptr_t)15) + off, lds0 + L_DUM);
      }
      if (w < 2) {
        int L = 64 * w + lane; if (L > 99) L = 99;
        const int vec = L / 20, part = vec + (vec >= 3);
        dma16(part * 2560 + (vec < 2 ? cb : cb3) * 320 + (L - vec * 20) * 16, ws + WS_MOD, lds0 + L_MOD + w * 1024);
      }
      dma_res_tile((const char*)x + (size_t)t3 * (D_ * 2) + cb3 * 160, lds0 + L_RES, w, lane);
      const int wm = w & 1, wk = w >> 1;
      const int fsw = ((4 * wk + g) ^ (l15 >> 1)) * 16;
      const int ra = (64 * wm + l15) * 128 + fsw;
      const int ca_ = 16384 + (2 * wk + (g >> 1)) * 2560 + l15 * 32 + (g & 1) * 16;
      bf16x8 rf[2][4], cf[2][5];
      f32x4 acc[5][4];
#pragma unroll
      for (int j = 0; j < 5; ++j)
#pragma unroll
        for (int i = 0; i < 4; ++i) acc[j][i] = (f32x4){0.f, 0.f, 0.f, 0.f};
      const float sgn = isim ? -1.0f : 1.0f;
      const float* rsum = (const float*)(ws + WS_RSUM) + isim * 2048 + t0 + 64 * wm + 32 * wk + l15;
      const float rs0 = rsum[0] * sgn, rs1 = rsum[16] * sgn;
      auto ldf = [&](auto par, auto ic, int buf) {
        constexpr int PP = decltype(par)::value, n = decltype(ic)::value;
        const char* lb = smem + buf * P2_STAGE;
        if constexpr (n == 0) rf[PP][0] = lds_frag(lb + ra);
        else if constexpr (n < 6) cf[PP][n - 1] = lds_frag(lb + ca_ + (n - 1) * 512);
        else rf[PP][n - 5] = lds_frag(lb + ra + (n - 5) * 2048);
      };
      auto body = [&](auto par, auto dl, int rd) {
        constexpr int PP = decltype(par)::value, DL = decltype(dl)::value;
        auto fm = [&](auto mc) {
          constexpr int m = decltype(mc)::value, i = m / 5, j = m % 5;
          acc[j][i] = MFMA16(cf[PP][j], rf[PP][i], acc[j][i]);
        };
        auto fr = [&](auto rc) { ldf(IC<PP ^ 1>{}, rc, rd); };
        run_sched<SchedP2, 0, 0, 0, DL>(fm, fr);
      };
      auto load0 = [&] { static_for<9>([&](auto ic) { ldf(IC<0>{}, ic, 0); }); };
      consumer<P2_NS, 32>(load0, body);
      STAMP(1);
      auto finish = [&](auto keepc) {
        constexpr int KP = decltype(keepc)::value;
        char* dst = smem + P2_X + w * 10752 + lane * 16;
#pragma unroll
        for (int j = 0; j < 5; ++j) {
          *(f32x4*)(dst + (2 * j) * 1024) = acc[j][2 * (KP ^ 1)];
          *(f32x4*)(dst + (2 * j + 1) * 1024) = acc[j][2 * (KP ^ 1) + 1];
        }
        wait_vm<0>();
        wait_lgkm0();
        bar();
        const char* srcp = smem + P2_X + (w ^ 2) * 10752 + lane * 16;
        char* pq = ws + WS_PQ + ((size_t)(grp2 * 2 + wm) * 80 + isim * 40 + cb * 5) * 2048 + (32 * KP + l15) * 32 + g * 8;
        f32x4 p0[5], p1[5], sa[5], c1[5];
#pragma unroll
        for (int j = 0; j < 5; ++j) {
          p0[j] = *(const f32x4*)(srcp + (2 * j) * 1024);
          p1[j] = *(const f32x4*)(srcp + (2 * j + 1) * 1024);
          sa[j] = *(const f32x4*)(mod + 16 * j + 4 * g);
          c1[j] = *(const f32x4*)(mod + 80 + 16 * j + 4 * g);
        }
        u32x2 o0[5], o1[5];
#pragma unroll
        for (int j = 0; j < 5; ++j) {
          const f32x4 cs = c1[j] * sgn;
          const f32x4 v0 = cs * (acc[j][2 * KP] + p0[j]) + sa[j] * rs0;
          const f32x4 v1 = cs * (acc[j][2 * KP + 1] + p1[j]) + sa[j] * rs1;
          o0[j] = (u32x2){pk2(v0[0], v0[1]), pk2(v0[2], v0[3])};
          o1[j] = (u32x2){pk2(v1[0], v1[1]), pk2(v1[2], v1[3])};
        }
#pragma unroll
        for (int j = 0; j < 5; ++j) {
          st8u_wt(pq + j * 2048, o0[j]);
          st8u_wt(pq + j * 2048 + 512, o1[j]);
        }
      };
      if (wk == 0) finish(IC<0>{}); else finish(IC<1>{});
    }
    STAMP(2);
    if (!prod) wait_vm<0>();
    bar();
    STAMP(3);
    if (wv == 4) {
      if (lane == 0) add_count(ws, grp2);
      wait_count(ws, grp3, 16);
    }
    bar();
  }

  {
    const char* wdt = ws + WS_WDT + (size_t)cb3 * 204800;
    if (prod) {
      const char* rbase = ws + WS_PQ + (size_t)(t3 >> 6) * (80 * 2048) + w * 4096;
      STAMP(4);
      producer_seam<10, 4, 5>([&](int ks, int buf) {
#pragma unroll
        for (int j = 0; j < 4; ++j) dma16s(lane * 16, rbase + ks * 16384 + j * 1024, lds0 + buf * P3_STAGE + w * 4096 + j * 1024);
      }, [&](int ks, int buf) {
#pragma unroll
        for (int j = 0; j < 5; ++j)
          dma16(lane * 16, wdt + ks * 20480 + w * 5120 + j * 1024, lds0 + buf * P3_STAGE + 16384 + w * 5120 + j * 1024);
      }, [] {});
      bar();
      const char* w1t = ws + WS_W1T + (size_t)cb4 * 122880;
      dma16(w * 1024 + lane * 16, ws + WS_E, lds0 + P4_E + w * 1024);
#pragma unroll
      for (int s = 0; s < PFD; ++s)
#pragma unroll
        for (int j = 0; j < 3; ++j)
          dma16(lane * 16, w1t + s * 12288 + w * 3072 + j * 1024, lds0 + s * P4_STAGE + 16384 + w * 3072 + j * 1024);
    } else {
      const int sw0 = (g ^ (l15 >> 1)) * 16, sw1 = ((4 + g) ^ (l15 >> 1)) * 16;
      const int ra = (g >> 1) * 2048 + (16 * w + l15) * 32 + (g & 1) * 16, ca_ = 16384 + l15 * 128;
      bf16x8 rf[2][2], cf[2][2][5];
      f32x4 acc[5];
#pragma unroll
      for (int j = 0; j < 5; ++j) acc[j] = (f32x4){0.f, 0.f, 0.f, 0.f};
      auto ldf = [&](auto hc, auto ic, int buf) {
        constexpr int H = decltype(hc)::value, n = decltype(ic)::value, kk = n / 6, m = n % 6;
        const char* lb = smem + buf * P3_STAGE;
        if constexpr (m == 0) rf[H][kk] = lds_frag(lb + ra + H * 8192 + kk * 4096);
        else cf[H][kk][m - 1] = lds_frag(lb + (kk ? sw1 : sw0) + ca_ + H * 10240 + (m - 1) * 2048);
      };
      auto half = [&](auto par, auto dl, int buf) {
        constexpr int PP = decltype(par)::value, DL = decltype(dl)::value;
        auto fm = [&](auto mc) {
          constexpr int m = decltype(mc)::value, kk = m / 5, j = m % 5;
          acc[j] = MFMA16(cf[PP][kk][j], rf[PP][kk], acc[j]);
        };
        auto fr = [&](auto rc) { ldf(IC<PP ^ 1>{}, rc, buf); };
        run_sched<SchedP3, 0, 0, 0, DL>(fm, fr);
      };
      char* res = smem + L_RES + (16 * w + l15) * 160 + g * 8;
      u32x2 xr[5];
      f32x4 ga[5], c1[5];
#pragma unroll
      for (int j = 0; j < 5; ++j) {
        xr[j] = *(const u32x2*)(res + j * 32);
        ga[j] = *(const f32x4*)(mod + 160 + 16 * j + 4 * g);
        c1[j] = *(const f32x4*)(mod + 240 + 16 * j + 4 * g);
      }
      auto load0 = [&] { STAMP(4); static_for<12>([&](auto ic) { ldf(IC<0>{}, ic, 0); }); };
      consumer_half<10>(load0, half);
      STAMP(5);
      bar();
      char* xc = ws + WS_XC + ((size_t)(t3 >> 6) * 40 + cb3 * 5) * 2048 + (16 * w + l15) * 32 + g * 8;
      float xf[20];
      float s = 0.f;
#pragma unroll
      for (int j = 0; j < 5; ++j) {
        const f32x4 y = acc[j];
        const u32x2 o = {pk2(lo16(xr[j][0]) + ga[j][0] * y[0], hi16(xr[j][0]) + ga[j][1] * y[1]),
                         pk2(lo16(xr[j][1]) + ga[j][2] * y[2], hi16(xr[j][1]) + ga[j][3] * y[3])};
        xr[j] = o;
        xf[4 * j] = lo16(o[0]); xf[4 * j + 1] = hi16(o[0]); xf[4 * j + 2] = lo16(o[1]); xf[4 * j + 3] = hi16(o[1]);
        st8u_wt(xc + j * 2048, (u32x2){pk2(xf[4 * j] * c1[j][0], xf[4 * j + 1] * c1[j][1]), pk2(xf[4 * j + 2] * c1[j][2], xf[4 * j + 3] * c1[j][3])});
        s += (xf[4 * j] + xf[4 * j + 1]) + (xf[4 * j + 2] + xf[4 * j + 3]);
      }
      s += __shfl_xor(s, 16, 64);
      s += __shfl_xor(s, 32, 64);
      const float ml = s * (1.0f / 80.0f);
      float m2 = 0.f;
#pragma unroll
      for (int e = 0; e < 20; ++e) { float d = xf[e] - ml; m2 += d * d; }
      m2 += __shfl_xor(m2, 16, 64);
      m2 += __shfl_xor(m2, 32, 64);
      if (lane < 16) st8_wt(ws + WS_ST + ((size_t)(t3 >> 6) * 8 + cb3) * 512 + (16 * w + l15) * 8, (f32x2){s, m2});
#pragma unroll
      for (int j = 0; j < 5; ++j) *(u32x2*)(res + j * 32) = xr[j];
      STAMP(6);
      wait_vm<0>();
    }
    bar();
    STAMP(7);
    if (wv == 4) {
      if (lane == 0) add_count(ws, 16 + grp3);
      wait_count(ws, 16 + grp4, 16);
    }
    bar();
  }

  {
    const int t0 = grp4 * 128;
    const char* w1t = ws + WS_W1T + (size_t)cb4 * 122880;
    if (prod) {
      const char* rbase = ws + WS_XC + (size_t)(grp4 * 2 + (w >> 1)) * (40 * 2048) + (w & 1) * 4096;
      STAMP(8);
      dma16s(w * 2048 + lane * 16, ws + WS_ST + (size_t)t0 * 64, lds0 + P4_ST + w * 2048);
      dma16s(w * 2048 + 1024 + lane * 16, ws + WS_ST + (size_t)t0 * 64, lds0 + P4_ST + w * 2048 + 1024);
      producer_seam<10, 4, 3>([&](int ks, int buf) {
#pragma unroll
        for (int j = 0; j < 4; ++j) dma16s(lane * 16, rbase + ks * 8192 + j * 1024, lds0 + buf * P4_STAGE + w * 4096 + j * 1024);
      }, [&](int ks, int buf) {
#pragma unroll
        for (int j = 0; j < 3; ++j)
          dma16(lane * 16, w1t + ks * 12288 + w * 3072 + j * 1024, lds0 + buf * P4_STAGE + 16384 + w * 3072 + j * 1024);
      }, [&] {
        if (tid < 128) {
          const char* sp = smem + P4_ST + (tid >> 6) * 4096 + (tid & 63) * 8;
          f32x2 pp[8];
#pragma unroll
          for (int e = 0; e < 8; ++e) pp[e] = *(const f32x2*)(sp + e * 512);
          const float mean = (((pp[0][0] + pp[1][0]) + (pp[2][0] + pp[3][0])) + ((pp[4][0] + pp[5][0]) + (pp[6][0] + pp[7][0]))) * (1.0f / D_);
          float m2 = ((pp[0][1] + pp[1][1]) + (pp[2][1] + pp[3][1])) + ((pp[4][1] + pp[5][1]) + (pp[6][1] + pp[7][1]));
          float dd = 0.f;
#pragma unroll
          for (int e = 0; e < 8; ++e) { const float d = pp[e][0] * (1.0f / 80.0f) - mean; dd += d * d; }
          m2 += 80.0f * dd;
          const float rden = 1.0f / bfr(sqrtf(bfr(bfr(m2 * (1.0f / D_)) + 1e-5f)));
          *(f32x2*)(smem + P4_RS + tid * 8) = (f32x2){bfr(mean), rden};
        }
        wait_lgkm0();
      });
      bar();
      const char* w2t = ws + WS_W2T + (size_t)cb3 * F_ * 160;
#pragma unroll
      for (int s = 0; s < PFD; ++s)
#pragma unroll
        for (int j = 0; j < 5; ++j)
          dma16(lane * 16, w2t + s * 20480 + w * 5120 + j * 1024, lds0 + s * P3_STAGE + 16384 + w * 5120 + j * 1024);
    } else {
      const int q = l15 >> 2, p4 = lane & 3;
      const int wm = w & 1, wn = w >> 1;
      const int rpos = 16 * (g >> 1) + 4 * (g & 1) + q;
      const int xs = (rpos >> 2) & 1;
      int cadr[3];
#pragma unroll
      for (int j = 0; j < 3; ++j) {
        const int jj = 3 * wn + j;
        cadr[j] = 16384 + rpos * 192 + (2 * (jj ^ xs) + (p4 >> 1)) * 16 + (p4 & 1) * 8;
      }
      const int ra = wm * 8192 + (g >> 1) * 2048 + l15 * 32 + (g & 1) * 16;
      int ea = P4_E + (l15 < 2 ? l15 : 2) * 1280 + g * 16;
      bf16x8 rf[2][2][5], cf[2][2][3];
      f32x4 acc[3][5];
#pragma unroll
      for (int j = 0; j < 3; ++j)
#pragma unroll
        for (int i = 0; i < 5; ++i) acc[j][i] = (f32x4){0.f, 0.f, 0.f, 0.f};
      auto ldf = [&](auto par, auto ic, int buf) {
        constexpr int PP = decltype(par)::value, n = decltype(ic)::value, kk = n / 8, m = n % 8;
        const char* lb = smem + buf * P4_STAGE;
        if constexpr (m == 0) rf[PP][kk][0] = lds_frag(lb + ra + kk * 4096);
        else if constexpr (m < 4) cf[PP][kk][m - 1] = cat_frag(lds_tr(lb + cadr[m - 1] + kk * 6144), lds_tr(lb + cadr[m - 1] + kk * 6144 + 1536));
        else if constexpr (m < 7) rf[PP][kk][m - 3] = lds_frag(lb + ra + kk * 4096 + (m - 3) * 512);
        else {
          rf[PP][kk][4] = lds_frag(smem + ea + kk * 64);
          if constexpr (kk == 1) ea += 128;
        }
      };
      auto body = [&](auto par, auto dl, int rd) {
        constexpr int PP = decltype(par)::value, DL = decltype(dl)::value;
        auto fm = [&](auto mc) {
          constexpr int m = decltype(mc)::value, kk = m / 15, i = (m % 15) / 3, j = m % 3;
          acc[j][i] = MFMA16(cf[PP][kk][j], rf[PP][kk][i], acc[j][i]);
        };
        auto fr = [&](auto rc) { ldf(IC<PP ^ 1>{}, rc, rd); };
        run_sched<SchedP4, 0, 0, 0, DL>(fm, fr);
      };
      auto load0 = [&] { STAMP(8); static_for<16>([&](auto ic) { ldf(IC<0>{}, ic, 0); }); };
      consumer<4, 10>(load0, body);
      STAMP(9);
      bar();
      f32x4 u[3], v[3];
#pragma unroll
      for (int j = 0; j < 3; ++j)
#pragma unroll
        for (int e = 0; e < 4; ++e) {
          const int a = __float_as_int(acc[j][4][e]);
          u[j][e] = __int_as_float(__builtin_amdgcn_update_dpp(0, a, 0x150, 0xf, 0xf, false));
          v[j][e] = __int_as_float(__builtin_amdgcn_update_dpp(0, a, 0x151, 0xf, 0xf, false));
        }
      char* h1 = ws + WS_H1 + ((size_t)(grp4 * 2 + wm) * 96 + cb4 * 6 + 3 * wn) * 2048 + l15 * 32 + g * 8;
      f32x2 rsv[4];
#pragma unroll
      for (int i = 0; i < 4; ++i) rsv[i] = *(const f32x2*)(smem + P4_RS + (64 * wm + 16 * i + l15) * 8);
#pragma unroll
      for (int i = 0; i < 4; ++i) {
        const f32x2 rs = rsv[i];
#pragma unroll
        for (int j = 0; j < 3; ++j) {
          const f32x4 hv = (acc[j][i] - u[j] * rs[0]) * rs[1] + v[j];
          st8u_wt(h1 + j * 2048 + i * 512,
                  (u32x2){pk2(fmaxf(hv[0], 0.f), fmaxf(hv[1], 0.f)), pk2(fmaxf(hv[2], 0.f), fmaxf(hv[3], 0.f))});
        }
      }
      STAMP(10);
      wait_vm<0>();
    }
    bar();
    STAMP(11);
    if (wv == 4) {
      if (lane == 0) add_count(ws, 32 + grp4);
      wait_count(ws, 32 + grp3, 16);
    }
    bar();
  }

  {
    const char* w2t = ws + WS_W2T + (size_t)cb3 * F_ * 160;
    if (prod) {
      const char* rbase = ws + WS_H1 + (size_t)(t3 >> 6) * (96 * 2048) + w * 4096;
      STAMP(12);
      producer_seam<12, 4, 5>([&](int ks, int buf) {
#pragma unroll
        for (int j = 0; j < 4; ++j) dma16s(lane * 16, rbase + ks * 16384 + j * 1024, lds0 + buf * P3_STAGE + w * 4096 + j * 1024);
      }, [&](int ks, int buf) {
#pragma unroll
        for (int j = 0; j < 5; ++j)
          dma16(lane * 16, w2t + ks * 20480 + w * 5120 + j * 1024, lds0 + buf * P3_STAGE + 16384 + w * 5120 + j * 1024);
      }, [] {});
    } else {
      const int q = l15 >> 2, p4 = lane & 3;
      const int ra = (g >> 1) * 2048 + (16 * w + l15) * 32 + (g & 1) * 16;
      const int ca_ = 16384 + (16 * (g >> 1) + 4 * (g & 1) + q) * 160 + p4 * 8;
      bf16x8 rf[2][2], cf[2][2][5];
      f32x4 acc[5];
#pragma unroll
      for (int j = 0; j < 5; ++j) acc[j] = (f32x4){0.f, 0.f, 0.f, 0.f};
      auto ldf = [&](auto hc, auto ic, int buf) {
        constexpr int H = decltype(hc)::value, n = decltype(ic)::value, kk = n / 6, m = n % 6;
        const char* lb = smem + buf * P3_STAGE;
        if constexpr (m == 0) rf[H][kk] = lds_frag(lb + ra + H * 8192 + kk * 4096);
        else cf[H][kk][m - 1] = cat_frag(lds_tr(lb + ca_ + H * 10240 + kk * 5120 + (m - 1) * 32),
                                         lds_tr(lb + ca_ + H * 10240 + kk * 5120 + (m - 1) * 32 + 1280));
      };
      auto half = [&](auto par, auto dl, int buf) {
        constexpr int PP = decltype(par)::value, DL = decltype(dl)::value;
        auto fm = [&](auto mc) {
          constexpr int m = decltype(mc)::value, kk = m / 5, j = m % 5;
          acc[j] = MFMA16(cf[PP][kk][j], rf[PP][kk], acc[j]);
        };
        auto fr = [&](auto rc) { ldf(IC<PP ^ 1>{}, rc, buf); };
        run_sched<SchedP3, 0, 0, 0, DL>(fm, fr);
      };
      char* res = smem + L_RES + (16 * w + l15) * 160 + g * 8;
      u32x2 xr[5];
      f32x4 gb[5];
#pragma unroll
      for (int j = 0; j < 5; ++j) {
        xr[j] = *(const u32x2*)(res + j * 32);
        gb[j] = *(const f32x4*)(mod + 320 + 16 * j + 4 * g);
      }
      auto load0 = [&] { STAMP(12); static_for<12>([&](auto ic) { ldf(IC<0>{}, ic, 0); }); };
      consumer_half<12>(load0, half);
      STAMP(13);
#pragma unroll
      for (int j = 0; j < 5; ++j) {
        const f32x4 y = acc[j];
        *(u32x2*)(res + j * 32) = (u32x2){pk2(lo16(xr[j][0]) + gb[j][0] * y[0], hi16(xr[j][0]) + gb[j][1] * y[1]),
                                          pk2(lo16(xr[j][1]) + gb[j][2] * y[2], hi16(xr[j][1]) + gb[j][3] * y[3])};
      }
      wait_vm<0>();
    }
    wait_lgkm0();
    bar();
    for (int id = tid; id < 640; id += 512) {
      const int row = id / 10, c = id - row * 10;
      *(u32x4*)((char*)out + (size_t)(t3 + row) * (D_ * 2) + cb3 * 160 + c * 16) = *(const u32x4*)(smem + L_RES + row * 160 + c * 16);
    }
  }
  STAMP(14);
  STAMP_OUT();
  asm volatile(".Lmega_pf_end:");
}

extern "C" void kernel_launch(void* const* d_in, const int* in_sizes, int n_in,
                              void* d_out, int out_size, void* d_ws, size_t ws_size,
                              hipStream_t stream) {
  const unsigned short* x    = (const unsigned short*)d_in[0];
  const unsigned short* wsre = (const unsigned short*)d_in[1];
  const unsigned short* wsim = (const unsigned short*)d_in[2];
  const unsigned short* wdre = (const unsigned short*)d_in[3];
  const unsigned short* wdim = (const unsigned short*)d_in[4];
  const unsigned short* w1   = (const unsigned short*)d_in[6];
  const unsigned short* w2   = (const unsigned short*)d_in[7];
  const unsigned short* c    = (const unsigned short*)d_in[8];
  const unsigned short* wmod = (const unsigned short*)d_in[9];
  char* ws = (char*)d_ws;
  unsigned short* out = (unsigned short*)d_out;
  (void)hipFuncSetAttribute((const void*)mega, hipFuncAttributeMaxDynamicSharedMemorySize, L_TOTAL);
  k1_ln_mod<<<640 + 64 * WPF_KB + 24, 256, 0, stream>>>(x, c, wmod, wsre, wsim, w2, wdre, wdim, w1, out, ws);
  mega<<<256, 512, L_TOTAL, stream>>>(wsre, wsim, x, ws, out);
}
```

```cpp
#define K1_CODE_PF
#define SEAM_CODE_PF
#include <hip/hip_runtime.h>
#include <hip/hip_bf16.h>
#include <stdint.h>

typedef __attribute__((ext_vector_type(8))) __bf16 bf16x8;
typedef __attribute__((ext_vector_type(4))) __bf16 bf16x4;
typedef __attribute__((ext_vector_type(2))) __bf16 bf16x2;
typedef __attribute__((ext_vector_type(4))) float f32x4;
typedef __attribute__((ext_vector_type(2))) float f32x2;
typedef __attribute__((ext_vector_type(4))) short s16x4;
typedef __attribute__((ext_vector_type(8))) short s16x8;
typedef __attribute__((ext_vector_type(4))) unsigned u32x4;
typedef __attribute__((ext_vector_type(2))) unsigned u32x2;
typedef __attribute__((address_space(3))) s16x4* lds_s16x4_ptr;
typedef __attribute__((address_space(3))) void* lds_vptr;

constexpr int S_ = 2048, D_ = 640, F_ = 1536, MODN = 3840;

constexpr size_t WS_MOD = 0;
constexpr size_t WS_E   = 16384;
constexpr size_t WS_CNT = 20480;
constexpr size_t WS_ST  = 32768;
constexpr size_t WS_RSUM = WS_ST + (size_t)S_ * 64;
constexpr size_t WS_HT  = WS_RSUM + 16384;
constexpr size_t WS_PQ  = WS_HT + (size_t)D_ * S_ * 2;
constexpr size_t WS_XC  = WS_PQ + (size_t)S_ * 1280 * 2;
constexpr size_t WS_H1  = WS_XC + (size_t)S_ * D_ * 2;
constexpr size_t WS_W2T = WS_H1 + (size_t)S_ * F_ * 2;
constexpr size_t WS_W1T = WS_W2T + (size_t)F_ * D_ * 2;
constexpr size_t WS_WDT = WS_W1T + (size_t)F_ * D_ * 2;

#define STAMP_DECL
#define STAMP(i)
#define STAMP_OUT()
template <int V> struct IC { static constexpr int value = V; };
template <int N, int I = 0, class F> __device__ __forceinline__ void static_for(F f) {
  if constexpr (I < N) { f(IC<I>{}); static_for<N, I + 1>(f); }
}

__device__ __forceinline__ float bf2f(unsigned short u) { return __uint_as_float(((unsigned)u) << 16); }
__device__ __forceinline__ float bfr(float f) { return (float)(__bf16)f; }
__device__ __forceinline__ unsigned pk2(float a, float b) {
  bf16x2 v = __builtin_convertvector((f32x2){a, b}, bf16x2);
  return __builtin_bit_cast(unsigned, v);
}
__device__ __forceinline__ float lo16(unsigned u) { return __uint_as_float(u << 16); }
__device__ __forceinline__ float hi16(unsigned u) { return __uint_as_float(u & 0xffff0000u); }

__device__ __forceinline__ void dma16(unsigned voff, const char* sbase, unsigned lds) {
  asm volatile("s_mov_b32 m0, %2\n\ts_nop 0\n\tglobal_load_lds_dwordx4 %0, %1"
               ::"v"(voff), "s"(sbase), "s"(lds) : "memory");
}
__device__ __forceinline__ void dma16nt(unsigned voff, const char* sbase, unsigned lds) {
  asm volatile("s_mov_b32 m0, %2\n\ts_nop 0\n\tglobal_load_lds_dwordx4 %0, %1 nt"
               ::"v"(voff), "s"(sbase), "s"(lds) : "memory");
}
__device__ __forceinline__ void dma16s(unsigned voff, const char* sbase, unsigned lds) {
  asm volatile("s_mov_b32 m0, %2\n\ts_nop 0\n\tglobal_load_lds_dwordx4 %0, %1 sc1"
               ::"v"(voff), "s"(sbase), "s"(lds) : "memory");
}
#define LD1(p) __builtin_nontemporal_load(p)
__device__ __forceinline__ void dma16h(unsigned voff, const char* sbase, unsigned lds) {
  asm volatile("s_mov_b32 m0, %2\n\ts_mov_b32 exec_hi, 0\n\tglobal_load_lds_dwordx4 %0, %1\n\ts_mov_b32 exec_hi, -1"
               ::"v"(voff), "s"(sbase), "s"(lds) : "memory");
}
__device__ __forceinline__ unsigned lds_off(const void* p) {
  return (unsigned)(uintptr_t)(__attribute__((address_space(3))) const char*)p;
}
#define SB0() __builtin_amdgcn_sched_barrier(0)
template <int N> __device__ __forceinline__ void wait_vm_lgkm0() {
  asm volatile("s_waitcnt vmcnt(%0) lgkmcnt(0)" ::"n"(N) : "memory");
}
__device__ __forceinline__ void bar() {
  asm volatile("" ::: "memory");
  __builtin_amdgcn_s_barrier();
  asm volatile("" ::: "memory");
}
__device__ __forceinline__ bf16x8 lds_frag(const char* p) { return *(const bf16x8*)p; }
__device__ __forceinline__ s16x4 lds_tr(const char* p) {
  return __builtin_amdgcn_ds_read_tr16_b64_v4i16((lds_s16x4_ptr)p);
}
__device__ __forceinline__ bf16x8 cat_frag(s16x4 a, s16x4 b) {
  s16x8 v = __builtin_shufflevector(a, b, 0, 1, 2, 3, 4, 5, 6, 7);
  return __builtin_bit_cast(bf16x8, v);
}
#define MFMA16(a, b, c) __builtin_amdgcn_mfma_f32_16x16x32_bf16((a), (b), (c), 0, 0, 0)

template <class Sc, int POS, int NM, int NR, int DL, class FM, class FR>
__device__ __forceinline__ void run_sched(FM& fm, FR& fr) {
  if constexpr (Sc::s[POS] != 0) {
    constexpr char c = Sc::s[POS];
    if constexpr (c == 'M') {
      fm(IC<NM>{}); SB0();
      run_sched<Sc, POS + 1, NM + 1, NR, DL>(fm, fr);
    } else {
      if constexpr (DL) { fr(IC<NR>{}); SB0(); }
      run_sched<Sc, POS + 1, NM, NR + 1, DL>(fm, fr);
    }
  }
}
template <int N> __device__ __forceinline__ void wait_vm() { asm volatile("s_waitcnt vmcnt(%0)" ::"n"(N) : "memory"); }
__device__ __forceinline__ void wait_lgkm0() { asm volatile("s_waitcnt lgkmcnt(0)" ::: "memory"); }

template <int NS, int P, int TI>
__device__ __forceinline__ void prod_tail() {
  if constexpr (TI < NS - 1) {
    wait_vm<P*(NS - 2 - TI)>();
    bar();
    prod_tail<NS, P, TI + 1>();
  }
}
constexpr int ramp_A(int j, int NS) {
  int a = 2;
  for (int i = 1; i <= j; ++i) { int lim = (i == 1) ? NS : (i - 1) + NS; a = (a + 2 < lim) ? a + 2 : lim; }
  return a;
}
constexpr int ramp_len(int NS) { int j = 1; while (ramp_A(j, NS) != ((j == 1) ? NS : (j - 1) + NS) || j < 2) ++j; return j; }
template <int NS, int P, int J, int R, class Issue>
__device__ __forceinline__ void prod_ramp(Issue& issue) {
  if constexpr (J < R) {
    constexpr int a0 = (J == 0) ? 0 : ramp_A(J - 1, NS), a1 = ramp_A(J, NS);
#pragma unroll
    for (int s = a0; s < a1; ++s) issue(s, s % NS);
    wait_vm<P*(a1 - (J + 1))>();
    bar();
    prod_ramp<NS, P, J + 1, R>(issue);
  }
}
template <int NS, int NK, int P, class Issue>
__device__ __forceinline__ void producer(Issue issue) {
  constexpr int R = ramp_len(NS);
  static_assert(R - 1 + NS <= NK, "ramp too long");
  prod_ramp<NS, P, 0, R>(issue);
#pragma unroll
  for (int s = ramp_A(R - 1, NS); s < R - 1 + NS; ++s) issue(s, s % NS);
  int wr = (R - 1) % NS;
#pragma nounroll
  for (int i = R - 1; i < NK - NS; ++i) {
    wait_vm<P*(NS - 2)>();
    bar();
    issue(i + NS, wr);
    wr = (wr + 1 == NS) ? 0 : wr + 1;
  }
  prod_tail<NS, P, 0>();
}
#define NO_SBAR
#define CODE_PF
#define MAP84
#define PFD 2
template <int NK, int PR, int PC, class IR, class ICC, class Mid>
__device__ __forceinline__ void producer_seam(IR issueR, ICC issueC, Mid mid) {
  constexpr int P = PR + PC;
  issueR(0, 0); issueR(1, 1);
  wait_vm<PR>(); bar();
  issueR(2, 2); issueC(2, 2); issueR(3, 3); issueC(3, 3);
  wait_vm<2 * P>(); bar();
  mid();
  constexpr int J0 = 2;
#pragma nounroll
  for (int j = J0; j <= NK - 3; ++j) {
    issueR(j + 2, (j + 2) & 3); issueC(j + 2, (j + 2) & 3);
    wait_vm<2 * P>(); bar();
  }
  wait_vm<P>(); bar();
  wait_vm<0>(); bar();
}
template <int NS, int NK, class Load0, class Body>
__device__ __forceinline__ void consumer(Load0 load0, Body body) {
  static_assert((NK & 1) == 0, "even number of stages");
  bar();
  load0();
  int rd = 1;
#pragma nounroll
  for (int i = 0; i < NK - 2; i += 2) {
    wait_lgkm0();
    bar();
    SB0();
    body(IC<0>{}, IC<1>{}, rd);
    SB0();
    rd = (rd + 1 == NS) ? 0 : rd + 1;
    wait_lgkm0();
    bar();
    SB0();
    body(IC<1>{}, IC<1>{}, rd);
    SB0();
    rd = (rd + 1 == NS) ? 0 : rd + 1;
  }
  wait_lgkm0();
  bar();
  SB0();
  body(IC<0>{}, IC<1>{}, rd);
  SB0();
  body(IC<1>{}, IC<0>{}, 0);
  SB0();
}
template <int NK, class Load0, class Half>
__device__ __forceinline__ void consumer_half(Load0 load0, Half half) {
  bar();
  load0();
#pragma nounroll
  for (int s = 0; s < NK - 1; ++s) {
    SB0();
    half(IC<0>{}, IC<1>{}, s & 3);
    SB0();
    wait_lgkm0();
    bar();
    SB0();
    half(IC<1>{}, IC<1>{}, (s + 1) & 3);
    SB0();
  }
  half(IC<0>{}, IC<1>{}, (NK - 1) & 3);
  SB0();
  half(IC<1>{}, IC<0>{}, 0);
  SB0();
}
typedef __attribute__((address_space(1))) unsigned gu32;
__device__ __forceinline__ void st16_wt(void* p, u32x4 v) {
  asm volatile("global_store_dwordx4 %0, %1, off sc1\n\ts_nop 1" ::"v"(p), "v"(v) : "memory");
}
__device__ __forceinline__ void st8u_wt(void* p, u32x2 v) {
  asm volatile("global_store_dwordx2 %0, %1, off sc1\n\ts_nop 1" ::"v"(p), "v"(v) : "memory");
}
__device__ __forceinline__ void st8_wt(void* p, f32x2 v) {
  asm volatile("global_store_dwordx2 %0, %1, off sc1\n\ts_nop 1" ::"v"(p), "v"(v) : "memory");
}
__device__ __forceinline__ void wait_count(char* ws, int idx, unsigned need) {
  gu32* cnt = (gu32*)(ws + WS_CNT + idx * 256);
#pragma nounroll
  for (unsigned spins = 0; spins < (1u << 22); ++spins) {
    if ((unsigned)__builtin_amdgcn_readfirstlane(__hip_atomic_load(cnt, __ATOMIC_RELAXED, __HIP_MEMORY_SCOPE_AGENT)) >= need) break;
    __builtin_amdgcn_s_sleep(1);
  }
  asm volatile("" ::: "memory");
}
__device__ __forceinline__ void add_count(char* ws, int idx) {
  __hip_atomic_fetch_add((gu32*)(ws + WS_CNT + idx * 256), 1u, __ATOMIC_RELAXED, __HIP_MEMORY_SCOPE_AGENT);
}
__device__ __forceinline__ int swap23(int r) { return (r & ~12) | ((r & 4) << 1) | ((r & 8) >> 1); }

#define WPF_KB 4
__global__ __launch_bounds__(256) void k1_ln_mod(const unsigned short* __restrict__ x,
                                                 const unsigned short* __restrict__ c,
                                                 const unsigned short* __restrict__ wmod,
                                                 const unsigned short* __restrict__ wre,
                                                 const unsigned short* __restrict__ wim,
                                                 const unsigned short* __restrict__ w2,
                                                 const unsigned short* __restrict__ dre,
                                                 const unsigned short* __restrict__ dim,
                                                 const unsigned short* __restrict__ w1,
                                                 const unsigned short* __restrict__ outp,
                                                 char* __restrict__ ws) {
  __shared__ __attribute__((aligned(16))) char smem[16 * 1296 + 256];
  const int tid = threadIdx.x, lane = tid & 63;
  const int bid = blockIdx.x;
  if (bid < 128) {
    const int xcd = bid & 7, i = bid >> 3;
    const int rg = xcd * 4 + (i >> 2), sub = i & 3;
    const int row0 = rg * 64 + sub * 16;
    const int r = tid >> 4, j = tid & 15;
    const u32x4* src = (const u32x4*)(x + (size_t)(row0 + r) * D_);
    u32x4 v[5];
#pragma unroll
    for (int q = 0; q < 5; ++q) v[q] = LD1(src + j + 16 * q);
    float f[40];
#pragma unroll
    for (int q = 0; q < 5; ++q)
#pragma unroll
      for (int e = 0; e < 4; ++e) { f[q * 8 + 2 * e] = lo16(v[q][e]); f[q * 8 + 2 * e + 1] = hi16(v[q][e]); }
    float s = 0.f;
#pragma unroll
    for (int e = 0; e < 40; ++e) s += f[e];
#pragma unroll
    for (int o = 1; o < 16; o <<= 1) s += __shfl_xor(s, o, 64);
    const float mean = s * (1.0f / D_);
    float s2 = 0.f;
#pragma unroll
    for (int e = 0; e < 40; ++e) { float d = f[e] - mean; s2 += d * d; }
#pragma unroll
    for (int o = 1; o < 16; o <<= 1) s2 += __shfl_xor(s2, o, 64);
    const float mb = bfr(mean);
    const float vb = bfr(s2 * (1.0f / D_));
    const float rden = 1.0f / bfr(sqrtf(bfr(vb + 1e-5f)));
#pragma unroll
    for (int q = 0; q < 5; ++q) {
      u32x4 o;
#pragma unroll
      for (int e = 0; e < 4; ++e) {
        float a = bfr(f[q * 8 + 2 * e] - mb) * rden;
        float b = bfr(f[q * 8 + 2 * e + 1] - mb) * rden;
        o[e] = pk2(a, b);
      }
      *(u32x4*)(smem + r * 1296 + (j + 16 * q) * 16) = o;
    }
    wait_lgkm0();
    bar();
    const int grp = tid >> 4, li = tid & 15, qq = li >> 2, pp = li & 3;
    char* ht = ws + WS_HT + (size_t)(row0 >> 4) * 2560;
#pragma unroll
    for (int it = 0; it < 5; ++it) {
      const int pr = it * 16 + grp;
      const int cblk = pr >> 1, half = pr & 1;
      const char* a0 = smem + (8 * half + qq) * 1296 + (16 * cblk + 4 * pp) * 2;
      s16x4 t0 = lds_tr(a0);
      s16x4 t1 = lds_tr(a0 + 4 * 1296);
      s16x8 o = __builtin_shufflevector(t0, t1, 0, 1, 2, 3, 4, 5, 6, 7);
      const int d = 16 * cblk + li;
      const int cb = d / 80, rr = d - cb * 80;
      *(s16x8*)(ht + (size_t)cb * (128 * 2560) + rr * 32 + half * 16) = o;
    }
    if (bid == 0 && tid < 96) *(u32x4*)(ws + WS_E + 2560 + tid * 16) = (u32x4){0u, 0u, 0u, 0u};
    if (bid == 1 && tid < 48) *(unsigned*)(ws + WS_CNT + tid * 256) = 0u;
  } else if (bid < 368) {
    const int g = bid - 128;
    const int cgp = (g & 7) * 30 + (g >> 3);
    const int l2 = tid & 1, rg = tid >> 1;
    u32x4 v[5];
    unsigned short cv16[5];
#pragma unroll
    for (int q = 0; q < 5; ++q) {
      const int k = q * 128 + rg;
      v[q] = *(const u32x4*)(wmod + (size_t)k * MODN + cgp * 16 + l2 * 8);
      cv16[q] = c[k];
    }
    float acc[8];
#pragma unroll
    for (int e = 0; e < 8; ++e) acc[e] = 0.f;
#pragma unroll
    for (int q = 0; q < 5; ++q) {
      const float cv = bf2f(cv16[q]);
      const float sv = bfr(cv / (1.0f + __expf(-cv)));
#pragma unroll
      for (int e = 0; e < 4; ++e) {
        acc[2 * e] += sv * lo16(v[q][e]);
        acc[2 * e + 1] += sv * hi16(v[q][e]);
      }
    }
#pragma unroll
    for (int o = 2; o < 64; o <<= 1)
#pragma unroll
      for (int e = 0; e < 8; ++e) acc[e] += __shfl_xor(acc[e], o, 64);
    float* red = (float*)smem;
    if (lane < 2) {
#pragma unroll
      for (int e = 0; e < 8; ++e) red[(tid >> 6) * 16 + lane * 8 + e] = acc[e];
    }
    wait_lgkm0();
    bar();
    if (tid < 16) {
      float m = bfr((red[tid] + red[16 + tid]) + (red[32 + tid] + red[48 + tid]));
      const int n = cgp * 16 + tid;
      const int part = n / D_, d = n - part * D_;
      if (part == 1 || part == 4) m = bfr(1.0f + m);
      ((float*)(ws + WS_MOD))[n] = m;
      if (part == 4) ((unsigned short*)(ws + WS_E))[d] = (unsigned short)(__float_as_uint(m) >> 16);
      if (part == 3) ((unsigned short*)(ws + WS_E))[D_ + d] = (unsigned short)(__float_as_uint(m) >> 16);
    }
  } else if (bid < 464) {
    const int k0 = (bid - 368) * 16;
    u32x4 v[5];
#pragma unroll
    for (int q = 0; q < 5; ++q) v[q] = LD1((const u32x4*)((const char*)w2 + (size_t)k0 * (D_ * 2) + (tid + 256 * q) * 16));
#pragma unroll
    for (int q = 0; q < 5; ++q) {
      const int id = tid + 256 * q;
      const int r = id / 80, ch = id - r * 80;
      const int cb = ch / 10, cc = ch - cb * 10;
      *(u32x4*)(ws + WS_W2T + ((size_t)cb * F_ + k0 + swap23(r)) * 160 + cc * 16) = v[q];
    }
  } else if (bid < 560) {
    const int c0 = (bid - 464) * 1280;
    u32x4 v[5];
#pragma unroll
    for (int q = 0; q < 5; ++q) v[q] = LD1((const u32x4*)((const char*)w1 + (size_t)(c0 + tid + 256 * q) * 16));
#pragma unroll
    for (int q = 0; q < 5; ++q) {
      const int id = c0 + tid + 256 * q;
      const int k = id / 192, ch = id - k * 192;
      const int cb = ch / 12, cc = ch - cb * 12;
      const int r = swap23(k);
      *(u32x4*)(ws + WS_W1T + ((size_t)cb * D_ + r) * 192 + (cc ^ (((r >> 2) & 1) << 1)) * 16) = v[q];
    }
  } else if (bid < 640) {
    const int m = bid >= 600, c0 = (bid - 560 - 40 * m) * 1280;
    u32x4 v[5];
#pragma unroll
    for (int q = 0; q < 5; ++q) v[q] = LD1((const u32x4*)((const char*)(m ? dim : dre) + (size_t)(c0 + tid + 256 * q) * 16));
#pragma unroll
    for (int q = 0; q < 5; ++q) {
      const int id = c0 + tid + 256 * q;
      const int e = id / 80, ch = id - e * 80;
      const int cb = e / 80, row = e - cb * 80;
      *(u32x4*)(ws + WS_WDT + ((size_t)(cb * 20 + m * 10 + (ch >> 3)) * 80 + row) * 128 + ((ch & 7) ^ ((row >> 1) & 7)) * 16) = v[q];
    }
  } else if (bid >= 640 + 64 * WPF_KB) {
    const int t = ((bid - 640 - 64 * WPF_KB) >> 3) * 256 + tid;
    const char* a;
    if (t < 384) a = ws + (size_t)t * 65536;
    else if (t < 512) a = (const char*)wre + (size_t)(t - 384) * 65536;
    else if (t < 640) a = (const char*)wim + (size_t)(t - 512) * 65536;
    else if (t < 680) a = (const char*)x + (size_t)(t - 640) * 65536;
    else if (t < 720) a = (const char*)outp + (size_t)(t - 680) * 65536;
    else a = ws;
    {
      const char* pc;
      unsigned d0, len;
      asm volatile("s_getpc_b64 %0\n.Lk1_pf_here:\n\ts_mov_b32 %1, .Lmega_pf_here-.Lk1_pf_here\n\ts_mov_b32 %2, .Lmega_pf_end-.Lmega_pf_here"
                   : "=s"(pc), "=s"(d0), "=s"(len));
      const unsigned off = (unsigned)(bid - 640 - 64 * WPF_KB) * 256 * 64 / 8 * 0 + (unsigned)t * 64;
      if (t >= 0 && off < len + 128 && (bid & 7) == 0 && false) a = pc;
      if ((bid & 7) == 1 && off < len + 128) {
        unsigned u;
        asm volatile("global_load_dword %0, %1, off" : "=v"(u) : "v"((const char*)(((uintptr_t)pc + (int)d0 - 128) & ~(uintptr_t)63) + off) : "memory");
        wait_vm<0>();
        asm volatile("" ::"v"(u));
      }
    }
    unsigned v;
    asm volatile("global_load_dword %0, %1, off" : "=v"(v) : "v"(a) : "memory");
    wait_vm<0>();
    asm volatile("" ::"v"(v));
  } else {
    const int pw = (bid - 640) * 4 + __builtin_amdgcn_readfirstlane(tid >> 6);
    const char* src = (const char*)(pw < 512 ? wre : wim) + (size_t)(pw & 511) * 16384 + lane * 16;
    u32x4 v[16];
#pragma unroll
    for (int i = 0; i < 16; ++i) v[i] = *(const u32x4*)(src + i * 1024);
    float rs[4];
#pragma unroll
    for (int r = 0; r < 4; ++r) {
      float a0 = 0.f, a1 = 0.f;
#pragma unroll
      for (int q = 0; q < 4; ++q)
#pragma unroll
        for (int e = 0; e < 4; ++e) { a0 += lo16(v[4 * r + q][e]); a1 += hi16(v[4 * r + q][e]); }
      rs[r] = a0 + a1;
    }
#pragma unroll
    for (int o = 1; o < 64; o <<= 1)
#pragma unroll
      for (int r = 0; r < 4; ++r) rs[r] += __shfl_xor(rs[r], o, 64);
    if (lane == 0) *(f32x4*)(ws + WS_RSUM + pw * 16) = (f32x4){rs[0], rs[1], rs[2], rs[3]};
  }
}

__device__ __forceinline__ void dma_res_tile(const char* base  , unsigned lds, int w, int lane) {
#pragma unroll
  for (int j = 0; j < 3; ++j) {
    int L = 160 * w + 64 * j + lane;
    if (L > 639) L = 639;
    const int r = L / 10, ch = L - r * 10;
    if (j < 2) dma16(r * (D_ * 2) + ch * 16, base, lds + (160 * w + 64 * j) * 16);
    else dma16h(r * (D_ * 2) + ch * 16, base, lds + (160 * w + 128) * 16);
  }
}
template <int N>
__device__ __forceinline__ void l2_warm(const char* base, unsigned dummy_lds, int lane) {
#pragma unroll
  for (int i = 0; i < N; ++i) dma16(lane * 16, base + i * 1024, dummy_lds);
}

struct SchedP2 { static constexpr char s[] = "MRMRMRMMRMRMRMMRMRMRMMMMMMMMM"; };
struct SchedP3 { static constexpr char s[] = "RRMRMRRMRMRRMRMRRMRMMM"; };
struct SchedP4 { static constexpr char s[] = "MRMRMRMMRMRMRMMRMRMMRMRMRMMRMRMRMMRMRMMMMMMMMM"; };
constexpr int cnt_ch(const char* s, char c) { int n = 0; for (; *s; ++s) n += (*s == c); return n; }
static_assert(cnt_ch(SchedP2::s, 'M') == 20 && cnt_ch(SchedP2::s, 'R') == 9, "");
static_assert(cnt_ch(SchedP3::s, 'M') == 10 && cnt_ch(SchedP3::s, 'R') == 12, "");
static_assert(cnt_ch(SchedP4::s, 'M') == 30 && cnt_ch(SchedP4::s, 'R') == 16, "");
constexpr int L_RES = 147456, L_MOD = 159744, L_DUM = L_MOD + 2048, L_TOTAL = L_DUM + 1024;
#define P2NS 5
constexpr int P2_NS = P2NS, P2_STAGE = 26624, P2_X = 73728;
constexpr int P3_STAGE = 36864;
constexpr int P4_STAGE = 28672, P4_E = 4 * P4_STAGE, P4_ST = P4_E + 4096, P4_RS = P4_ST + 8192, P4_STG = 2 * P4_STAGE;

__global__ __launch_bounds__(512, 2) void mega(const unsigned short* __restrict__ wre,
                                               const unsigned short* __restrict__ wim,
                                               const unsigned short* __restrict__ x,
                                               char* __restrict__ ws,
                                               unsigned short* __restrict__ out) {
  STAMP_DECL
  STAMP(0);
  extern __shared__ __attribute__((aligned(16))) char smem[];
  const int tid = threadIdx.x, lane = tid & 63;
  const int wv = __builtin_amdgcn_readfirstlane(tid >> 6);
  const int w = wv & 3;
  const bool prod = wv < 4;
  const int bid = blockIdx.x;
  const int xcd = bid & 7, bi = bid >> 3;
  const int cb = bi & 7, rsel = bi >> 3;
  const int g = lane >> 4, l15 = lane & 15;
  const unsigned lds0 = lds_off(smem);
  const float* mod = (const float*)(smem + L_MOD);
  const int cb3 = (xcd & 1) * 4 + (bi & 3);
  const int t3 = ((xcd >> 1) * 8 + (bi >> 2)) * 64;
  const int cb4 = (xcd & 1) * 8 + (bi & 7), grp4 = (xcd >> 1) * 4 + (bi >> 3);
  const int grp3 = t3 >> 7;

  {
    const int isim = rsel >> 1;
    const int grp2 = xcd * 2 + (rsel & 1);
    const int t0 = grp2 * 128;
    if (prod) {
      const char* rbase = (const char*)(isim ? wim : wre) + (size_t)t0 * (S_ * 2);
      const char* cbase = ws + WS_HT + (size_t)cb * 32 * 10240;
      unsigned roff[4];
#pragma unroll
      for (int j = 0; j < 4; ++j) {
        const int row = 32 * w + 8 * j + (lane >> 3);
        roff[j] = row * (S_ * 2) + 16 * ((lane & 7) ^ ((row >> 1) & 7));
      }
      auto run = [&](auto ncc) {
        constexpr int NC = decltype(ncc)::value;
        const int c0 = (NC == 3) ? w * 3072 : 6144 + (w - 2) * 2048;
        producer<P2_NS, 32, 4 + NC>([&](int ks, int buf) {
          const unsigned lb = lds0 + buf * P2_STAGE;
#pragma unroll
          for (int j = 0; j < 4; ++j) dma16(roff[j], rbase + ks * 128, lb + (32 * w + 8 * j) * 128);
#pragma unroll
          for (int j = 0; j < NC; ++j) dma16(lane * 16, cbase + ks * 10240 + c0 + j * 1024, lb + 16384 + c0 + j * 1024);
        });
      };
      if (w < 2) run(IC<3>{}); else run(IC<2>{});
      bar();
      const char* wdt = ws + WS_WDT + (size_t)cb3 * 204800;
#pragma unroll
      for (int s = 0; s < PFD; ++s)
#pragma unroll
        for (int j = 0; j < 5; ++j)
          dma16(lane * 16, wdt + s * 20480 + w * 5120 + j * 1024, lds0 + s * P3_STAGE + 16384 + w * 5120 + j * 1024);
    } else {
      {
        const char* pc;
        unsigned len;
        asm volatile("s_getpc_b64 %0\n.Lmega_pf_here:\n\ts_mov_b32 %1, .Lmega_pf_end-.Lmega_pf_here" : "=s"(pc), "=s"(len));
        const unsigned off = (bi * 4 + w) * 1024;
        if (off + 1024 <= len) dma16(lane * 16, (const char*)((uintptr_t)pc & ~(uintptr_t)15) + off, lds0 + L_DUM);
      }
      if (w < 2) {
        int L = 64 * w + lane; if (L > 99) L = 99;
        const int vec = L / 20, part = vec + (vec >= 3);
        dma16(part * 2560 + (vec < 2 ? cb : cb3) * 320 + (L - vec * 20) * 16, ws + WS_MOD, lds0 + L_MOD + w * 1024);
      }
      dma_res_tile((const char*)x + (size_t)t3 * (D_ * 2) + cb3 * 160, lds0 + L_RES, w, lane);
      const int wm = w & 1, wk = w >> 1;
      const int fsw = ((4 * wk + g) ^ (l15 >> 1)) * 16;
      const int ra = (64 * wm + l15) * 128 + fsw;
      const int ca_ = 16384 + (2 * wk + (g >> 1)) * 2560 + l15 * 32 + (g & 1) * 16;
      bf16x8 rf[2][4], cf[2][5];
      f32x4 acc[5][4];
#pragma unroll
      for (int j = 0; j < 5; ++j)
#pragma unroll
        for (int i = 0; i < 4; ++i) acc[j][i] = (f32x4){0.f, 0.f, 0.f, 0.f};
      const float sgn = isim ? -1.0f : 1.0f;
      const float* rsum = (const float*)(ws + WS_RSUM) + isim * 2048 + t0 + 64 * wm + 32 * wk + l15;
      const float rs0 = rsum[0] * sgn, rs1 = rsum[16] * sgn;
      auto ldf = [&](auto par, auto ic, int buf) {
        constexpr int PP = decltype(par)::value, n = decltype(ic)::value;
        const char* lb = smem + buf * P2_STAGE;
        if constexpr (n == 0) rf[PP][0] = lds_frag(lb + ra);
        else if constexpr (n < 6) cf[PP][n - 1] = lds_frag(lb + ca_ + (n - 1) * 512);
        else rf[PP][n - 5] = lds_frag(lb + ra + (n - 5) * 2048);
      };
      auto body = [&](auto par, auto dl, int rd) {
        constexpr int PP = decltype(par)::value, DL = decltype(dl)::value;
        auto fm = [&](auto mc) {
          constexpr int m = decltype(mc)::value, i = m / 5, j = m % 5;
          acc[j][i] = MFMA16(cf[PP][j], rf[PP][i], acc[j][i]);
        };
        auto fr = [&](auto rc) { ldf(IC<PP ^ 1>{}, rc, rd); };
        run_sched<SchedP2, 0, 0, 0, DL>(fm, fr);
      };
      auto load0 = [&] { static_for<9>([&](auto ic) { ldf(IC<0>{}, ic, 0); }); };
      consumer<P2_NS, 32>(load0, body);
      STAMP(1);
      auto finish = [&](auto keepc) {
        constexpr int KP = decltype(keepc)::value;
        char* dst = smem + P2_X + w * 10752 + lane * 16;
#pragma unroll
        for (int j = 0; j < 5; ++j) {
          *(f32x4*)(dst + (2 * j) * 1024) = acc[j][2 * (KP ^ 1)];
          *(f32x4*)(dst + (2 * j + 1) * 1024) = acc[j][2 * (KP ^ 1) + 1];
        }
        wait_vm<0>();
        wait_lgkm0();
        bar();
        const char* srcp = smem + P2_X + (w ^ 2) * 10752 + lane * 16;
        char* pq = ws + WS_PQ + ((size_t)(grp2 * 2 + wm) * 80 + isim * 40 + cb * 5) * 2048 + (32 * KP + l15) * 32 + g * 8;
        f32x4 p0[5], p1[5], sa[5], c1[5];
#pragma unroll
        for (int j = 0; j < 5; ++j) {
          p0[j] = *(const f32x4*)(srcp + (2 * j) * 1024);
          p1[j] = *(const f32x4*)(srcp + (2 * j + 1) * 1024);
          sa[j] = *(const f32x4*)(mod + 16 * j + 4 * g);
          c1[j] = *(const f32x4*)(mod + 80 + 16 * j + 4 * g);
        }
        u32x2 o0[5], o1[5];
#pragma unroll
        for (int j = 0; j < 5; ++j) {
          const f32x4 cs = c1[j] * sgn;
          const f32x4 v0 = cs * (acc[j][2 * KP] + p0[j]) + sa[j] * rs0;
          const f32x4 v1 = cs * (acc[j][2 * KP + 1] + p1[j]) + sa[j] * rs1;
          o0[j] = (u32x2){pk2(v0[0], v0[1]), pk2(v0[2], v0[3])};
          o1[j] = (u32x2){pk2(v1[0], v1[1]), pk2(v1[2], v1[3])};
        }
#pragma unroll
        for (int j = 0; j < 5; ++j) {
          st8u_wt(pq + j * 2048, o0[j]);
          st8u_wt(pq + j * 2048 + 512, o1[j]);
        }
      };
      if (wk == 0) finish(IC<0>{}); else finish(IC<1>{});
    }
    STAMP(2);
    if (!prod) wait_vm<0>();
    bar();
    STAMP(3);
    if (wv > 4) {
      const char* pc;
      unsigned len;
      asm volatile("s_getpc_b64 %0\n.Lmega_pf2_here:\n\ts_mov_b32 %1, .Lmega_pf_end-.Lmega_pf2_here" : "=s"(pc), "=s"(len));
      const unsigned off = (bi * 3 + (wv - 5)) * 1024;
      if (off + 1024 <= len) dma16(lane * 16, (const char*)((uintptr_t)pc & ~(uintptr_t)15) + off, lds0 + L_DUM);
    }
    if (wv == 4) {
      if (lane == 0) add_count(ws, grp2);
      wait_count(ws, grp3, 16);
    }
    bar();
  }

  {
    const char* wdt = ws + WS_WDT + (size_t)cb3 * 204800;
    if (prod) {
      const char* rbase = ws + WS_PQ + (size_t)(t3 >> 6) * (80 * 2048) + w * 4096;
      STAMP(4);
      producer_seam<10, 4, 5>([&](int ks, int buf) {
#pragma unroll
        for (int j = 0; j < 4; ++j) dma16s(lane * 16, rbase + ks * 16384 + j * 1024, lds0 + buf * P3_STAGE + w * 4096 + j * 1024);
      }, [&](int ks, int buf) {
#pragma unroll
        for (int j = 0; j < 5; ++j)
          dma16(lane * 16, wdt + ks * 20480 + w * 5120 + j * 1024, lds0 + buf * P3_STAGE + 16384 + w * 5120 + j * 1024);
      }, [] {});
      bar();
      const char* w1t = ws + WS_W1T + (size_t)cb4 * 122880;
      dma16(w * 1024 + lane * 16, ws + WS_E, lds0 + P4_E + w * 1024);
#pragma unroll
      for (int s = 0; s < PFD; ++s)
#pragma unroll
        for (int j = 0; j < 3; ++j)
          dma16(lane * 16, w1t + s * 12288 + w * 3072 + j * 1024, lds0 + s * P4_STAGE + 16384 + w * 3072 + j * 1024);
    } else {
      const int sw0 = (g ^ (l15 >> 1)) * 16, sw1 = ((4 + g) ^ (l15 >> 1)) * 16;
      const int ra = (g >> 1) * 2048 + (16 * w + l15) * 32 + (g & 1) * 16, ca_ = 16384 + l15 * 128;
      bf16x8 rf[2][2], cf[2][2][5];
      f32x4 acc[5];
#pragma unroll
      for (int j = 0; j < 5; ++j) acc[j] = (f32x4){0.f, 0.f, 0.f, 0.f};
      auto ldf = [&](auto hc, auto ic, int buf) {
        constexpr int H = decltype(hc)::value, n = decltype(ic)::value, kk = n / 6, m = n % 6;
        const char* lb = smem + buf * P3_STAGE;
        if constexpr (m == 0) rf[H][kk] = lds_frag(lb + ra + H * 8192 + kk * 4096);
        else cf[H][kk][m - 1] = lds_frag(lb + (kk ? sw1 : sw0) + ca_ + H * 10240 + (m - 1) * 2048);
      };
      auto half = [&](auto par, auto dl, int buf) {
        constexpr int PP = decltype(par)::value, DL = decltype(dl)::value;
        auto fm = [&](auto mc) {
          constexpr int m = decltype(mc)::value, kk = m / 5, j = m % 5;
          acc[j] = MFMA16(cf[PP][kk][j], rf[PP][kk], acc[j]);
        };
        auto fr = [&](auto rc) { ldf(IC<PP ^ 1>{}, rc, buf); };
        run_sched<SchedP3, 0, 0, 0, DL>(fm, fr);
      };
      char* res = smem + L_RES + (16 * w + l15) * 160 + g * 8;
      u32x2 xr[5];
      f32x4 ga[5], c1[5];
#pragma unroll
      for (int j = 0; j < 5; ++j) {
        xr[j] = *(const u32x2*)(res + j * 32);
        ga[j] = *(const f32x4*)(mod + 160 + 16 * j + 4 * g);
        c1[j] = *(const f32x4*)(mod + 240 + 16 * j + 4 * g);
      }
      auto load0 = [&] { STAMP(4); static_for<12>([&](auto ic) { ldf(IC<0>{}, ic, 0); }); };
      consumer_half<10>(load0, half);
      STAMP(5);
      bar();
      char* xc = ws + WS_XC + ((size_t)(t3 >> 6) * 40 + cb3 * 5) * 2048 + (16 * w + l15) * 32 + g * 8;
      float xf[20];
      float s = 0.f;
#pragma unroll
      for (int j = 0; j < 5; ++j) {
        const f32x4 y = acc[j];
        const u32x2 o = {pk2(lo16(xr[j][0]) + ga[j][0] * y[0], hi16(xr[j][0]) + ga[j][1] * y[1]),
                         pk2(lo16(xr[j][1]) + ga[j][2] * y[2], hi16(xr[j][1]) + ga[j][3] * y[3])};
        xr[j] = o;
        xf[4 * j] = lo16(o[0]); xf[4 * j + 1] = hi16(o[0]); xf[4 * j + 2] = lo16(o[1]); xf[4 * j + 3] = hi16(o[1]);
        st8u_wt(xc + j * 2048, (u32x2){pk2(xf[4 * j] * c1[j][0], xf[4 * j + 1] * c1[j][1]), pk2(xf[4 * j + 2] * c1[j][2], xf[4 * j + 3] * c1[j][3])});
        s += (xf[4 * j] + xf[4 * j + 1]) + (xf[4 * j + 2] + xf[4 * j + 3]);
      }
      s += __shfl_xor(s, 16, 64);
      s += __shfl_xor(s, 32, 64);
      const float ml = s * (1.0f / 80.0f);
      float m2 = 0.f;
#pragma unroll
      for (int e = 0; e < 20; ++e) { float d = xf[e] - ml; m2 += d * d; }
      m2 += __shfl_xor(m2, 16, 64);
      m2 += __shfl_xor(m2, 32, 64);
      if (lane < 16) st8_wt(ws + WS_ST + ((size_t)(t3 >> 6) * 8 + cb3) * 512 + (16 * w + l15) * 8, (f32x2){s, m2});
#pragma unroll
      for (int j = 0; j < 5; ++j) *(u32x2*)(res + j * 32) = xr[j];
      STAMP(6);
      wait_vm<0>();
    }
    bar();
    STAMP(7);
    if (wv == 4) {
      if (lane == 0) add_count(ws, 16 + grp3);
      wait_count(ws, 16 + grp4, 16);
    }
    bar();
  }

  {
    const int t0 = grp4 * 128;
    const char* w1t = ws + WS_W1T + (size_t)cb4 * 122880;
    if (prod) {
      const char* rbase = ws + WS_XC + (size_t)(grp4 * 2 + (w >> 1)) * (40 * 2048) + (w & 1) * 4096;
      STAMP(8);
      dma16s(w * 2048 + lane * 16, ws + WS_ST + (size_t)t0 * 64, lds0 + P4_ST + w * 2048);
      dma16s(w * 2048 + 1024 + lane * 16, ws + WS_ST + (size_t)t0 * 64, lds0 + P4_ST + w * 2048 + 1024);
      producer_seam<10, 4, 3>([&](int ks, int buf) {
#pragma unroll
        for (int j = 0; j < 4; ++j) dma16s(lane * 16, rbase + ks * 8192 + j * 1024, lds0 + buf * P4_STAGE + w * 4096 + j * 1024);
      }, [&](int ks, int buf) {
#pragma unroll
        for (int j = 0; j < 3; ++j)
          dma16(lane * 16, w1t + ks * 12288 + w * 3072 + j * 1024, lds0 + buf * P4_STAGE + 16384 + w * 3072 + j * 1024);
      }, [&] {
        if (tid < 128) {
          const char* sp = smem + P4_ST + (tid >> 6) * 4096 + (tid & 63) * 8;
          f32x2 pp[8];
#pragma unroll
          for (int e = 0; e < 8; ++e) pp[e] = *(const f32x2*)(sp + e * 512);
          const float mean = (((pp[0][0] + pp[1][0]) + (pp[2][0] + pp[3][0])) + ((pp[4][0] + pp[5][0]) + (pp[6][0] + pp[7][0]))) * (1.0f / D_);
          float m2 = ((pp[0][1] + pp[1][1]) + (pp[2][1] + pp[3][1])) + ((pp[4][1] + pp[5][1]) + (pp[6][1] + pp[7][1]));
          float dd = 0.f;
#pragma unroll
          for (int e = 0; e < 8; ++e) { const float d = pp[e][0] * (1.0f / 80.0f) - mean; dd += d * d; }
          m2 += 80.0f * dd;
          const float rden = 1.0f / bfr(sqrtf(bfr(bfr(m2 * (1.0f / D_)) + 1e-5f)));
          *(f32x2*)(smem + P4_RS + tid * 8) = (f32x2){bfr(mean), rden};
        }
        wait_lgkm0();
      });
      bar();
      const char* w2t = ws + WS_W2T + (size_t)cb3 * F_ * 160;
#pragma unroll
      for (int s = 0; s < PFD; ++s)
#pragma unroll
        for (int j = 0; j < 5; ++j)
          dma16(lane * 16, w2t + s * 20480 + w * 5120 + j * 1024, lds0 + s * P3_STAGE + 16384 + w * 5120 + j * 1024);
    } else {
      const int q = l15 >> 2, p4 = lane & 3;
      const int wm = w & 1, wn = w >> 1;
      const int rpos = 16 * (g >> 1) + 4 * (g & 1) + q;
      const int xs = (rpos >> 2) & 1;
      int cadr[3];
#pragma unroll
      for (int j = 0; j < 3; ++j) {
        const int jj = 3 * wn + j;
        cadr[j] = 16384 + rpos * 192 + (2 * (jj ^ xs) + (p4 >> 1)) * 16 + (p4 & 1) * 8;
      }
      const int ra = wm * 8192 + (g >> 1) * 2048 + l15 * 32 + (g & 1) * 16;
      int ea = P4_E + (l15 < 2 ? l15 : 2) * 1280 + g * 16;
      bf16x8 rf[2][2][5], cf[2][2][3];
      f32x4 acc[3][5];
#pragma unroll
      for (int j = 0; j < 3; ++j)
#pragma unroll
        for (int i = 0; i < 5; ++i) acc[j][i] = (f32x4){0.f, 0.f, 0.f, 0.f};
      auto ldf = [&](auto par, auto ic, int buf) {
        constexpr int PP = decltype(par)::value, n = decltype(ic)::value, kk = n / 8, m = n % 8;
        const char* lb = smem + buf * P4_STAGE;
        if constexpr (m == 0) rf[PP][kk][0] = lds_frag(lb + ra + kk * 4096);
        else if constexpr (m < 4) cf[PP][kk][m - 1] = cat_frag(lds_tr(lb + cadr[m - 1] + kk * 6144), lds_tr(lb + cadr[m - 1] + kk * 6144 + 1536));
        else if constexpr (m < 7) rf[PP][kk][m - 3] = lds_frag(lb + ra + kk * 4096 + (m - 3) * 512);
        else {
          rf[PP][kk][4] = lds_frag(smem + ea + kk * 64);
          if constexpr (kk == 1) ea += 128;
        }
      };
      auto body = [&](auto par, auto dl, int rd) {
        constexpr int PP = decltype(par)::value, DL = decltype(dl)::value;
        auto fm = [&](auto mc) {
          constexpr int m = decltype(mc)::value, kk = m / 15, i = (m % 15) / 3, j = m % 3;
          acc[j][i] = MFMA16(cf[PP][kk][j], rf[PP][kk][i], acc[j][i]);
        };
        auto fr = [&](auto rc) { ldf(IC<PP ^ 1>{}, rc, rd); };
        run_sched<SchedP4, 0, 0, 0, DL>(fm, fr);
      };
      auto load0 = [&] { STAMP(8); static_for<16>([&](auto ic) { ldf(IC<0>{}, ic, 0); }); };
      consumer<4, 10>(load0, body);
      STAMP(9);
      bar();
      f32x4 u[3], v[3];
#pragma unroll
      for (int j = 0; j < 3; ++j)
#pragma unroll
        for (int e = 0; e < 4; ++e) {
          const int a = __float_as_int(acc[j][4][e]);
          u[j][e] = __int_as_float(__builtin_amdgcn_update_dpp(0, a, 0x150, 0xf, 0xf, false));
          v[j][e] = __int_as_float(__builtin_amdgcn_update_dpp(0, a, 0x151, 0xf, 0xf, false));
        }
      char* h1 = ws + WS_H1 + ((size_t)(grp4 * 2 + wm) * 96 + cb4 * 6 + 3 * wn) * 2048 + l15 * 32 + g * 8;
      f32x2 rsv[4];
#pragma unroll
      for (int i = 0; i < 4; ++i) rsv[i] = *(const f32x2*)(smem + P4_RS + (64 * wm + 16 * i + l15) * 8);
#pragma unroll
      for (int i = 0; i < 4; ++i) {
        const f32x2 rs = rsv[i];
#pragma unroll
        for (int j = 0; j < 3; ++j) {
          const f32x4 hv = (acc[j][i] - u[j] * rs[0]) * rs[1] + v[j];
          st8u_wt(h1 + j * 2048 + i * 512,
                  (u32x2){pk2(fmaxf(hv[0], 0.f), fmaxf(hv[1], 0.f)), pk2(fmaxf(hv[2], 0.f), fmaxf(hv[3], 0.f))});
        }
      }
      STAMP(10);
      wait_vm<0>();
    }
    bar();
    STAMP(11);
    if (wv == 4) {
      if (lane == 0) add_count(ws, 32 + grp4);
      wait_count(ws, 32 + grp3, 16);
    }
    bar();
  }

  {
    const char* w2t = ws + WS_W2T + (size_t)cb3 * F_ * 160;
    if (prod) {
      const char* rbase = ws + WS_H1 + (size_t)(t3 >> 6) * (96 * 2048) + w * 4096;
      STAMP(12);
      producer_seam<12, 4, 5>([&](int ks, int buf) {
#pragma unroll
        for (int j = 0; j < 4; ++j) dma16s(lane * 16, rbase + ks * 16384 + j * 1024, lds0 + buf * P3_STAGE + w * 4096 + j * 1024);
      }, [&](int ks, int buf) {
#pragma unroll
        for (int j = 0; j < 5; ++j)
          dma16(lane * 16, w2t + ks * 20480 + w * 5120 + j * 1024, lds0 + buf * P3_STAGE + 16384 + w * 5120 + j * 1024);
      }, [] {});
    } else {
      const int q = l15 >> 2, p4 = lane & 3;
      const int ra = (g >> 1) * 2048 + (16 * w + l15) * 32 + (g & 1) * 16;
      const int ca_ = 16384 + (16 * (g >> 1) + 4 * (g & 1) + q) * 160 + p4 * 8;
      bf16x8 rf[2][2], cf[2][2][5];
      f32x4 acc[5];
#pragma unroll
      for (int j = 0; j < 5; ++j) acc[j] = (f32x4){0.f, 0.f, 0.f, 0.f};
      auto ldf = [&](auto hc, auto ic, int buf) {
        constexpr int H = decltype(hc)::value, n = decltype(ic)::value, kk = n / 6, m = n % 6;
        const char* lb = smem + buf * P3_STAGE;
        if constexpr (m == 0) rf[H][kk] = lds_frag(lb + ra + H * 8192 + kk * 4096);
        else cf[H][kk][m - 1] = cat_frag(lds_tr(lb + ca_ + H * 10240 + kk * 5120 + (m - 1) * 32),
                                         lds_tr(lb + ca_ + H * 10240 + kk * 5120 + (m - 1) * 32 + 1280));
      };
      auto half = [&](auto par, auto dl, int buf) {
        constexpr int PP = decltype(par)::value, DL = decltype(dl)::value;
        auto fm = [&](auto mc) {
          constexpr int m = decltype(mc)::value, kk = m / 5, j = m % 5;
          acc[j] = MFMA16(cf[PP][kk][j], rf[PP][kk], acc[j]);
        };
        auto fr = [&](auto rc) { ldf(IC<PP ^ 1>{}, rc, buf); };
        run_sched<SchedP3, 0, 0, 0, DL>(fm, fr);
      };
      char* res = smem + L_RES + (16 * w + l15) * 160 + g * 8;
      u32x2 xr[5];
      f32x4 gb[5];
#pragma unroll
      for (int j = 0; j < 5; ++j) {
        xr[j] = *(const u32x2*)(res + j * 32);
        gb[j] = *(const f32x4*)(mod + 320 + 16 * j + 4 * g);
      }
      auto load0 = [&] { STAMP(12); static_for<12>([&](auto ic) { ldf(IC<0>{}, ic, 0); }); };
      consumer_half<12>(load0, half);
      STAMP(13);
#pragma unroll
      for (int j = 0; j < 5; ++j) {
        const f32x4 y = acc[j];
        *(u32x2*)(res + j * 32) = (u32x2){pk2(lo16(xr[j][0]) + gb[j][0] * y[0], hi16(xr[j][0]) + gb[j][1] * y[1]),
                                          pk2(lo16(xr[j][1]) + gb[j][2] * y[2], hi16(xr[j][1]) + gb[j][3] * y[3])};
      }
      wait_vm<0>();
    }
    wait_lgkm0();
    bar();
    for (int id = tid; id < 640; id += 512) {
      const int row = id / 10, c = id - row * 10;
      *(u32x4*)((char*)out + (size_t)(t3 + row) * (D_ * 2) + cb3 * 160 + c * 16) = *(const u32x4*)(smem + L_RES + row * 160 + c * 16);
    }
  }
  STAMP(14);
  STAMP_OUT();
  asm volatile(".Lmega_pf_end:");
}

extern "C" void kernel_launch(void* const* d_in, const int* in_sizes, int n_in,
                              void* d_out, int out_size, void* d_ws, size_t ws_size,
                              hipStream_t stream) {
  const unsigned short* x    = (const unsigned short*)d_in[0];
  const unsigned short* wsre = (const unsigned short*)d_in[1];
  const unsigned short* wsim = (const unsigned short*)d_in[2];
  const unsigned short* wdre = (const unsigned short*)d_in[3];
  const unsigned short* wdim = (const unsigned short*)d_in[4];
  const unsigned short* w1   = (const unsigned short*)d_in[6];
  const unsigned short* w2   = (const unsigned short*)d_in[7];
  const unsigned short* c    = (const unsigned short*)d_in[8];
  const unsigned short* wmod = (const unsigned short*)d_in[9];
  char* ws = (char*)d_ws;
  unsigned short* out = (unsigned short*)d_out;
  (void)hipFuncSetAttribute((const void*)mega, hipFuncAttributeMaxDynamicSharedMemorySize, L_TOTAL);
  k1_ln_mod<<<640 + 64 * WPF_KB + 24, 256, 0, stream>>>(x, c, wmod, wsre, wsim, w2, wdre, wdim, w1, out, ws);
  mega<<<256, 512, L_TOTAL, stream>>>(wsre, wsim, x, ws, out);
}
```

```cpp
#include <hip/hip_runtime.h>
#include <hip/hip_bf16.h>
#include <stdint.h>

typedef __attribute__((ext_vector_type(8))) __bf16 bf16x8;
typedef __attribute__((ext_vector_type(4))) __bf16 bf16x4;
typedef __attribute__((ext_vector_type(2))) __bf16 bf16x2;
typedef __attribute__((ext_vector_type(4))) float f32x4;
typedef __attribute__((ext_vector_type(2))) float f32x2;
typedef __attribute__((ext_vector_type(4))) short s16x4;
typedef __attribute__((ext_vector_type(8))) short s16x8;
typedef __attribute__((ext_vector_type(4))) unsigned u32x4;
typedef __attribute__((ext_vector_type(2))) unsigned u32x2;
typedef __attribute__((address_space(3))) s16x4* lds_s16x4_ptr;
typedef __attribute__((address_space(3))) void* lds_vptr;

constexpr int S_ = 2048, D_ = 640, F_ = 1536, MODN = 3840;

constexpr size_t WS_MOD = 0;
constexpr size_t WS_E   = 16384;
constexpr size_t WS_CNT = 20480;
constexpr size_t WS_ST  = 32768;
constexpr size_t WS_RSUM = WS_ST + (size_t)S_ * 64;
constexpr size_t WS_HT  = WS_RSUM + 16384;
constexpr size_t WS_PQ  = WS_HT + (size_t)D_ * S_ * 2;
constexpr size_t WS_XC  = WS_PQ + (size_t)S_ * 1280 * 2;
constexpr size_t WS_H1  = WS_XC + (size_t)S_ * D_ * 2;
constexpr size_t WS_W2T = WS_H1 + (size_t)S_ * F_ * 2;
constexpr size_t WS_W1T = WS_W2T + (size_t)F_ * D_ * 2;
constexpr size_t WS_WDT = WS_W1T + (size_t)F_ * D_ * 2;

template <int V> struct IC { static constexpr int value = V; };
template <int N, int I = 0, class F> __device__ __forceinline__ void static_for(F f) {
  if constexpr (I < N) { f(IC<I>{}); static_for<N, I + 1>(f); }
}

__device__ __forceinline__ float bf2f(unsigned short u) { return __uint_as_float(((unsigned)u) << 16); }
__device__ __forceinline__ float bfr(float f) { return (float)(__bf16)f; }
__device__ __forceinline__ unsigned pk2(float a, float b) {
  bf16x2 v = __builtin_convertvector((f32x2){a, b}, bf16x2);
  return __builtin_bit_cast(unsigned, v);
}
__device__ __forceinline__ float lo16(unsigned u) { return __uint_as_float(u << 16); }
__device__ __forceinline__ float hi16(unsigned u) { return __uint_as_float(u & 0xffff0000u); }

__device__ __forceinline__ void dma16(unsigned voff, const char* sbase, unsigned lds) {
  asm volatile("s_mov_b32 m0, %2\n\ts_nop 0\n\tglobal_load_lds_dwordx4 %0, %1"
               ::"v"(voff), "s"(sbase), "s"(lds) : "memory");
}
__device__ __forceinline__ void dma16nt(unsigned voff, const char* sbase, unsigned lds) {
  asm volatile("s_mov_b32 m0, %2\n\ts_nop 0\n\tglobal_load_lds_dwordx4 %0, %1 nt"
               ::"v"(voff), "s"(sbase), "s"(lds) : "memory");
}
__device__ __forceinline__ void dma16s(unsigned voff, const char* sbase, unsigned lds) {
  asm volatile("s_mov_b32 m0, %2\n\ts_nop 0\n\tglobal_load_lds_dwordx4 %0, %1 sc1"
               ::"v"(voff), "s"(sbase), "s"(lds) : "memory");
}
#define LD1(p) __builtin_nontemporal_load(p)
__device__ __forceinline__ void dma16h(unsigned voff, const char* sbase, unsigned lds) {
  asm volatile("s_mov_b32 m0, %2\n\ts_mov_b32 exec_hi, 0\n\tglobal_load_lds_dwordx4 %0, %1\n\ts_mov_b32 exec_hi, -1"
               ::"v"(voff), "s"(sbase), "s"(lds) : "memory");
}
__device__ __forceinline__ unsigned lds_off(const void* p) {
  return (unsigned)(uintptr_t)(__attribute__((address_space(3))) const char*)p;
}
#define SB0() __builtin_amdgcn_sched_barrier(0)
template <int N> __device__ __forceinline__ void wait_vm_lgkm0() {
  asm volatile("s_waitcnt vmcnt(%0) lgkmcnt(0)" ::"n"(N) : "memory");
}
__device__ __forceinline__ void bar() {
  asm volatile("" ::: "memory");
  __builtin_amdgcn_s_barrier();
  asm volatile("" ::: "memory");
}
__device__ __forceinline__ bf16x8 lds_frag(const char* p) { return *(const bf16x8*)p; }
__device__ __forceinline__ s16x4 lds_tr(const char* p) {
  return __builtin_amdgcn_ds_read_tr16_b64_v4i16((lds_s16x4_ptr)p);
}
__device__ __forceinline__ bf16x8 cat_frag(s16x4 a, s16x4 b) {
  s16x8 v = __builtin_shufflevector(a, b, 0, 1, 2, 3, 4, 5, 6, 7);
  return __builtin_bit_cast(bf16x8, v);
}
#define MFMA16(a, b, c) __builtin_amdgcn_mfma_f32_16x16x32_bf16((a), (b), (c), 0, 0, 0)

template <class Sc, int POS, int NM, int NR, int DL, class FM, class FR>
__device__ __forceinline__ void run_sched(FM& fm, FR& fr) {
  if constexpr (Sc::s[POS] != 0) {
    constexpr char c = Sc::s[POS];
    if constexpr (c == 'M') {
      fm(IC<NM>{}); SB0();
      run_sched<Sc, POS + 1, NM + 1, NR, DL>(fm, fr);
    } else {
      if constexpr (DL) { fr(IC<NR>{}); SB0(); }
      run_sched<Sc, POS + 1, NM, NR + 1, DL>(fm, fr);
    }
  }
}
template <int N> __device__ __forceinline__ void wait_vm() { asm volatile("s_waitcnt vmcnt(%0)" ::"n"(N) : "memory"); }
__device__ __forceinline__ void wait_lgkm0() { asm volatile("s_waitcnt lgkmcnt(0)" ::: "memory"); }

template <int NS, int P, int TI>
__device__ __forceinline__ void prod_tail() {
  if constexpr (TI < NS - 1) {
    wait_vm<P*(NS - 2 - TI)>();
    bar();
    prod_tail<NS, P, TI + 1>();
  }
}
constexpr int ramp_A(int j, int NS) {
  int a = 2;
  for (int i = 1; i <= j; ++i) { int lim = (i == 1) ? NS : (i - 1) + NS; a = (a + 2 < lim) ? a + 2 : lim; }
  return a;
}
constexpr int ramp_len(int NS) { int j = 1; while (ramp_A(j, NS) != ((j == 1) ? NS : (j - 1) + NS) || j < 2) ++j; return j; }
template <int NS, int P, int J, int R, class Issue>
__device__ __forceinline__ void prod_ramp(Issue& issue) {
  if constexpr (J < R) {
    constexpr int a0 = (J == 0) ? 0 : ramp_A(J - 1, NS), a1 = ramp_A(J, NS);
#pragma unroll
    for (int s = a0; s < a1; ++s) issue(s, s % NS);
    wait_vm<P*(a1 - (J + 1))>();
    bar();
    prod_ramp<NS, P, J + 1, R>(issue);
  }
}
template <int NS, int NK, int P, class Issue>
__device__ __forceinline__ void producer(Issue issue) {
  constexpr int R = ramp_len(NS);
  static_assert(R - 1 + NS <= NK, "ramp too long");
  prod_ramp<NS, P, 0, R>(issue);
#pragma unroll
  for (int s = ramp_A(R - 1, NS); s < R - 1 + NS; ++s) issue(s, s % NS);
  int wr = (R - 1) % NS;
#pragma nounroll
  for (int i = R - 1; i < NK - NS; ++i) {
    wait_vm<P*(NS - 2)>();
    bar();
    issue(i + NS, wr);
    wr = (wr + 1 == NS) ? 0 : wr + 1;
  }
  prod_tail<NS, P, 0>();
}
template <int NK, int PR, int PC, class IR, class ICC, class Mid>
__device__ __forceinline__ void producer_seam(IR issueR, ICC issueC, Mid mid) {
  constexpr int P = PR + PC;
  issueR(0, 0); issueR(1, 1);
  wait_vm<PR>(); bar();
  issueR(2, 2); issueC(2, 2); issueR(3, 3); issueC(3, 3);
  wait_vm<2 * P>(); bar();
  mid();
  constexpr int J0 = 2;
#pragma nounroll
  for (int j = J0; j <= NK - 3; ++j) {
    issueR(j + 2, (j + 2) & 3); issueC(j + 2, (j + 2) & 3);
    wait_vm<2 * P>(); bar();
  }
  wait_vm<P>(); bar();
  wait_vm<0>(); bar();
}
template <int NS, int NK, class Load0, class Body>
__device__ __forceinline__ void consumer(Load0 load0, Body body) {
  static_assert((NK & 1) == 0, "even number of stages");
  bar();
  load0();
  int rd = 1;
#pragma nounroll
  for (int i = 0; i < NK - 2; i += 2) {
    wait_lgkm0();
    bar();
    SB0();
    body(IC<0>{}, IC<1>{}, rd);
    SB0();
    rd = (rd + 1 == NS) ? 0 : rd + 1;
    wait_lgkm0();
    bar();
    SB0();
    body(IC<1>{}, IC<1>{}, rd);
    SB0();
    rd = (rd + 1 == NS) ? 0 : rd + 1;
  }
  wait_lgkm0();
  bar();
  SB0();
  body(IC<0>{}, IC<1>{}, rd);
  SB0();
  body(IC<1>{}, IC<0>{}, 0);
  SB0();
}
template <int NK, class Load0, class Half>
__device__ __forceinline__ void consumer_half(Load0 load0, Half half) {
  bar();
  load0();
#pragma nounroll
  for (int s = 0; s < NK - 1; ++s) {
    SB0();
    half(IC<0>{}, IC<1>{}, s & 3);
    SB0();
    wait_lgkm0();
    bar();
    SB0();
    half(IC<1>{}, IC<1>{}, (s + 1) & 3);
    SB0();
  }
  half(IC<0>{}, IC<1>{}, (NK - 1) & 3);
  SB0();
  half(IC<1>{}, IC<0>{}, 0);
  SB0();
}
typedef __attribute__((address_space(1))) unsigned gu32;
__device__ __forceinline__ void st16_wt(void* p, u32x4 v) {
  asm volatile("global_store_dwordx4 %0, %1, off sc1\n\ts_nop 1" ::"v"(p), "v"(v) : "memory");
}
__device__ __forceinline__ void st8u_wt(void* p, u32x2 v) {
  asm volatile("global_store_dwordx2 %0, %1, off sc1\n\ts_nop 1" ::"v"(p), "v"(v) : "memory");
}
__device__ __forceinline__ void st8_wt(void* p, f32x2 v) {
  asm volatile("global_store_dwordx2 %0, %1, off sc1\n\ts_nop 1" ::"v"(p), "v"(v) : "memory");
}
__device__ __forceinline__ void wait_count(char* ws, int idx, unsigned need) {
  gu32* cnt = (gu32*)(ws + WS_CNT + idx * 256);
#pragma nounroll
  for (unsigned spins = 0; spins < (1u << 22); ++spins) {
    if ((unsigned)__builtin_amdgcn_readfirstlane(__hip_atomic_load(cnt, __ATOMIC_RELAXED, __HIP_MEMORY_SCOPE_AGENT)) >= need) break;
    __builtin_amdgcn_s_sleep(1);
  }
  asm volatile("" ::: "memory");
}
__device__ __forceinline__ void add_count(char* ws, int idx) {
  __hip_atomic_fetch_add((gu32*)(ws + WS_CNT + idx * 256), 1u, __ATOMIC_RELAXED, __HIP_MEMORY_SCOPE_AGENT);
}
__device__ __forceinline__ int swap23(int r) { return (r & ~12) | ((r & 4) << 1) | ((r & 8) >> 1); }

__global__ __launch_bounds__(256) void k1_ln_mod(const unsigned short* __restrict__ x,
                                                 const unsigned short* __restrict__ c,
                                                 const unsigned short* __restrict__ wmod,
                                                 const unsigned short* __restrict__ wre,
                                                 const unsigned short* __restrict__ wim,
                                                 const unsigned short* __restrict__ w2,
                                                 const unsigned short* __restrict__ dre,
                                                 const unsigned short* __restrict__ dim,
                                                 const unsigned short* __restrict__ w1,
                                                 const unsigned short* __restrict__ outp,
                                                 char* __restrict__ ws) {
  __shared__ __attribute__((aligned(16))) char smem[16 * 1296 + 256];
  const int tid = threadIdx.x, lane = tid & 63;
  const int bid = blockIdx.x;
  if (bid < 128) {
    const int xcd = bid & 7, i = bid >> 3;
    const int rg = xcd * 4 + (i >> 2), sub = i & 3;
    const int row0 = rg * 64 + sub * 16;
    const int r = tid >> 4, j = tid & 15;
    const u32x4* src = (const u32x4*)(x + (size_t)(row0 + r) * D_);
    u32x4 v[5];
#pragma unroll
    for (int q = 0; q < 5; ++q) v[q] = src[j + 16 * q];
    float f[40];
#pragma unroll
    for (int q = 0; q < 5; ++q)
#pragma unroll
      for (int e = 0; e < 4; ++e) { f[q * 8 + 2 * e] = lo16(v[q][e]); f[q * 8 + 2 * e + 1] = hi16(v[q][e]); }
    float s = 0.f;
#pragma unroll
    for (int e = 0; e < 40; ++e) s += f[e];
#pragma unroll
    for (int o = 1; o < 16; o <<= 1) s += __shfl_xor(s, o, 64);
    const float mean = s * (1.0f / D_);
    float s2 = 0.f;
#pragma unroll
    for (int e = 0; e < 40; ++e) { float d = f[e] - mean; s2 += d * d; }
#pragma unroll
    for (int o = 1; o < 16; o <<= 1) s2 += __shfl_xor(s2, o, 64);
    const float mb = bfr(mean);
    const float vb = bfr(s2 * (1.0f / D_));
    const float rden = 1.0f / bfr(sqrtf(bfr(vb + 1e-5f)));
#pragma unroll
    for (int q = 0; q < 5; ++q) {
      u32x4 o;
#pragma unroll
      for (int e = 0; e < 4; ++e) {
        float a = bfr(f[q * 8 + 2 * e] - mb) * rden;
        float b = bfr(f[q * 8 + 2 * e + 1] - mb) * rden;
        o[e] = pk2(a, b);
      }
      *(u32x4*)(smem + r * 1296 + (j + 16 * q) * 16) = o;
    }
    wait_lgkm0();
    bar();
    const int grp = tid >> 4, li = tid & 15, qq = li >> 2, pp = li & 3;
    char* ht = ws + WS_HT + (size_t)(row0 >> 4) * 2560;
#pragma unroll
    for (int it = 0; it < 5; ++it) {
      const int pr = it * 16 + grp;
      const int cblk = pr >> 1, half = pr & 1;
      const char* a0 = smem + (8 * half + qq) * 1296 + (16 * cblk + 4 * pp) * 2;
      s16x4 t0 = lds_tr(a0);
      s16x4 t1 = lds_tr(a0 + 4 * 1296);
      s16x8 o = __builtin_shufflevector(t0, t1, 0, 1, 2, 3, 4, 5, 6, 7);
      const int d = 16 * cblk + li;
      const int cb = d / 80, rr = d - cb * 80;
      *(u32x4*)(ht + (size_t)cb * (128 * 2560) + rr * 32 + half * 16) = __builtin_bit_cast(u32x4, o);
    }
    if (bid == 0 && tid < 96) *(u32x4*)(ws + WS_E + 2560 + tid * 16) = (u32x4){0u, 0u, 0u, 0u};
    if (bid == 1 && tid < 48) *(unsigned*)(ws + WS_CNT + tid * 256) = 0u;
  } else if (bid < 368) {
    const int g = bid - 128;
    const int cgp = (g & 7) * 30 + (g >> 3);
    const int l2 = tid & 1, rg = tid >> 1;
    u32x4 v[5];
    unsigned short cv16[5];
#pragma unroll
    for (int q = 0; q < 5; ++q) {
      const int k = q * 128 + rg;
      v[q] = *(const u32x4*)(wmod + (size_t)k * MODN + cgp * 16 + l2 * 8);
      cv16[q] = c[k];
    }
    float acc[8];
#pragma unroll
    for (int e = 0; e < 8; ++e) acc[e] = 0.f;
#pragma unroll
    for (int q = 0; q < 5; ++q) {
      const float cv = bf2f(cv16[q]);
      const float sv = bfr(cv / (1.0f + __expf(-cv)));
#pragma unroll
      for (int e = 0; e < 4; ++e) {
        acc[2 * e] += sv * lo16(v[q][e]);
        acc[2 * e + 1] += sv * hi16(v[q][e]);
      }
    }
#pragma unroll
    for (int o = 2; o < 64; o <<= 1)
#pragma unroll
      for (int e = 0; e < 8; ++e) acc[e] += __shfl_xor(acc[e], o, 64);
    float* red = (float*)smem;
    if (lane < 2) {
#pragma unroll
      for (int e = 0; e < 8; ++e) red[(tid >> 6) * 16 + lane * 8 + e] = acc[e];
    }
    wait_lgkm0();
    bar();
    if (tid < 16) {
      float m = bfr((red[tid] + red[16 + tid]) + (red[32 + tid] + red[48 + tid]));
      const int n = cgp * 16 + tid;
      const int part = n / D_, d = n - part * D_;
      if (part == 1 || part == 4) m = bfr(1.0f + m);
      ((float*)(ws + WS_MOD))[n] = m;
      if (part == 4) ((unsigned short*)(ws + WS_E))[d] = (unsigned short)(__float_as_uint(m) >> 16);
      if (part == 3) ((unsigned short*)(ws + WS_E))[D_ + d] = (unsigned short)(__float_as_uint(m) >> 16);
    }
  } else if (bid < 464) {
    const int k0 = (bid - 368) * 16;
    u32x4 v[5];
#pragma unroll
    for (int q = 0; q < 5; ++q) v[q] = LD1((const u32x4*)((const char*)w2 + (size_t)k0 * (D_ * 2) + (tid + 256 * q) * 16));
#pragma unroll
    for (int q = 0; q < 5; ++q) {
      const int id = tid + 256 * q;
      const int r = id / 80, ch = id - r * 80;
      const int cb = ch / 10, cc = ch - cb * 10;
      *(u32x4*)(ws + WS_W2T + ((size_t)cb * F_ + k0 + swap23(r)) * 160 + cc * 16) = v[q];
    }
  } else if (bid < 560) {
    const int c0 = (bid - 464) * 1280;
    u32x4 v[5];
#pragma unroll
    for (int q = 0; q < 5; ++q) v[q] = LD1((const u32x4*)((const char*)w1 + (size_t)(c0 + tid + 256 * q) * 16));
#pragma unroll
    for (int q = 0; q < 5; ++q) {
      const int id = c0 + tid + 256 * q;
      const int k = id / 192, ch = id - k * 192;
      const int cb = ch / 12, cc = ch - cb * 12;
      const int r = swap23(k);
      *(u32x4*)(ws + WS_W1T + ((size_t)cb * D_ + r) * 192 + (cc ^ (((r >> 2) & 1) << 1)) * 16) = v[q];
    }
  } else if (bid < 640) {
    const int m = bid >= 600, c0 = (bid - 560 - 40 * m) * 1280;
    u32x4 v[5];
#pragma unroll
    for (int q = 0; q < 5; ++q) v[q] = LD1((const u32x4*)((const char*)(m ? dim : dre) + (size_t)(c0 + tid + 256 * q) * 16));
#pragma unroll
    for (int q = 0; q < 5; ++q) {
      const int id = c0 + tid + 256 * q;
      const int e = id / 80, ch = id - e * 80;
      const int cb = e / 80, row = e - cb * 80;
      *(u32x4*)(ws + WS_WDT + ((size_t)(cb * 20 + m * 10 + (ch >> 3)) * 80 + row) * 128 + ((ch & 7) ^ ((row >> 1) & 7)) * 16) = v[q];
    }
  } else if (bid >= 640 + 256) {
    const int t = ((bid - 640 - 256) >> 3) * 256 + tid;
    const char* a;
    if (t < 384) a = ws + (size_t)t * 65536;
    else if (t < 512) a = (const char*)wre + (size_t)(t - 384) * 65536;
    else if (t < 640) a = (const char*)wim + (size_t)(t - 512) * 65536;
    else if (t < 680) a = (const char*)x + (size_t)(t - 640) * 65536;
    else if (t < 720) a = (const char*)outp + (size_t)(t - 680) * 65536;
    else a = ws;
    unsigned v;
    asm volatile("global_load_dword %0, %1, off" : "=v"(v) : "v"(a) : "memory");
    wait_vm<0>();
    asm volatile("" ::"v"(v));
  } else {
    const int pw = (bid - 640) * 4 + __builtin_amdgcn_readfirstlane(tid >> 6);
    const char* src = (const char*)(pw < 512 ? wre : wim) + (size_t)(pw & 511) * 16384 + lane * 16;
    u32x4 v[16];
#pragma unroll
    for (int i = 0; i < 16; ++i) v[i] = *(const u32x4*)(src + i * 1024);
    float rs[4];
#pragma unroll
    for (int r = 0; r < 4; ++r) {
      float a0 = 0.f, a1 = 0.f;
#pragma unroll
      for (int q = 0; q < 4; ++q)
#pragma unroll
        for (int e = 0; e < 4; ++e) { a0 += lo16(v[4 * r + q][e]); a1 += hi16(v[4 * r + q][e]); }
      rs[r] = a0 + a1;
    }
#pragma unroll
    for (int o = 1; o < 64; o <<= 1)
#pragma unroll
      for (int r = 0; r < 4; ++r) rs[r] += __shfl_xor(rs[r], o, 64);
    if (lane == 0) *(f32x4*)(ws + WS_RSUM + pw * 16) = (f32x4){rs[0], rs[1], rs[2], rs[3]};
  }
}

__device__ __forceinline__ void dma_res_tile(const char* base  , unsigned lds, int w, int lane) {
#pragma unroll
  for (int j = 0; j < 3; ++j) {
    int L = 160 * w + 64 * j + lane;
    if (L > 639) L = 639;
    const int r = L / 10, ch = L - r * 10;
    if (j < 2) dma16(r * (D_ * 2) + ch * 16, base, lds + (160 * w + 64 * j) * 16);
    else dma16h(r * (D_ * 2) + ch * 16, base, lds + (160 * w + 128) * 16);
  }
}
template <int N>
__device__ __forceinline__ void l2_warm(const char* base, unsigned dummy_lds, int lane) {
#pragma unroll
  for (int i = 0; i < N; ++i) dma16(lane * 16, base + i * 1024, dummy_lds);
}

struct SchedP2 { static constexpr char s[] = "MRMRMRMMRMRMRMMRMRMRMMMMMMMMM"; };
struct SchedP3 { static constexpr char s[] = "RRMRMRRMRMRRMRMRRMRMMM"; };
struct SchedP4 { static constexpr char s[] = "MRMRMRMMRMRMRMMRMRMMRMRMRMMRMRMRMMRMRMMMMMMMMM"; };
constexpr int cnt_ch(const char* s, char c) { int n = 0; for (; *s; ++s) n += (*s == c); return n; }
static_assert(cnt_ch(SchedP2::s, 'M') == 20 && cnt_ch(SchedP2::s, 'R') == 9, "");
static_assert(cnt_ch(SchedP3::s, 'M') == 10 && cnt_ch(SchedP3::s, 'R') == 12, "");
static_assert(cnt_ch(SchedP4::s, 'M') == 30 && cnt_ch(SchedP4::s, 'R') == 16, "");
constexpr int L_RES = 147456, L_MOD = 159744, L_DUM = L_MOD + 2048, L_TOTAL = L_DUM + 1024;
constexpr int P2_NS = 5, P2_STAGE = 26624, P2_X = 73728;
constexpr int P3_STAGE = 36864;
constexpr int P4_STAGE = 28672, P4_E = 4 * P4_STAGE, P4_ST = P4_E + 4096, P4_RS = P4_ST + 8192, P4_STG = 2 * P4_STAGE;

__global__ __launch_bounds__(512, 2) void mega(const unsigned short* __restrict__ wre,
                                               const unsigned short* __restrict__ wim,
                                               const unsigned short* __restrict__ x,
                                               char* __restrict__ ws,
                                               unsigned short* __restrict__ out) {
  extern __shared__ __attribute__((aligned(16))) char smem[];
  const int tid = threadIdx.x, lane = tid & 63;
  const int wv = __builtin_amdgcn_readfirstlane(tid >> 6);
  const int w = wv & 3;
  const bool prod = wv < 4;
  const int bid = blockIdx.x;
  const int xcd = bid & 7, bi = bid >> 3;
  const int cb = bi & 7, rsel = bi >> 3;
  const int g = lane >> 4, l15 = lane & 15;
  const unsigned lds0 = lds_off(smem);
  const float* mod = (const float*)(smem + L_MOD);
  const int cb3 = (xcd & 1) * 4 + (bi & 3);
  const int t3 = ((xcd >> 1) * 8 + (bi >> 2)) * 64;
  const int cb4 = (xcd & 1) * 8 + (bi & 7), grp4 = (xcd >> 1) * 4 + (bi >> 3);
  const int grp3 = t3 >> 7;

  {
    const int isim = rsel >> 1;
    const int grp2 = xcd * 2 + (rsel & 1);
    const int t0 = grp2 * 128;
    if (prod) {
      const char* rbase = (const char*)(isim ? wim : wre) + (size_t)t0 * (S_ * 2);
      const char* cbase = ws + WS_HT + (size_t)cb * 32 * 10240;
      unsigned roff[4];
#pragma unroll
      for (int j = 0; j < 4; ++j) {
        const int row = 32 * w + 8 * j + (lane >> 3);
        roff[j] = row * (S_ * 2) + 16 * ((lane & 7) ^ ((row >> 1) & 7));
      }
      auto run = [&](auto ncc) {
        constexpr int NC = decltype(ncc)::value;
        const int c0 = (NC == 3) ? w * 3072 : 6144 + (w - 2) * 2048;
        producer<P2_NS, 32, 4 + NC>([&](int ks, int buf) {
          const unsigned lb = lds0 + buf * P2_STAGE;
#pragma unroll
          for (int j = 0; j < 4; ++j) dma16(roff[j], rbase + ks * 128, lb + (32 * w + 8 * j) * 128);
#pragma unroll
          for (int j = 0; j < NC; ++j) dma16(lane * 16, cbase + ks * 10240 + c0 + j * 1024, lb + 16384 + c0 + j * 1024);
        });
      };
      if (w < 2) run(IC<3>{}); else run(IC<2>{});
      bar();
      const char* wdt = ws + WS_WDT + (size_t)cb3 * 204800;
#pragma unroll
      for (int j = 0; j < 5; ++j) dma16(lane * 16, wdt + w * 5120 + j * 1024, lds0 + 16384 + w * 5120 + j * 1024);
      bar();
#pragma unroll
      for (int s = 1; s < 2; ++s)
#pragma unroll
        for (int j = 0; j < 5; ++j)
          dma16(lane * 16, wdt + s * 20480 + w * 5120 + j * 1024, lds0 + s * P3_STAGE + 16384 + w * 5120 + j * 1024);
    } else {
      {
        const char* pc;
        unsigned len;
        asm volatile("s_getpc_b64 %0\n.Lmega_pf_here:\n\ts_mov_b32 %1, .Lmega_pf_end-.Lmega_pf_here" : "=s"(pc), "=s"(len));
        const unsigned off = (bi * 4 + w) * 1024;
        if (off + 1024 <= len) dma16(lane * 16, (const char*)((uintptr_t)pc & ~(uintptr_t)15) + off, lds0 + L_DUM);
      }
      if (w < 2) {
        int L = 64 * w + lane; if (L > 99) L = 99;
        const int vec = L / 20, part = vec + (vec >= 3);
        dma16(part * 2560 + (vec < 2 ? cb : cb3) * 320 + (L - vec * 20) * 16, ws + WS_MOD, lds0 + L_MOD + w * 1024);
      }
      dma_res_tile((const char*)x + (size_t)t3 * (D_ * 2) + cb3 * 160, lds0 + L_RES, w, lane);
      const int wm = w & 1, wk = w >> 1;
      const int fsw = ((4 * wk + g) ^ (l15 >> 1)) * 16;
      const int ra = (64 * wm + l15) * 128 + fsw;
      const int ca_ = 16384 + (2 * wk + (g >> 1)) * 2560 + l15 * 32 + (g & 1) * 16;
      bf16x8 rf[2][4], cf[2][5];
      f32x4 acc[5][4];
#pragma unroll
      for (int j = 0; j < 5; ++j)
#pragma unroll
        for (int i = 0; i < 4; ++i) acc[j][i] = (f32x4){0.f, 0.f, 0.f, 0.f};
      const float sgn = isim ? -1.0f : 1.0f;
      const float* rsum = (const float*)(ws + WS_RSUM) + isim * 2048 + t0 + 64 * wm + 32 * wk + l15;
      const float rsr0 = rsum[0], rsr1 = rsum[16];
      auto ldf = [&](auto par, auto ic, int buf) {
        constexpr int PP = decltype(par)::value, n = decltype(ic)::value;
        const char* lb = smem + buf * P2_STAGE;
        if constexpr (n == 0) rf[PP][0] = lds_frag(lb + ra);
        else if constexpr (n < 6) cf[PP][n - 1] = lds_frag(lb + ca_ + (n - 1) * 512);
        else rf[PP][n - 5] = lds_frag(lb + ra + (n - 5) * 2048);
      };
      auto body = [&](auto par, auto dl, int rd) {
        constexpr int PP = decltype(par)::value, DL = decltype(dl)::value;
        auto fm = [&](auto mc) {
          constexpr int m = decltype(mc)::value, i = m / 5, j = m % 5;
          acc[j][i] = MFMA16(cf[PP][j], rf[PP][i], acc[j][i]);
        };
        auto fr = [&](auto rc) { ldf(IC<PP ^ 1>{}, rc, rd); };
        run_sched<SchedP2, 0, 0, 0, DL>(fm, fr);
      };
      auto load0 = [&] { static_for<9>([&](auto ic) { ldf(IC<0>{}, ic, 0); }); };
      consumer<P2_NS, 32>(load0, body);
      auto finish = [&](auto keepc) {
        constexpr int KP = decltype(keepc)::value;
        bar();
        char* dst = smem + w * P3_STAGE + lane * 16;
#pragma unroll
        for (int j = 0; j < 5; ++j) {
          *(f32x4*)(dst + (2 * j) * 1024) = acc[j][2 * (KP ^ 1)];
          *(f32x4*)(dst + (2 * j + 1) * 1024) = acc[j][2 * (KP ^ 1) + 1];
        }
        wait_vm<0>();
        wait_lgkm0();
        bar();
        const char* srcp = smem + (w ^ 2) * P3_STAGE + lane * 16;
        char* pq = ws + WS_PQ + ((size_t)(grp2 * 2 + wm) * 80 + isim * 40 + cb * 5) * 2048 + (32 * KP + l15) * 32 + g * 8;
        const float rs0 = rsr0 * sgn, rs1 = rsr1 * sgn;
        f32x4 p0[5], p1[5], sa[5], c1[5];
#pragma unroll
        for (int j = 0; j < 5; ++j) {
          p0[j] = *(const f32x4*)(srcp + (2 * j) * 1024);
          p1[j] = *(const f32x4*)(srcp + (2 * j + 1) * 1024);
          sa[j] = *(const f32x4*)(mod + 16 * j + 4 * g);
          c1[j] = *(const f32x4*)(mod + 80 + 16 * j + 4 * g);
        }
        u32x2 o0[5], o1[5];
#pragma unroll
        for (int j = 0; j < 5; ++j) {
          const f32x4 cs = c1[j] * sgn;
          const f32x4 v0 = cs * (acc[j][2 * KP] + p0[j]) + sa[j] * rs0;
          const f32x4 v1 = cs * (acc[j][2 * KP + 1] + p1[j]) + sa[j] * rs1;
          o0[j] = (u32x2){pk2(v0[0], v0[1]), pk2(v0[2], v0[3])};
          o1[j] = (u32x2){pk2(v1[0], v1[1]), pk2(v1[2], v1[3])};
        }
#pragma unroll
        for (int j = 0; j < 5; ++j) {
          st8u_wt(pq + j * 2048, o0[j]);
          st8u_wt(pq + j * 2048 + 512, o1[j]);
        }
      };
      if (wk == 0) finish(IC<0>{}); else finish(IC<1>{});
    }
    if (!prod) wait_vm<0>();
    bar();
    if (wv == 4) {
      if (lane == 0) add_count(ws, grp2);
      wait_count(ws, grp3, 16);
    }
    bar();
  }

  {
    const char* wdt = ws + WS_WDT + (size_t)cb3 * 204800;
    if (prod) {
      const char* rbase = ws + WS_PQ + (size_t)(t3 >> 6) * (80 * 2048) + w * 4096;
      producer_seam<10, 4, 5>([&](int ks, int buf) {
#pragma unroll
        for (int j = 0; j < 4; ++j) dma16s(lane * 16, rbase + ks * 16384 + j * 1024, lds0 + buf * P3_STAGE + w * 4096 + j * 1024);
      }, [&](int ks, int buf) {
#pragma unroll
        for (int j = 0; j < 5; ++j)
          dma16(lane * 16, wdt + ks * 20480 + w * 5120 + j * 1024, lds0 + buf * P3_STAGE + 16384 + w * 5120 + j * 1024);
      }, [] {});
      bar();
      const char* w1t = ws + WS_W1T + (size_t)cb4 * 122880;
      dma16(w * 1024 + lane * 16, ws + WS_E, lds0 + P4_E + w * 1024);
#pragma unroll
      for (int s = 0; s < 2; ++s)
#pragma unroll
        for (int j = 0; j < 3; ++j)
          dma16(lane * 16, w1t + s * 12288 + w * 3072 + j * 1024, lds0 + s * P4_STAGE + 16384 + w * 3072 + j * 1024);
    } else {
      const int sw0 = (g ^ (l15 >> 1)) * 16, sw1 = ((4 + g) ^ (l15 >> 1)) * 16;
      const int ra = (g >> 1) * 2048 + (16 * w + l15) * 32 + (g & 1) * 16, ca_ = 16384 + l15 * 128;
      bf16x8 rf[2][2], cf[2][2][5];
      f32x4 acc[5];
#pragma unroll
      for (int j = 0; j < 5; ++j) acc[j] = (f32x4){0.f, 0.f, 0.f, 0.f};
      auto ldf = [&](auto hc, auto ic, int buf) {
        constexpr int H = decltype(hc)::value, n = decltype(ic)::value, kk = n / 6, m = n % 6;
        const char* lb = smem + buf * P3_STAGE;
        if constexpr (m == 0) rf[H][kk] = lds_frag(lb + ra + H * 8192 + kk * 4096);
        else cf[H][kk][m - 1] = lds_frag(lb + (kk ? sw1 : sw0) + ca_ + H * 10240 + (m - 1) * 2048);
      };
      auto half = [&](auto par, auto dl, int buf) {
        constexpr int PP = decltype(par)::value, DL = decltype(dl)::value;
        auto fm = [&](auto mc) {
          constexpr int m = decltype(mc)::value, kk = m / 5, j = m % 5;
          acc[j] = MFMA16(cf[PP][kk][j], rf[PP][kk], acc[j]);
        };
        auto fr = [&](auto rc) { ldf(IC<PP ^ 1>{}, rc, buf); };
        run_sched<SchedP3, 0, 0, 0, DL>(fm, fr);
      };
      char* res = smem + L_RES + (16 * w + l15) * 160 + g * 8;
      u32x2 xr[5];
      f32x4 ga[5], c1[5];
#pragma unroll
      for (int j = 0; j < 5; ++j) {
        xr[j] = *(const u32x2*)(res + j * 32);
        ga[j] = *(const f32x4*)(mod + 160 + 16 * j + 4 * g);
        c1[j] = *(const f32x4*)(mod + 240 + 16 * j + 4 * g);
      }
      auto load0 = [&] { static_for<12>([&](auto ic) { ldf(IC<0>{}, ic, 0); }); };
      consumer_half<10>(load0, half);
      bar();
      char* xc = ws + WS_XC + ((size_t)(t3 >> 6) * 40 + cb3 * 5) * 2048 + (16 * w + l15) * 32 + g * 8;
      float xf[20];
      float s = 0.f;
#pragma unroll
      for (int j = 0; j < 5; ++j) {
        const f32x4 y = acc[j];
        const u32x2 o = {pk2(lo16(xr[j][0]) + ga[j][0] * y[0], hi16(xr[j][0]) + ga[j][1] * y[1]),
                         pk2(lo16(xr[j][1]) + ga[j][2] * y[2], hi16(xr[j][1]) + ga[j][3] * y[3])};
        xr[j] = o;
        xf[4 * j] = lo16(o[0]); xf[4 * j + 1] = hi16(o[0]); xf[4 * j + 2] = lo16(o[1]); xf[4 * j + 3] = hi16(o[1]);
        st8u_wt(xc + j * 2048, (u32x2){pk2(xf[4 * j] * c1[j][0], xf[4 * j + 1] * c1[j][1]), pk2(xf[4 * j + 2] * c1[j][2], xf[4 * j + 3] * c1[j][3])});
        s += (xf[4 * j] + xf[4 * j + 1]) + (xf[4 * j + 2] + xf[4 * j + 3]);
      }
      s += __shfl_xor(s, 16, 64);
      s += __shfl_xor(s, 32, 64);
      const float ml = s * (1.0f / 80.0f);
      float m2 = 0.f;
#pragma unroll
      for (int e = 0; e < 20; ++e) { float d = xf[e] - ml; m2 += d * d; }
      m2 += __shfl_xor(m2, 16, 64);
      m2 += __shfl_xor(m2, 32, 64);
      if (lane < 16) st8_wt(ws + WS_ST + ((size_t)(t3 >> 6) * 8 + cb3) * 512 + (16 * w + l15) * 8, (f32x2){s, m2});
#pragma unroll
      for (int j = 0; j < 5; ++j) *(u32x2*)(res + j * 32) = xr[j];
      wait_vm<0>();
    }
    bar();
    if (wv == 4) {
      if (lane == 0) add_count(ws, 16 + grp3);
      wait_count(ws, 16 + grp4, 16);
    }
    bar();
  }

  {
    const int t0 = grp4 * 128;
    const char* w1t = ws + WS_W1T + (size_t)cb4 * 122880;
    if (prod) {
      const char* rbase = ws + WS_XC + (size_t)(grp4 * 2 + (w >> 1)) * (40 * 2048) + (w & 1) * 4096;
      dma16s(w * 2048 + lane * 16, ws + WS_ST + (size_t)t0 * 64, lds0 + P4_ST + w * 2048);
      dma16s(w * 2048 + 1024 + lane * 16, ws + WS_ST + (size_t)t0 * 64, lds0 + P4_ST + w * 2048 + 1024);
      producer_seam<10, 4, 3>([&](int ks, int buf) {
#pragma unroll
        for (int j = 0; j < 4; ++j) dma16s(lane * 16, rbase + ks * 8192 + j * 1024, lds0 + buf * P4_STAGE + w * 4096 + j * 1024);
      }, [&](int ks, int buf) {
#pragma unroll
        for (int j = 0; j < 3; ++j)
          dma16(lane * 16, w1t + ks * 12288 + w * 3072 + j * 1024, lds0 + buf * P4_STAGE + 16384 + w * 3072 + j * 1024);
      }, [&] {
        if (tid < 128) {
          const char* sp = smem + P4_ST + (tid >> 6) * 4096 + (tid & 63) * 8;
          f32x2 pp[8];
#pragma unroll
          for (int e = 0; e < 8; ++e) pp[e] = *(const f32x2*)(sp + e * 512);
          const float mean = (((pp[0][0] + pp[1][0]) + (pp[2][0] + pp[3][0])) + ((pp[4][0] + pp[5][0]) + (pp[6][0] + pp[7][0]))) * (1.0f / D_);
          float m2 = ((pp[0][1] + pp[1][1]) + (pp[2][1] + pp[3][1])) + ((pp[4][1] + pp[5][1]) + (pp[6][1] + pp[7][1]));
          float dd = 0.f;
#pragma unroll
          for (int e = 0; e < 8; ++e) { const float d = pp[e][0] * (1.0f / 80.0f) - mean; dd += d * d; }
          m2 += 80.0f * dd;
          const float rden = 1.0f / bfr(sqrtf(bfr(bfr(m2 * (1.0f / D_)) + 1e-5f)));
          *(f32x2*)(smem + P4_RS + tid * 8) = (f32x2){bfr(mean), rden};
        }
        wait_lgkm0();
      });
      bar();
      const char* w2t = ws + WS_W2T + (size_t)cb3 * F_ * 160;
#pragma unroll
      for (int s = 0; s < 2; ++s)
#pragma unroll
        for (int j = 0; j < 5; ++j)
          dma16(lane * 16, w2t + s * 20480 + w * 5120 + j * 1024, lds0 + s * P3_STAGE + 16384 + w * 5120 + j * 1024);
    } else {
      const int q = l15 >> 2, p4 = lane & 3;
      const int wm = w & 1, wn = w >> 1;
      const int rpos = 16 * (g >> 1) + 4 * (g & 1) + q;
      const int xs = (rpos >> 2) & 1;
      int cadr[3];
#pragma unroll
      for (int j = 0; j < 3; ++j) {
        const int jj = 3 * wn + j;
        cadr[j] = 16384 + rpos * 192 + (2 * (jj ^ xs) + (p4 >> 1)) * 16 + (p4 & 1) * 8;
      }
      const int ra = wm * 8192 + (g >> 1) * 2048 + l15 * 32 + (g & 1) * 16;
      int ea = P4_E + (l15 < 2 ? l15 : 2) * 1280 + g * 16;
      bf16x8 rf[2][2][5], cf[2][2][3];
      f32x4 acc[3][5];
#pragma unroll
      for (int j = 0; j < 3; ++j)
#pragma unroll
        for (int i = 0; i < 5; ++i) acc[j][i] = (f32x4){0.f, 0.f, 0.f, 0.f};
      auto ldf = [&](auto par, auto ic, int buf) {
        constexpr int PP = decltype(par)::value, n = decltype(ic)::value, kk = n / 8, m = n % 8;
        const char* lb = smem + buf * P4_STAGE;
        if constexpr (m == 0) rf[PP][kk][0] = lds_frag(lb + ra + kk * 4096);
        else if constexpr (m < 4) cf[PP][kk][m - 1] = cat_frag(lds_tr(lb + cadr[m - 1] + kk * 6144), lds_tr(lb + cadr[m - 1] + kk * 6144 + 1536));
        else if constexpr (m < 7) rf[PP][kk][m - 3] = lds_frag(lb + ra + kk * 4096 + (m - 3) * 512);
        else {
          rf[PP][kk][4] = lds_frag(smem + ea + kk * 64);
          if constexpr (kk == 1) ea += 128;
        }
      };
      auto body = [&](auto par, auto dl, int rd) {
        constexpr int PP = decltype(par)::value, DL = decltype(dl)::value;
        auto fm = [&](auto mc) {
          constexpr int m = decltype(mc)::value, kk = m / 15, i = (m % 15) / 3, j = m % 3;
          acc[j][i] = MFMA16(cf[PP][kk][j], rf[PP][kk][i], acc[j][i]);
        };
        auto fr = [&](auto rc) { ldf(IC<PP ^ 1>{}, rc, rd); };
        run_sched<SchedP4, 0, 0, 0, DL>(fm, fr);
      };
      auto load0 = [&] { static_for<16>([&](auto ic) { ldf(IC<0>{}, ic, 0); }); };
      consumer<4, 10>(load0, body);
      f32x2 rsv[4];
#pragma unroll
      for (int i = 0; i < 4; ++i) rsv[i] = *(const f32x2*)(smem + P4_RS + (64 * wm + 16 * i + l15) * 8);
      wait_lgkm0();
      bar();
      f32x4 u[3], v[3];
#pragma unroll
      for (int j = 0; j < 3; ++j)
#pragma unroll
        for (int e = 0; e < 4; ++e) {
          const int a = __float_as_int(acc[j][4][e]);
          u[j][e] = __int_as_float(__builtin_amdgcn_update_dpp(0, a, 0x150, 0xf, 0xf, false));
          v[j][e] = __int_as_float(__builtin_amdgcn_update_dpp(0, a, 0x151, 0xf, 0xf, false));
        }
      char* h1 = ws + WS_H1 + ((size_t)(grp4 * 2 + wm) * 96 + cb4 * 6 + 3 * wn) * 2048 + l15 * 32 + g * 8;
#pragma unroll
      for (int i = 0; i < 4; ++i) {
        const f32x2 rs = rsv[i];
#pragma unroll
        for (int j = 0; j < 3; ++j) {
          const f32x4 hv = (acc[j][i] - u[j] * rs[0]) * rs[1] + v[j];
          st8u_wt(h1 + j * 2048 + i * 512,
                  (u32x2){pk2(fmaxf(hv[0], 0.f), fmaxf(hv[1], 0.f)), pk2(fmaxf(hv[2], 0.f), fmaxf(hv[3], 0.f))});
        }
      }
      wait_vm<0>();
    }
    bar();
    if (wv == 4) {
      if (lane == 0) add_count(ws, 32 + grp4);
      wait_count(ws, 32 + grp3, 16);
    }
    bar();
  }

  {
    const char* w2t = ws + WS_W2T + (size_t)cb3 * F_ * 160;
    if (prod) {
      const char* rbase = ws + WS_H1 + (size_t)(t3 >> 6) * (96 * 2048) + w * 4096;
      producer_seam<12, 4, 5>([&](int ks, int buf) {
#pragma unroll
        for (int j = 0; j < 4; ++j) dma16s(lane * 16, rbase + ks * 16384 + j * 1024, lds0 + buf * P3_STAGE + w * 4096 + j * 1024);
      }, [&](int ks, int buf) {
#pragma unroll
        for (int j = 0; j < 5; ++j)
          dma16(lane * 16, w2t + ks * 20480 + w * 5120 + j * 1024, lds0 + buf * P3_STAGE + 16384 + w * 5120 + j * 1024);
      }, [] {});
    } else {
      const int q = l15 >> 2, p4 = lane & 3;
      const int ra = (g >> 1) * 2048 + (16 * w + l15) * 32 + (g & 1) * 16;
      const int ca_ = 16384 + (16 * (g >> 1) + 4 * (g & 1) + q) * 160 + p4 * 8;
      bf16x8 rf[2][2], cf[2][2][5];
      f32x4 acc[5];
#pragma unroll
      for (int j = 0; j < 5; ++j) acc[j] = (f32x4){0.f, 0.f, 0.f, 0.f};
      auto ldf = [&](auto hc, auto ic, int buf) {
        constexpr int H = decltype(hc)::value, n = decltype(ic)::value, kk = n / 6, m = n % 6;
        const char* lb = smem + buf * P3_STAGE;
        if constexpr (m == 0) rf[H][kk] = lds_frag(lb + ra + H * 8192 + kk * 4096);
        else cf[H][kk][m - 1] = cat_frag(lds_tr(lb + ca_ + H * 10240 + kk * 5120 + (m - 1) * 32),
                                         lds_tr(lb + ca_ + H * 10240 + kk * 5120 + (m - 1) * 32 + 1280));
      };
      auto half = [&](auto par, auto dl, int buf) {
        constexpr int PP = decltype(par)::value, DL = decltype(dl)::value;
        auto fm = [&](auto mc) {
          constexpr int m = decltype(mc)::value, kk = m / 5, j = m % 5;
          acc[j] = MFMA16(cf[PP][kk][j], rf[PP][kk], acc[j]);
        };
        auto fr = [&](auto rc) { ldf(IC<PP ^ 1>{}, rc, buf); };
        run_sched<SchedP3, 0, 0, 0, DL>(fm, fr);
      };
      char* res = smem + L_RES + (16 * w + l15) * 160 + g * 8;
      u32x2 xr[5];
      f32x4 gb[5];
#pragma unroll
      for (int j = 0; j < 5; ++j) {
        xr[j] = *(const u32x2*)(res + j * 32);
        gb[j] = *(const f32x4*)(mod + 320 + 16 * j + 4 * g);
      }
      auto load0 = [&] { static_for<12>([&](auto ic) { ldf(IC<0>{}, ic, 0); }); };
      consumer_half<12>(load0, half);
#pragma unroll
      for (int j = 0; j < 5; ++j) {
        const f32x4 y = acc[j];
        *(u32x2*)(res + j * 32) = (u32x2){pk2(lo16(xr[j][0]) + gb[j][0] * y[0], hi16(xr[j][0]) + gb[j][1] * y[1]),
                                          pk2(lo16(xr[j][1]) + gb[j][2] * y[2], hi16(xr[j][1]) + gb[j][3] * y[3])};
      }
      wait_vm<0>();
    }
    wait_lgkm0();
    bar();
    for (int id = tid; id < 640; id += 512) {
      const int row = id / 10, c = id - row * 10;
      *(u32x4*)((char*)out + (size_t)(t3 + row) * (D_ * 2) + cb3 * 160 + c * 16) = *(const u32x4*)(smem + L_RES + row * 160 + c * 16);
    }
  }
  asm volatile(".Lmega_pf_end:");
}

extern "C" void kernel_launch(void* const* d_in, const int* in_sizes, int n_in,
                              void* d_out, int out_size, void* d_ws, size_t ws_size,
                              hipStream_t stream) {
  const unsigned short* x    = (const unsigned short*)d_in[0];
  const unsigned short* wsre = (const unsigned short*)d_in[1];
  const unsigned short* wsim = (const unsigned short*)d_in[2];
  const unsigned short* wdre = (const unsigned short*)d_in[3];
  const unsigned short* wdim = (const unsigned short*)d_in[4];
  const unsigned short* w1   = (const unsigned short*)d_in[6];
  const unsigned short* w2   = (const unsigned short*)d_in[7];
  const unsigned short* c    = (const unsigned short*)d_in[8];
  const unsigned short* wmod = (const unsigned short*)d_in[9];
  char* ws = (char*)d_ws;
  unsigned short* out = (unsigned short*)d_out;
  (void)hipFuncSetAttribute((const void*)mega, hipFuncAttributeMaxDynamicSharedMemorySize, L_TOTAL);
  k1_ln_mod<<<640 + 256 + 24, 256, 0, stream>>>(x, c, wmod, wsre, wsim, w2, wdre, wdim, w1, out, ws);
  mega<<<256, 512, L_TOTAL, stream>>>(wsre, wsim, x, ws, out);
}
```

```cpp
#include <hip/hip_runtime.h>
#include <hip/hip_bf16.h>
#include <stdint.h>

typedef __attribute__((ext_vector_type(8))) __bf16 bf16x8;
typedef __attribute__((ext_vector_type(2))) __bf16 bf16x2;
typedef __attribute__((ext_vector_type(4))) float f32x4;
typedef __attribute__((ext_vector_type(2))) float f32x2;
typedef __attribute__((ext_vector_type(4))) short s16x4;
typedef __attribute__((ext_vector_type(8))) short s16x8;
typedef __attribute__((ext_vector_type(4))) unsigned u32x4;
typedef __attribute__((ext_vector_type(2))) unsigned u32x2;
typedef __attribute__((address_space(3))) s16x4* lds_s16x4_ptr;

constexpr int S_ = 2048, D_ = 640, F_ = 1536, MODN = 3840;

constexpr size_t WS_MOD = 0;
constexpr size_t WS_E   = 16384;
constexpr size_t WS_CNT = 20480;
constexpr size_t WS_ST  = 32768;
constexpr size_t WS_RSUM = WS_ST + (size_t)S_ * 64;
constexpr size_t WS_HT  = WS_RSUM + 16384;
constexpr size_t WS_PQ  = WS_HT + (size_t)D_ * S_ * 2;
constexpr size_t WS_XC  = WS_PQ + (size_t)S_ * 1280 * 2;
constexpr size_t WS_H1  = WS_XC + (size_t)S_ * D_ * 2;
constexpr size_t WS_W2T = WS_H1 + (size_t)S_ * F_ * 2;
constexpr size_t WS_W1T = WS_W2T + (size_t)F_ * D_ * 2;
constexpr size_t WS_WDT = WS_W1T + (size_t)F_ * D_ * 2;

template <int V> struct IC { static constexpr int value = V; };
template <int N, int I = 0, class F> __device__ __forceinline__ void static_for(F f) {
  if constexpr (I < N) { f(IC<I>{}); static_for<N, I + 1>(f); }
}

__device__ __forceinline__ float bf2f(unsigned short u) { return __uint_as_float(((unsigned)u) << 16); }
__device__ __forceinline__ float bfr(float f) { return (float)(__bf16)f; }
__device__ __forceinline__ unsigned pk2(float a, float b) {
  bf16x2 v = __builtin_convertvector((f32x2){a, b}, bf16x2);
  return __builtin_bit_cast(unsigned, v);
}
__device__ __forceinline__ float lo16(unsigned u) { return __uint_as_float(u << 16); }
__device__ __forceinline__ float hi16(unsigned u) { return __uint_as_float(u & 0xffff0000u); }

__device__ __forceinline__ void dma16(unsigned voff, const char* sbase, unsigned lds) {
  asm volatile("s_mov_b32 m0, %2\n\ts_nop 0\n\tglobal_load_lds_dwordx4 %0, %1"
               ::"v"(voff), "s"(sbase), "s"(lds) : "memory");
}
__device__ __forceinline__ void dma16s(unsigned voff, const char* sbase, unsigned lds) {
  asm volatile("s_mov_b32 m0, %2\n\ts_nop 0\n\tglobal_load_lds_dwordx4 %0, %1 sc1"
               ::"v"(voff), "s"(sbase), "s"(lds) : "memory");
}
#define LD1(p) __builtin_nontemporal_load(p)
__device__ __forceinline__ void dma16h(unsigned voff, const char* sbase, unsigned lds) {
  asm volatile("s_mov_b32 m0, %2\n\ts_mov_b32 exec_hi, 0\n\tglobal_load_lds_dwordx4 %0, %1\n\ts_mov_b32 exec_hi, -1"
               ::"v"(voff), "s"(sbase), "s"(lds) : "memory");
}
__device__ __forceinline__ unsigned lds_off(const void* p) {
  return (unsigned)(uintptr_t)(__attribute__((address_space(3))) const char*)p;
}
#define SB0() __builtin_amdgcn_sched_barrier(0)
__device__ __forceinline__ void bar() {
  asm volatile("" ::: "memory");
  __builtin_amdgcn_s_barrier();
  asm volatile("" ::: "memory");
}
__device__ __forceinline__ bf16x8 lds_frag(const char* p) { return *(const bf16x8*)p; }
__device__ __forceinline__ s16x4 lds_tr(const char* p) {
  return __builtin_amdgcn_ds_read_tr16_b64_v4i16((lds_s16x4_ptr)p);
}
__device__ __forceinline__ bf16x8 cat_frag(s16x4 a, s16x4 b) {
  s16x8 v = __builtin_shufflevector(a, b, 0, 1, 2, 3, 4, 5, 6, 7);
  return __builtin_bit_cast(bf16x8, v);
}
#define MFMA16(a, b, c) __builtin_amdgcn_mfma_f32_16x16x32_bf16((a), (b), (c), 0, 0, 0)

template <class Sc, int POS, int NM, int NR, int DL, class FM, class FR>
__device__ __forceinline__ void run_sched(FM& fm, FR& fr) {
  if constexpr (Sc::s[POS] != 0) {
    constexpr char c = Sc::s[POS];
    if constexpr (c == 'M') {
      fm(IC<NM>{}); SB0();
      run_sched<Sc, POS + 1, NM + 1, NR, DL>(fm, fr);
    } else {
      if constexpr (DL) { fr(IC<NR>{}); SB0(); }
      run_sched<Sc, POS + 1, NM, NR + 1, DL>(fm, fr);
    }
  }
}
template <int N> __device__ __forceinline__ void wait_vm() { asm volatile("s_waitcnt vmcnt(%0)" ::"n"(N) : "memory"); }
__device__ __forceinline__ void wait_lgkm0() { asm volatile("s_waitcnt lgkmcnt(0)" ::: "memory"); }

template <int NS, int P, int TI>
__device__ __forceinline__ void prod_tail() {
  if constexpr (TI < NS - 1) {
    wait_vm<P*(NS - 2 - TI)>();
    bar();
    prod_tail<NS, P, TI + 1>();
  }
}
constexpr int ramp_A(int j, int NS) {
  int a = 2;
  for (int i = 1; i <= j; ++i) { int lim = (i == 1) ? NS : (i - 1) + NS; a = (a + 2 < lim) ? a + 2 : lim; }
  return a;
}
constexpr int ramp_len(int NS) { int j = 1; while (ramp_A(j, NS) != ((j == 1) ? NS : (j - 1) + NS) || j < 2) ++j; return j; }
template <int NS, int P, int J, int R, class Issue>
__device__ __forceinline__ void prod_ramp(Issue& issue) {
  if constexpr (J < R) {
    constexpr int a0 = (J == 0) ? 0 : ramp_A(J - 1, NS), a1 = ramp_A(J, NS);
#pragma unroll
    for (int s = a0; s < a1; ++s) issue(s, s % NS);
    wait_vm<P*(a1 - (J + 1))>();
    bar();
    prod_ramp<NS, P, J + 1, R>(issue);
  }
}
template <int NS, int NK, int P, class Issue>
__device__ __forceinline__ void producer(Issue issue) {
  constexpr int R = ramp_len(NS);
  static_assert(R - 1 + NS <= NK, "ramp too long");
  prod_ramp<NS, P, 0, R>(issue);
#pragma unroll
  for (int s = ramp_A(R - 1, NS); s < R - 1 + NS; ++s) issue(s, s % NS);
  int wr = (R - 1) % NS;
#pragma nounroll
  for (int i = R - 1; i < NK - NS; ++i) {
    wait_vm<P*(NS - 2)>();
    bar();
    issue(i + NS, wr);
    wr = (wr + 1 == NS) ? 0 : wr + 1;
  }
  prod_tail<NS, P, 0>();
}
template <int NK, int PR, int PC, class IR, class ICC, class Mid>
__device__ __forceinline__ void producer_seam(IR issueR, ICC issueC, Mid mid) {
  constexpr int P = PR + PC;
  issueR(0, 0); issueR(1, 1);
  wait_vm<PR>(); bar();
  issueR(2, 2); issueC(2, 2); issueR(3, 3); issueC(3, 3);
  wait_vm<2 * P>(); bar();
  mid();
  constexpr int J0 = 2;
#pragma nounroll
  for (int j = J0; j <= NK - 3; ++j) {
    issueR(j + 2, (j + 2) & 3); issueC(j + 2, (j + 2) & 3);
    wait_vm<2 * P>(); bar();
  }
  wait_vm<P>(); bar();
  wait_vm<0>(); bar();
}
template <int NS, int NK, class Load0, class Body>
__device__ __forceinline__ void consumer(Load0 load0, Body body) {
  static_assert((NK & 1) == 0, "even number of stages");
  bar();
  load0();
  int rd = 1;
#pragma nounroll
  for (int i = 0; i < NK - 2; i += 2) {
    wait_lgkm0();
    bar();
    SB0();
    body(IC<0>{}, IC<1>{}, rd);
    SB0();
    rd = (rd + 1 == NS) ? 0 : rd + 1;
    wait_lgkm0();
    bar();
    SB0();
    body(IC<1>{}, IC<1>{}, rd);
    SB0();
    rd = (rd + 1 == NS) ? 0 : rd + 1;
  }
  wait_lgkm0();
  bar();
  SB0();
  body(IC<0>{}, IC<1>{}, rd);
  SB0();
  body(IC<1>{}, IC<0>{}, 0);
  SB0();
}
template <int NK, class Load0, class Half>
__device__ __forceinline__ void consumer_half(Load0 load0, Half half) {
  bar();
  load0();
#pragma nounroll
  for (int s = 0; s < NK - 1; ++s) {
    SB0();
    half(IC<0>{}, IC<1>{}, s & 3);
    SB0();
    wait_lgkm0();
    bar();
    SB0();
    half(IC<1>{}, IC<1>{}, (s + 1) & 3);
    SB0();
  }
  half(IC<0>{}, IC<1>{}, (NK - 1) & 3);
  SB0();
  half(IC<1>{}, IC<0>{}, 0);
  SB0();
}
typedef __attribute__((address_space(1))) unsigned gu32;
__device__ __forceinline__ void st8u_wt(void* p, u32x2 v) {
  asm volatile("global_store_dwordx2 %0, %1, off sc1\n\ts_nop 1" ::"v"(p), "v"(v) : "memory");
}
__device__ __forceinline__ void st8_wt(void* p, f32x2 v) {
  asm volatile("global_store_dwordx2 %0, %1, off sc1\n\ts_nop 1" ::"v"(p), "v"(v) : "memory");
}
__device__ __forceinline__ void wait_count(char* ws, int idx, unsigned need) {
  gu32* cnt = (gu32*)(ws + WS_CNT + idx * 256);
#pragma nounroll
  for (unsigned spins = 0; spins < (1u << 22); ++spins) {
    if ((unsigned)__builtin_amdgcn_readfirstlane(__hip_atomic_load(cnt, __ATOMIC_RELAXED, __HIP_MEMORY_SCOPE_AGENT)) >= need) break;
    __builtin_amdgcn_s_sleep(1);
  }
  asm volatile("" ::: "memory");
}
__device__ __forceinline__ void add_count(char* ws, int idx) {
  __hip_atomic_fetch_add((gu32*)(ws + WS_CNT + idx * 256), 1u, __ATOMIC_RELAXED, __HIP_MEMORY_SCOPE_AGENT);
}
__device__ __forceinline__ int swap23(int r) { return (r & ~12) | ((r & 4) << 1) | ((r & 8) >> 1); }

__global__ __launch_bounds__(256) void k1_ln_mod(const unsigned short* __restrict__ x,
                                                 const unsigned short* __restrict__ c,
                                                 const unsigned short* __restrict__ wmod,
                                                 const unsigned short* __restrict__ wre,
                                                 const unsigned short* __restrict__ wim,
                                                 const unsigned short* __restrict__ w2,
                                                 const unsigned short* __restrict__ dre,
                                                 const unsigned short* __restrict__ dim,
                                                 const unsigned short* __restrict__ w1,
                                                 const unsigned short* __restrict__ outp,
                                                 char* __restrict__ ws) {
  __shared__ __attribute__((aligned(16))) char smem[16 * 1552];
  const int tid = threadIdx.x, lane = tid & 63;
  const int bid = blockIdx.x;
  if (bid < 128) {
    const int xcd = bid & 7, i = bid >> 3;
    const int rg = xcd * 4 + (i >> 2), sub = i & 3;
    const int row0 = rg * 64 + sub * 16;
    const int r = tid >> 4, j = tid & 15;
    const u32x4* src = (const u32x4*)(x + (size_t)(row0 + r) * D_);
    u32x4 v[5];
#pragma unroll
    for (int q = 0; q < 5; ++q) v[q] = src[j + 16 * q];
    float f[40];
#pragma unroll
    for (int q = 0; q < 5; ++q)
#pragma unroll
      for (int e = 0; e < 4; ++e) { f[q * 8 + 2 * e] = lo16(v[q][e]); f[q * 8 + 2 * e + 1] = hi16(v[q][e]); }
    float s = 0.f;
#pragma unroll
    for (int e = 0; e < 40; ++e) s += f[e];
#pragma unroll
    for (int o = 1; o < 16; o <<= 1) s += __shfl_xor(s, o, 64);
    const float mean = s * (1.0f / D_);
    float s2 = 0.f;
#pragma unroll
    for (int e = 0; e < 40; ++e) { float d = f[e] - mean; s2 += d * d; }
#pragma unroll
    for (int o = 1; o < 16; o <<= 1) s2 += __shfl_xor(s2, o, 64);
    const float mb = bfr(mean);
    const float vb = bfr(s2 * (1.0f / D_));
    const float rden = 1.0f / bfr(sqrtf(bfr(vb + 1e-5f)));
#pragma unroll
    for (int q = 0; q < 5; ++q) {
      u32x4 o;
#pragma unroll
      for (int e = 0; e < 4; ++e) {
        float a = bfr(f[q * 8 + 2 * e] - mb) * rden;
        float b = bfr(f[q * 8 + 2 * e + 1] - mb) * rden;
        o[e] = pk2(a, b);
      }
      *(u32x4*)(smem + r * 1296 + (j + 16 * q) * 16) = o;
    }
    wait_lgkm0();
    bar();
    const int grp = tid >> 4, li = tid & 15, qq = li >> 2, pp = li & 3;
    char* ht = ws + WS_HT + (size_t)(row0 >> 4) * 2560;
#pragma unroll
    for (int it = 0; it < 5; ++it) {
      const int pr = it * 16 + grp;
      const int cblk = pr >> 1, half = pr & 1;
      const char* a0 = smem + (8 * half + qq) * 1296 + (16 * cblk + 4 * pp) * 2;
      s16x4 t0 = lds_tr(a0);
      s16x4 t1 = lds_tr(a0 + 4 * 1296);
      s16x8 o = __builtin_shufflevector(t0, t1, 0, 1, 2, 3, 4, 5, 6, 7);
      const int d = 16 * cblk + li;
      const int cb = d / 80, rr = d - cb * 80;
      *(u32x4*)(ht + (size_t)cb * (128 * 2560) + rr * 32 + half * 16) = __builtin_bit_cast(u32x4, o);
    }
    if (bid == 0 && tid < 96) *(u32x4*)(ws + WS_E + 2560 + tid * 16) = (u32x4){0u, 0u, 0u, 0u};
    if (bid == 1 && tid < 48) *(unsigned*)(ws + WS_CNT + tid * 256) = 0u;
  } else if (bid < 368) {
    const int g = bid - 128;
    const int cgp = (g & 7) * 30 + (g >> 3);
    const int l2 = tid & 1, rg = tid >> 1;
    u32x4 v[5];
    unsigned short cv16[5];
#pragma unroll
    for (int q = 0; q < 5; ++q) {
      const int k = q * 128 + rg;
      v[q] = *(const u32x4*)(wmod + (size_t)k * MODN + cgp * 16 + l2 * 8);
      cv16[q] = c[k];
    }
    float acc[8];
#pragma unroll
    for (int e = 0; e < 8; ++e) acc[e] = 0.f;
#pragma unroll
    for (int q = 0; q < 5; ++q) {
      const float cv = bf2f(cv16[q]);
      const float sv = bfr(cv / (1.0f + __expf(-cv)));
#pragma unroll
      for (int e = 0; e < 4; ++e) {
        acc[2 * e] += sv * lo16(v[q][e]);
        acc[2 * e + 1] += sv * hi16(v[q][e]);
      }
    }
#pragma unroll
    for (int o = 2; o < 64; o <<= 1)
#pragma unroll
      for (int e = 0; e < 8; ++e) acc[e] += __shfl_xor(acc[e], o, 64);
    float* red = (float*)smem;
    if (lane < 2) {
#pragma unroll
      for (int e = 0; e < 8; ++e) red[(tid >> 6) * 16 + lane * 8 + e] = acc[e];
    }
    wait_lgkm0();
    bar();
    if (tid < 16) {
      float m = bfr((red[tid] + red[16 + tid]) + (red[32 + tid] + red[48 + tid]));
      const int n = cgp * 16 + tid;
      const int part = n / D_, d = n - part * D_;
      if (part == 1 || part == 4) m = bfr(1.0f + m);
      ((float*)(ws + WS_MOD))[n] = m;
      if (part == 4) ((unsigned short*)(ws + WS_E))[d] = (unsigned short)(__float_as_uint(m) >> 16);
      if (part == 3) ((unsigned short*)(ws + WS_E))[D_ + d] = (unsigned short)(__float_as_uint(m) >> 16);
    }
  } else if (bid < 560) {
    const bool isw2 = bid < 464;
    const int b1 = bid - 464;
    if (isw2 || b1 < 80) {
      const int kg = isw2 ? bid - 368 : (b1 >> 1);
      const int nch = isw2 ? 80 : 96;
      const int n0 = isw2 ? 0 : (b1 & 1) * 768;
      const char* src = isw2 ? (const char*)w2 + (size_t)kg * 16 * (D_ * 2) : (const char*)w1 + (size_t)kg * 16 * (F_ * 2) + n0 * 2;
      const int pitch = isw2 ? D_ * 2 : F_ * 2;
      const int r = tid >> 4, j = tid & 15;
      u32x4 v[6];
#pragma unroll
      for (int q = 0; q < 6; ++q)
        if (q < 5 || !isw2) v[q] = LD1((const u32x4*)(src + (size_t)r * pitch + (j + 16 * q) * 16));
#pragma unroll
      for (int q = 0; q < 6; ++q)
        if (q < 5 || !isw2) *(u32x4*)(smem + r * 1552 + (j + 16 * q) * 16) = v[q];
      wait_lgkm0();
      bar();
      const int grp = tid >> 4, li = tid & 15, qq = li >> 2, pp = li & 3;
#pragma unroll
      for (int it = 0; it < 6; ++it) {
        if (it < 5 || !isw2) {
          const int pr = it * 16 + grp;
          const int cblk = pr >> 1, half = pr & 1;
          const char* a0 = smem + (8 * half + qq) * 1552 + (16 * cblk + 4 * pp) * 2;
          s16x4 t0 = lds_tr(a0);
          s16x4 t1 = lds_tr(a0 + 4 * 1552);
          s16x8 o = __builtin_shufflevector(t0, t1, 0, 1, 2, 3, 4, 5, 6, 7);
          const int n = n0 + 16 * cblk + li;
          const int cbk = n / nch, rr = n - cbk * nch;
          char* dst = isw2 ? ws + WS_W2T + ((size_t)(cbk * 96 + kg) * 80 + rr) * 32 : ws + WS_W1T + ((size_t)(cbk * 40 + kg) * 96 + rr) * 32;
          *(u32x4*)(dst + half * 16) = __builtin_bit_cast(u32x4, o);
        }
      }
    }
  } else if (bid < 640) {
    const int m = bid >= 600, c0 = (bid - 560 - 40 * m) * 1280;
    u32x4 v[5];
#pragma unroll
    for (int q = 0; q < 5; ++q) v[q] = LD1((const u32x4*)((const char*)(m ? dim : dre) + (size_t)(c0 + tid + 256 * q) * 16));
#pragma unroll
    for (int q = 0; q < 5; ++q) {
      const int id = c0 + tid + 256 * q;
      const int e = id / 80, ch = id - e * 80;
      const int cb = e / 80, row = e - cb * 80;
      *(u32x4*)(ws + WS_WDT + ((size_t)(cb * 20 + m * 10 + (ch >> 3)) * 80 + row) * 128 + ((ch & 7) ^ ((row >> 1) & 7)) * 16) = v[q];
    }
  } else if (bid >= 640 + 256) {
    const int t = ((bid - 640 - 256) >> 3) * 256 + tid;
    const char* a;
    if (t < 384) a = ws + (size_t)t * 65536;
    else if (t < 512) a = (const char*)wre + (size_t)(t - 384) * 65536;
    else if (t < 640) a = (const char*)wim + (size_t)(t - 512) * 65536;
    else if (t < 680) a = (const char*)x + (size_t)(t - 640) * 65536;
    else if (t < 720) a = (const char*)outp + (size_t)(t - 680) * 65536;
    else a = ws;
    unsigned v;
    asm volatile("global_load_dword %0, %1, off" : "=v"(v) : "v"(a) : "memory");
    wait_vm<0>();
    asm volatile("" ::"v"(v));
  } else {
    const int pw = (bid - 640) * 4 + __builtin_amdgcn_readfirstlane(tid >> 6);
    const char* src = (const char*)(pw < 512 ? wre : wim) + (size_t)(pw & 511) * 16384 + lane * 16;
    u32x4 v[16];
#pragma unroll
    for (int i = 0; i < 16; ++i) v[i] = *(const u32x4*)(src + i * 1024);
    float rs[4];
#pragma unroll
    for (int r = 0; r < 4; ++r) {
      float a0 = 0.f, a1 = 0.f;
#pragma unroll
      for (int q = 0; q < 4; ++q)
#pragma unroll
        for (int e = 0; e < 4; ++e) { a0 += lo16(v[4 * r + q][e]); a1 += hi16(v[4 * r + q][e]); }
      rs[r] = a0 + a1;
    }
#pragma unroll
    for (int o = 1; o < 64; o <<= 1)
#pragma unroll
      for (int r = 0; r < 4; ++r) rs[r] += __shfl_xor(rs[r], o, 64);
    if (lane == 0) *(f32x4*)(ws + WS_RSUM + pw * 16) = (f32x4){rs[0], rs[1], rs[2], rs[3]};
  }
}

__device__ __forceinline__ void dma_res_tile(const char* base  , unsigned lds, int w, int lane) {
#pragma unroll
  for (int j = 0; j < 3; ++j) {
    int L = 160 * w + 64 * j + lane;
    if (L > 639) L = 639;
    const int r = L / 10, ch = L - r * 10;
    if (j < 2) dma16(r * (D_ * 2) + ch * 16, base, lds + (160 * w + 64 * j) * 16);
    else dma16h(r * (D_ * 2) + ch * 16, base, lds + (160 * w + 128) * 16);
  }
}

struct SchedP2 { static constexpr char s[] = "MRMRMRMRMRMRMRMRMRMRMRMRMRMRMMMMMM"; };
struct SchedP3 { static constexpr char s[] = "RRMRMRRMRMRRMRMRRMRMMM"; };
struct SchedP4 { static constexpr char s[] = "MRMRMRMMRMRMRMMRMRMMRMRMRMMRMRMRMMRMRMMMMMMMMM"; };
constexpr int cnt_ch(const char* s, char c) { int n = 0; for (; *s; ++s) n += (*s == c); return n; }
static_assert(cnt_ch(SchedP2::s, 'M') == 20 && cnt_ch(SchedP2::s, 'R') == 14, "");
static_assert(cnt_ch(SchedP3::s, 'M') == 10 && cnt_ch(SchedP3::s, 'R') == 12, "");
static_assert(cnt_ch(SchedP4::s, 'M') == 30 && cnt_ch(SchedP4::s, 'R') == 16, "");
constexpr int L_RES = 147456, L_MOD = 159744, L_DUM = L_MOD + 2048, L_TOTAL = L_DUM + 1024;
constexpr int P2_NS = 5, P2_STAGE = 26624;
constexpr int P3_STAGE = 36864;
constexpr int P4_STAGE = 28672, P4_E = 4 * P4_STAGE, P4_ST = P4_E + 4096, P4_RS = P4_ST + 8192;

__global__ __launch_bounds__(512, 2) void mega(const unsigned short* __restrict__ wre,
                                               const unsigned short* __restrict__ wim,
                                               const unsigned short* __restrict__ x,
                                               char* __restrict__ ws,
                                               unsigned short* __restrict__ out) {
  extern __shared__ __attribute__((aligned(16))) char smem[];
  const int tid = threadIdx.x, lane = tid & 63;
  const int wv = __builtin_amdgcn_readfirstlane(tid >> 6);
  const int w = wv & 3;
  const bool prod = wv < 4;
  const int bid = blockIdx.x;
  const int xcd = bid & 7, bi = bid >> 3;
  const int cb = bi & 7, rsel = bi >> 3;
  const int g = lane >> 4, l15 = lane & 15;
  const unsigned lds0 = lds_off(smem);
  const float* mod = (const float*)(smem + L_MOD);
  const int cb3 = (xcd & 1) * 4 + (bi & 3);
  const int t3 = ((xcd >> 1) * 8 + (bi >> 2)) * 64;
  const int cb4 = (xcd & 1) * 8 + (bi & 7), grp4 = (xcd >> 1) * 4 + (bi >> 3);
  const int grp3 = t3 >> 7;

  {
    const int isim = rsel >> 1;
    const int grp2 = xcd * 2 + (rsel & 1);
    const int t0 = grp2 * 128;
    if (prod) {
      const char* rbase = (const char*)(isim ? wim : wre) + (size_t)t0 * (S_ * 2);
      const char* cbase = ws + WS_HT + (size_t)cb * 32 * 10240;
      unsigned roff[4];
#pragma unroll
      for (int j = 0; j < 4; ++j) {
        const int row = 32 * w + 8 * j + (lane >> 3);
        roff[j] = row * (S_ * 2) + 16 * ((lane & 7) ^ ((row >> 1) & 7));
      }
      auto run = [&](auto ncc) {
        constexpr int NC = decltype(ncc)::value;
        const int c0 = (NC == 3) ? w * 3072 : 6144 + (w - 2) * 2048;
        producer<P2_NS, 32, 4 + NC>([&](int ks, int buf) {
          const unsigned lb = lds0 + buf * P2_STAGE;
#pragma unroll
          for (int j = 0; j < 4; ++j) dma16(roff[j], rbase + ks * 128, lb + (32 * w + 8 * j) * 128);
#pragma unroll
          for (int j = 0; j < NC; ++j) dma16(lane * 16, cbase + ks * 10240 + c0 + j * 1024, lb + 16384 + c0 + j * 1024);
        });
      };
      if (w < 2) run(IC<3>{}); else run(IC<2>{});
      bar();
      const char* wdt = ws + WS_WDT + (size_t)cb3 * 204800;
#pragma unroll
      for (int j = 0; j < 5; ++j) dma16(lane * 16, wdt + w * 5120 + j * 1024, lds0 + 16384 + w * 5120 + j * 1024);
#pragma unroll
      for (int j = 0; j < 5; ++j) dma16(lane * 16, wdt + 20480 + w * 5120 + j * 1024, lds0 + P3_STAGE + 16384 + w * 5120 + j * 1024);
    } else {
      {
        const char* pc;
        unsigned len;
        asm volatile("s_getpc_b64 %0\n.Lmega_pf_here:\n\ts_mov_b32 %1, .Lmega_pf_end-.Lmega_pf_here" : "=s"(pc), "=s"(len));
        const unsigned off = (bi * 4 + w) * 1024;
        if (off + 1024 <= len) dma16(lane * 16, (const char*)((uintptr_t)pc & ~(uintptr_t)15) + off, lds0 + L_DUM);
      }
      if (w < 2) {
        int L = 64 * w + lane; if (L > 99) L = 99;
        const int vec = L / 20, part = vec + (vec >= 3);
        dma16(part * 2560 + (vec < 2 ? cb : cb3) * 320 + (L - vec * 20) * 16, ws + WS_MOD, lds0 + L_MOD + w * 1024);
      }
      dma_res_tile((const char*)x + (size_t)t3 * (D_ * 2) + cb3 * 160, lds0 + L_RES, w, lane);
      const int sw0 = (g ^ (l15 >> 1)) * 16, sw1 = ((4 + g) ^ (l15 >> 1)) * 16;
      const int ra = (32 * w + l15) * 128;
      const int ca_ = 16384 + (g >> 1) * 2560 + l15 * 32 + (g & 1) * 16;
      bf16x8 rf[2][2][2], cf[2][2][5];
      f32x4 acc[5][2];
#pragma unroll
      for (int j = 0; j < 5; ++j) { acc[j][0] = (f32x4){0.f, 0.f, 0.f, 0.f}; acc[j][1] = (f32x4){0.f, 0.f, 0.f, 0.f}; }
      const float sgn = isim ? -1.0f : 1.0f;
      const float* rsum = (const float*)(ws + WS_RSUM) + isim * 2048 + t0 + 32 * w + l15;
      const float rsr0 = rsum[0], rsr1 = rsum[16];
      auto ldf = [&](auto par, auto ic, int buf) {
        constexpr int PP = decltype(par)::value, n = decltype(ic)::value, kk = n / 7, m = n % 7;
        const char* lb = smem + buf * P2_STAGE;
        if constexpr (m == 0) rf[PP][kk][0] = lds_frag(lb + ra + (kk ? sw1 : sw0));
        else if constexpr (m < 6) cf[PP][kk][m - 1] = lds_frag(lb + ca_ + kk * 5120 + (m - 1) * 512);
        else rf[PP][kk][1] = lds_frag(lb + ra + (kk ? sw1 : sw0) + 2048);
      };
      auto body = [&](auto par, auto dl, int rd) {
        constexpr int PP = decltype(par)::value, DL = decltype(dl)::value;
        auto fm = [&](auto mc) {
          constexpr int m = decltype(mc)::value, kk = m / 10, i = (m % 10) / 5, j = m % 5;
          acc[j][i] = MFMA16(cf[PP][kk][j], rf[PP][kk][i], acc[j][i]);
        };
        auto fr = [&](auto rc) { ldf(IC<PP ^ 1>{}, rc, rd); };
        run_sched<SchedP2, 0, 0, 0, DL>(fm, fr);
      };
      auto load0 = [&] { static_for<14>([&](auto ic) { ldf(IC<0>{}, ic, 0); }); wait_vm<0>(); };
      consumer<P2_NS, 32>(load0, body);
      f32x4 sa[5], c1[5];
#pragma unroll
      for (int j = 0; j < 5; ++j) {
        sa[j] = *(const f32x4*)(mod + 16 * j + 4 * g);
        c1[j] = *(const f32x4*)(mod + 80 + 16 * j + 4 * g);
      }
      bar();
      char* pq = ws + WS_PQ + ((size_t)(grp2 * 2 + (w >> 1)) * 80 + isim * 40 + cb * 5) * 2048 + (32 * (w & 1) + l15) * 32 + g * 8;
      const float rs0 = rsr0 * sgn, rs1 = rsr1 * sgn;
#pragma unroll
      for (int j = 0; j < 5; ++j) {
        const f32x4 cs = c1[j] * sgn;
        const f32x4 v0 = cs * acc[j][0] + sa[j] * rs0;
        const f32x4 v1 = cs * acc[j][1] + sa[j] * rs1;
        st8u_wt(pq + j * 2048, (u32x2){pk2(v0[0], v0[1]), pk2(v0[2], v0[3])});
        st8u_wt(pq + j * 2048 + 512, (u32x2){pk2(v1[0], v1[1]), pk2(v1[2], v1[3])});
      }
    }
    if (!prod) wait_vm<0>();
    bar();
    if (wv == 4) {
      if (lane == 0) add_count(ws, grp2);
      wait_count(ws, grp3, 16);
    }
    bar();
  }

  {
    const char* wdt = ws + WS_WDT + (size_t)cb3 * 204800;
    if (prod) {
      const char* rbase = ws + WS_PQ + (size_t)(t3 >> 6) * (80 * 2048) + w * 4096;
      producer_seam<10, 4, 5>([&](int ks, int buf) {
#pragma unroll
        for (int j = 0; j < 4; ++j) dma16s(lane * 16, rbase + ks * 16384 + j * 1024, lds0 + buf * P3_STAGE + w * 4096 + j * 1024);
      }, [&](int ks, int buf) {
#pragma unroll
        for (int j = 0; j < 5; ++j)
          dma16(lane * 16, wdt + ks * 20480 + w * 5120 + j * 1024, lds0 + buf * P3_STAGE + 16384 + w * 5120 + j * 1024);
      }, [] {});
      bar();
      const char* w1t = ws + WS_W1T + (size_t)cb4 * 122880;
      dma16(w * 1024 + lane * 16, ws + WS_E, lds0 + P4_E + w * 1024);
#pragma unroll
      for (int s = 0; s < 2; ++s)
#pragma unroll
        for (int j = 0; j < 3; ++j)
          dma16(lane * 16, w1t + s * 12288 + w * 3072 + j * 1024, lds0 + s * P4_STAGE + 16384 + w * 3072 + j * 1024);
    } else {
      const int sw0 = (g ^ (l15 >> 1)) * 16, sw1 = ((4 + g) ^ (l15 >> 1)) * 16;
      const int ra = (g >> 1) * 2048 + (16 * w + l15) * 32 + (g & 1) * 16, ca_ = 16384 + l15 * 128;
      bf16x8 rf[2][2], cf[2][2][5];
      f32x4 acc[5];
#pragma unroll
      for (int j = 0; j < 5; ++j) acc[j] = (f32x4){0.f, 0.f, 0.f, 0.f};
      auto ldf = [&](auto hc, auto ic, int buf) {
        constexpr int H = decltype(hc)::value, n = decltype(ic)::value, kk = n / 6, m = n % 6;
        const char* lb = smem + buf * P3_STAGE;
        if constexpr (m == 0) rf[H][kk] = lds_frag(lb + ra + H * 8192 + kk * 4096);
        else cf[H][kk][m - 1] = lds_frag(lb + (kk ? sw1 : sw0) + ca_ + H * 10240 + (m - 1) * 2048);
      };
      auto half = [&](auto par, auto dl, int buf) {
        constexpr int PP = decltype(par)::value, DL = decltype(dl)::value;
        auto fm = [&](auto mc) {
          constexpr int m = decltype(mc)::value, kk = m / 5, j = m % 5;
          acc[j] = MFMA16(cf[PP][kk][j], rf[PP][kk], acc[j]);
        };
        auto fr = [&](auto rc) { ldf(IC<PP ^ 1>{}, rc, buf); };
        run_sched<SchedP3, 0, 0, 0, DL>(fm, fr);
      };
      char* res = smem + L_RES + (16 * w + l15) * 160 + g * 8;
      u32x2 xr[5];
      f32x4 ga[5], c1[5];
#pragma unroll
      for (int j = 0; j < 5; ++j) {
        xr[j] = *(const u32x2*)(res + j * 32);
        ga[j] = *(const f32x4*)(mod + 160 + 16 * j + 4 * g);
        c1[j] = *(const f32x4*)(mod + 240 + 16 * j + 4 * g);
      }
      auto load0 = [&] { static_for<12>([&](auto ic) { ldf(IC<0>{}, ic, 0); }); };
      consumer_half<10>(load0, half);
      bar();
      char* xc = ws + WS_XC + ((size_t)(t3 >> 6) * 40 + cb3 * 5) * 2048 + (16 * w + l15) * 32 + g * 8;
      float xf[20];
      float s = 0.f;
#pragma unroll
      for (int j = 0; j < 5; ++j) {
        const f32x4 y = acc[j];
        const u32x2 o = {pk2(lo16(xr[j][0]) + ga[j][0] * y[0], hi16(xr[j][0]) + ga[j][1] * y[1]),
                         pk2(lo16(xr[j][1]) + ga[j][2] * y[2], hi16(xr[j][1]) + ga[j][3] * y[3])};
        xr[j] = o;
        xf[4 * j] = lo16(o[0]); xf[4 * j + 1] = hi16(o[0]); xf[4 * j + 2] = lo16(o[1]); xf[4 * j + 3] = hi16(o[1]);
        st8u_wt(xc + j * 2048, (u32x2){pk2(xf[4 * j] * c1[j][0], xf[4 * j + 1] * c1[j][1]), pk2(xf[4 * j + 2] * c1[j][2], xf[4 * j + 3] * c1[j][3])});
        s += (xf[4 * j] + xf[4 * j + 1]) + (xf[4 * j + 2] + xf[4 * j + 3]);
      }
      s += __shfl_xor(s, 16, 64);
      s += __shfl_xor(s, 32, 64);
      const float ml = s * (1.0f / 80.0f);
      float m2 = 0.f;
#pragma unroll
      for (int e = 0; e < 20; ++e) { float d = xf[e] - ml; m2 += d * d; }
      m2 += __shfl_xor(m2, 16, 64);
      m2 += __shfl_xor(m2, 32, 64);
      if (lane < 16) st8_wt(ws + WS_ST + ((size_t)(t3 >> 6) * 8 + cb3) * 512 + (16 * w + l15) * 8, (f32x2){s, m2});
#pragma unroll
      for (int j = 0; j < 5; ++j) *(u32x2*)(res + j * 32) = xr[j];
      wait_vm<0>();
    }
    bar();
    if (wv == 4) {
      if (lane == 0) add_count(ws, 16 + grp3);
      wait_count(ws, 16 + grp4, 16);
    }
    bar();
  }

  {
    const int t0 = grp4 * 128;
    const char* w1t = ws + WS_W1T + (size_t)cb4 * 122880;
    if (prod) {
      const char* rbase = ws + WS_XC + (size_t)(grp4 * 2 + (w >> 1)) * (40 * 2048) + (w & 1) * 4096;
      dma16s(w * 2048 + lane * 16, ws + WS_ST + (size_t)t0 * 64, lds0 + P4_ST + w * 2048);
      dma16s(w * 2048 + 1024 + lane * 16, ws + WS_ST + (size_t)t0 * 64, lds0 + P4_ST + w * 2048 + 1024);
      producer_seam<10, 4, 3>([&](int ks, int buf) {
#pragma unroll
        for (int j = 0; j < 4; ++j) dma16s(lane * 16, rbase + ks * 8192 + j * 1024, lds0 + buf * P4_STAGE + w * 4096 + j * 1024);
      }, [&](int ks, int buf) {
#pragma unroll
        for (int j = 0; j < 3; ++j)
          dma16(lane * 16, w1t + ks * 12288 + w * 3072 + j * 1024, lds0 + buf * P4_STAGE + 16384 + w * 3072 + j * 1024);
      }, [&] {
        if (tid < 128) {
          const char* sp = smem + P4_ST + (tid >> 6) * 4096 + (tid & 63) * 8;
          f32x2 pp[8];
#pragma unroll
          for (int e = 0; e < 8; ++e) pp[e] = *(const f32x2*)(sp + e * 512);
          const float mean = (((pp[0][0] + pp[1][0]) + (pp[2][0] + pp[3][0])) + ((pp[4][0] + pp[5][0]) + (pp[6][0] + pp[7][0]))) * (1.0f / D_);
          float m2 = ((pp[0][1] + pp[1][1]) + (pp[2][1] + pp[3][1])) + ((pp[4][1] + pp[5][1]) + (pp[6][1] + pp[7][1]));
          float dd = 0.f;
#pragma unroll
          for (int e = 0; e < 8; ++e) { const float d = pp[e][0] * (1.0f / 80.0f) - mean; dd += d * d; }
          m2 += 80.0f * dd;
          const float rden = 1.0f / bfr(sqrtf(bfr(bfr(m2 * (1.0f / D_)) + 1e-5f)));
          *(f32x2*)(smem + P4_RS + tid * 8) = (f32x2){bfr(mean), rden};
        }
        wait_lgkm0();
      });
      bar();
      const char* w2t = ws + WS_W2T + (size_t)cb3 * F_ * 160;
#pragma unroll
      for (int s = 0; s < 2; ++s)
#pragma unroll
        for (int j = 0; j < 5; ++j)
          dma16(lane * 16, w2t + s * 20480 + w * 5120 + j * 1024, lds0 + s * P3_STAGE + 16384 + w * 5120 + j * 1024);
    } else {
      const int wm = w & 1, wn = w >> 1;
      const int ca_ = 16384 + (g >> 1) * 3072 + (48 * wn + l15) * 32 + (g & 1) * 16;
      const int ra = wm * 8192 + (g >> 1) * 2048 + l15 * 32 + (g & 1) * 16;
      int ea = P4_E + (l15 < 2 ? l15 : 2) * 1280 + g * 16;
      bf16x8 rf[2][2][5], cf[2][2][3];
      f32x4 acc[3][5];
#pragma unroll
      for (int j = 0; j < 3; ++j)
#pragma unroll
        for (int i = 0; i < 5; ++i) acc[j][i] = (f32x4){0.f, 0.f, 0.f, 0.f};
      auto ldf = [&](auto par, auto ic, int buf) {
        constexpr int PP = decltype(par)::value, n = decltype(ic)::value, kk = n / 8, m = n % 8;
        const char* lb = smem + buf * P4_STAGE;
        if constexpr (m == 0) rf[PP][kk][0] = lds_frag(lb + ra + kk * 4096);
        else if constexpr (m < 4) cf[PP][kk][m - 1] = lds_frag(lb + ca_ + kk * 6144 + (m - 1) * 512);
        else if constexpr (m < 7) rf[PP][kk][m - 3] = lds_frag(lb + ra + kk * 4096 + (m - 3) * 512);
        else {
          rf[PP][kk][4] = lds_frag(smem + ea + kk * 64);
          if constexpr (kk == 1) ea += 128;
        }
      };
      auto body = [&](auto par, auto dl, int rd) {
        constexpr int PP = decltype(par)::value, DL = decltype(dl)::value;
        auto fm = [&](auto mc) {
          constexpr int m = decltype(mc)::value, kk = m / 15, i = (m % 15) / 3, j = m % 3;
          acc[j][i] = MFMA16(cf[PP][kk][j], rf[PP][kk][i], acc[j][i]);
        };
        auto fr = [&](auto rc) { ldf(IC<PP ^ 1>{}, rc, rd); };
        run_sched<SchedP4, 0, 0, 0, DL>(fm, fr);
      };
      auto load0 = [&] { static_for<16>([&](auto ic) { ldf(IC<0>{}, ic, 0); }); };
      consumer<4, 10>(load0, body);
      f32x2 rsv[4];
#pragma unroll
      for (int i = 0; i < 4; ++i) rsv[i] = *(const f32x2*)(smem + P4_RS + (64 * wm + 16 * i + l15) * 8);
      wait_lgkm0();
      bar();
      f32x4 u[3], v[3];
#pragma unroll
      for (int j = 0; j < 3; ++j)
#pragma unroll
        for (int e = 0; e < 4; ++e) {
          const int a = __float_as_int(acc[j][4][e]);
          u[j][e] = __int_as_float(__builtin_amdgcn_update_dpp(0, a, 0x150, 0xf, 0xf, false));
          v[j][e] = __int_as_float(__builtin_amdgcn_update_dpp(0, a, 0x151, 0xf, 0xf, false));
        }
      char* h1 = ws + WS_H1 + ((size_t)(grp4 * 2 + wm) * 96 + cb4 * 6 + 3 * wn) * 2048 + l15 * 32 + g * 8;
#pragma unroll
      for (int i = 0; i < 4; ++i) {
        const f32x2 rs = rsv[i];
#pragma unroll
        for (int j = 0; j < 3; ++j) {
          const f32x4 hv = (acc[j][i] - u[j] * rs[0]) * rs[1] + v[j];
          st8u_wt(h1 + j * 2048 + i * 512,
                  (u32x2){pk2(fmaxf(hv[0], 0.f), fmaxf(hv[1], 0.f)), pk2(fmaxf(hv[2], 0.f), fmaxf(hv[3], 0.f))});
        }
      }
      wait_vm<0>();
    }
    bar();
    if (wv == 4) {
      if (lane == 0) add_count(ws, 32 + grp4);
      wait_count(ws, 32 + grp3, 16);
    }
    bar();
  }

  {
    const char* w2t = ws + WS_W2T + (size_t)cb3 * F_ * 160;
    if (prod) {
      const char* rbase = ws + WS_H1 + (size_t)(t3 >> 6) * (96 * 2048) + w * 4096;
      producer_seam<12, 4, 5>([&](int ks, int buf) {
#pragma unroll
        for (int j = 0; j < 4; ++j) dma16s(lane * 16, rbase + ks * 16384 + j * 1024, lds0 + buf * P3_STAGE + w * 4096 + j * 1024);
      }, [&](int ks, int buf) {
#pragma unroll
        for (int j = 0; j < 5; ++j)
          dma16(lane * 16, w2t + ks * 20480 + w * 5120 + j * 1024, lds0 + buf * P3_STAGE + 16384 + w * 5120 + j * 1024);
      }, [] {});
    } else {
      const int ra = (g >> 1) * 2048 + (16 * w + l15) * 32 + (g & 1) * 16;
      const int ca_ = 16384 + (g >> 1) * 2560 + l15 * 32 + (g & 1) * 16;
      bf16x8 rf[2][2], cf[2][2][5];
      f32x4 acc[5];
#pragma unroll
      for (int j = 0; j < 5; ++j) acc[j] = (f32x4){0.f, 0.f, 0.f, 0.f};
      auto ldf = [&](auto hc, auto ic, int buf) {
        constexpr int H = decltype(hc)::value, n = decltype(ic)::value, kk = n / 6, m = n % 6;
        const char* lb = smem + buf * P3_STAGE;
        if constexpr (m == 0) rf[H][kk] = lds_frag(lb + ra + H * 8192 + kk * 4096);
        else cf[H][kk][m - 1] = lds_frag(lb + ca_ + H * 10240 + kk * 5120 + (m - 1) * 512);
      };
      auto half = [&](auto par, auto dl, int buf) {
        constexpr int PP = decltype(par)::value, DL = decltype(dl)::value;
        auto fm = [&](auto mc) {
          constexpr int m = decltype(mc)::value, kk = m / 5, j = m % 5;
          acc[j] = MFMA16(cf[PP][kk][j], rf[PP][kk], acc[j]);
        };
        auto fr = [&](auto rc) { ldf(IC<PP ^ 1>{}, rc, buf); };
        run_sched<SchedP3, 0, 0, 0, DL>(fm, fr);
      };
      char* res = smem + L_RES + (16 * w + l15) * 160 + g * 8;
      u32x2 xr[5];
      f32x4 gb[5];
#pragma unroll
      for (int j = 0; j < 5; ++j) {
        xr[j] = *(const u32x2*)(res + j * 32);
        gb[j] = *(const f32x4*)(mod + 320 + 16 * j + 4 * g);
      }
      auto load0 = [&] { static_for<12>([&](auto ic) { ldf(IC<0>{}, ic, 0); }); };
      consumer_half<12>(load0, half);
#pragma unroll
      for (int j = 0; j < 5; ++j) {
        const f32x4 y = acc[j];
        *(u32x2*)(res + j * 32) = (u32x2){pk2(lo16(xr[j][0]) + gb[j][0] * y[0], hi16(xr[j][0]) + gb[j][1] * y[1]),
                                          pk2(lo16(xr[j][1]) + gb[j][2] * y[2], hi16(xr[j][1]) + gb[j][3] * y[3])};
      }
      wait_vm<0>();
    }
    wait_lgkm0();
    bar();
    for (int id = tid; id < 640; id += 512) {
      const int row = id / 10, c = id - row * 10;
      *(u32x4*)((char*)out + (size_t)(t3 + row) * (D_ * 2) + cb3 * 160 + c * 16) = *(const u32x4*)(smem + L_RES + row * 160 + c * 16);
    }
  }
  asm volatile(".Lmega_pf_end:");
}

extern "C" void kernel_launch(void* const* d_in, const int* in_sizes, int n_in,
                              void* d_out, int out_size, void* d_ws, size_t ws_size,
                              hipStream_t stream) {
  const unsigned short* x    = (const unsigned short*)d_in[0];
  const unsigned short* wsre = (const unsigned short*)d_in[1];
  const unsigned short* wsim = (const unsigned short*)d_in[2];
  const unsigned short* wdre = (const unsigned short*)d_in[3];
  const unsigned short* wdim = (const unsigned short*)d_in[4];
  const unsigned short* w1   = (const unsigned short*)d_in[6];
  const unsigned short* w2   = (const unsigned short*)d_in[7];
  const unsigned short* c    = (const unsigned short*)d_in[8];
  const unsigned short* wmod = (const unsigned short*)d_in[9];
  char* ws = (char*)d_ws;
  unsigned short* out = (unsigned short*)d_out;
  (void)hipFuncSetAttribute((const void*)mega, hipFuncAttributeMaxDynamicSharedMemorySize, L_TOTAL);
  k1_ln_mod<<<640 + 256 + 24, 256, 0, stream>>>(x, c, wmod, wsre, wsim, w2, wdre, wdim, w1, out, ws);
  mega<<<256, 512, L_TOTAL, stream>>>(wsre, wsim, x, ws, out);
}
```

```cpp
#include <hip/hip_runtime.h>
#include <hip/hip_bf16.h>
#include <stdint.h>

typedef __attribute__((ext_vector_type(8))) __bf16 bf16x8;
typedef __attribute__((ext_vector_type(2))) __bf16 bf16x2;
typedef __attribute__((ext_vector_type(4))) float f32x4;
typedef __attribute__((ext_vector_type(2))) float f32x2;
typedef __attribute__((ext_vector_type(4))) short s16x4;
typedef __attribute__((ext_vector_type(8))) short s16x8;
typedef __attribute__((ext_vector_type(4))) unsigned u32x4;
typedef __attribute__((ext_vector_type(2))) unsigned u32x2;
typedef __attribute__((address_space(3))) s16x4* lds_s16x4_ptr;

constexpr int S_ = 2048, D_ = 640, F_ = 1536, MODN = 3840;

constexpr size_t WS_MOD = 0;
constexpr size_t WS_E   = 16384;
constexpr size_t WS_CNT = 20480;
constexpr size_t WS_ST  = 32768;
constexpr size_t WS_RSUM = WS_ST + (size_t)S_ * 64;
constexpr size_t WS_HT  = WS_RSUM + 16384;
constexpr size_t WS_PQ  = WS_HT + (size_t)D_ * S_ * 2;
constexpr size_t WS_XC  = WS_PQ + (size_t)S_ * 1280 * 2;
constexpr size_t WS_H1  = WS_XC + (size_t)S_ * D_ * 2;
constexpr size_t WS_W2T = WS_H1 + (size_t)S_ * F_ * 2;
constexpr size_t WS_W1T = WS_W2T + (size_t)F_ * D_ * 2;
constexpr size_t WS_WDT = WS_W1T + (size_t)F_ * D_ * 2;

template <int V> struct IC { static constexpr int value = V; };
template <int N, int I = 0, class F> __device__ __forceinline__ void static_for(F f) {
  if constexpr (I < N) { f(IC<I>{}); static_for<N, I + 1>(f); }
}

__device__ __forceinline__ float bf2f(unsigned short u) { return __uint_as_float(((unsigned)u) << 16); }
__device__ __forceinline__ float bfr(float f) { return (float)(__bf16)f; }
__device__ __forceinline__ unsigned pk2(float a, float b) {
  bf16x2 v = __builtin_convertvector((f32x2){a, b}, bf16x2);
  return __builtin_bit_cast(unsigned, v);
}
__device__ __forceinline__ float lo16(unsigned u) { return __uint_as_float(u << 16); }
__device__ __forceinline__ float hi16(unsigned u) { return __uint_as_float(u & 0xffff0000u); }

__device__ __forceinline__ void dma16(unsigned voff, const char* sbase, unsigned lds) {
  asm volatile("s_mov_b32 m0, %2\n\ts_nop 0\n\tglobal_load_lds_dwordx4 %0, %1"
               ::"v"(voff), "s"(sbase), "s"(lds) : "memory");
}
__device__ __forceinline__ void dma16s(unsigned voff, const char* sbase, unsigned lds) {
  asm volatile("s_mov_b32 m0, %2\n\ts_nop 0\n\tglobal_load_lds_dwordx4 %0, %1 sc1"
               ::"v"(voff), "s"(sbase), "s"(lds) : "memory");
}
#define LD1(p) __builtin_nontemporal_load(p)
__device__ __forceinline__ void dma16h(unsigned voff, const char* sbase, unsigned lds) {
  asm volatile("s_mov_b32 m0, %2\n\ts_mov_b32 exec_hi, 0\n\tglobal_load_lds_dwordx4 %0, %1\n\ts_mov_b32 exec_hi, -1"
               ::"v"(voff), "s"(sbase), "s"(lds) : "memory");
}
__device__ __forceinline__ unsigned lds_off(const void* p) {
  return (unsigned)(uintptr_t)(__attribute__((address_space(3))) const char*)p;
}
#define SB0() __builtin_amdgcn_sched_barrier(0)
__device__ __forceinline__ void bar() {
  asm volatile("" ::: "memory");
  __builtin_amdgcn_s_barrier();
  asm volatile("" ::: "memory");
}
__device__ __forceinline__ bf16x8 lds_frag(const char* p) { return *(const bf16x8*)p; }
__device__ __forceinline__ s16x4 lds_tr(const char* p) {
  return __builtin_amdgcn_ds_read_tr16_b64_v4i16((lds_s16x4_ptr)p);
}
__device__ __forceinline__ bf16x8 cat_frag(s16x4 a, s16x4 b) {
  s16x8 v = __builtin_shufflevector(a, b, 0, 1, 2, 3, 4, 5, 6, 7);
  return __builtin_bit_cast(bf16x8, v);
}
#define MFMA16(a, b, c) __builtin_amdgcn_mfma_f32_16x16x32_bf16((a), (b), (c), 0, 0, 0)

template <class Sc, int POS, int NM, int NR, int DL, class FM, class FR>
__device__ __forceinline__ void run_sched(FM& fm, FR& fr) {
  if constexpr (Sc::s[POS] != 0) {
    constexpr char c = Sc::s[POS];
    if constexpr (c == 'M') {
      fm(IC<NM>{}); SB0();
      run_sched<Sc, POS + 1, NM + 1, NR, DL>(fm, fr);
    } else {
      if constexpr (DL) { fr(IC<NR>{}); SB0(); }
      run_sched<Sc, POS + 1, NM, NR + 1, DL>(fm, fr);
    }
  }
}
template <int N> __device__ __forceinline__ void wait_vm() { asm volatile("s_waitcnt vmcnt(%0)" ::"n"(N) : "memory"); }
__device__ __forceinline__ void wait_lgkm0() { asm volatile("s_waitcnt lgkmcnt(0)" ::: "memory"); }

template <int NS, int P, int TI>
__device__ __forceinline__ void prod_tail() {
  if constexpr (TI < NS - 1) {
    wait_vm<P*(NS - 2 - TI)>();
    bar();
    prod_tail<NS, P, TI + 1>();
  }
}
constexpr int ramp_A(int j, int NS) {
  int a = 2;
  for (int i = 1; i <= j; ++i) { int lim = (i == 1) ? NS : (i - 1) + NS; a = (a + 2 < lim) ? a + 2 : lim; }
  return a;
}
constexpr int ramp_len(int NS) { int j = 1; while (ramp_A(j, NS) != ((j == 1) ? NS : (j - 1) + NS) || j < 2) ++j; return j; }
template <int NS, int P, int J, int R, class Issue>
__device__ __forceinline__ void prod_ramp(Issue& issue) {
  if constexpr (J < R) {
    constexpr int a0 = (J == 0) ? 0 : ramp_A(J - 1, NS), a1 = ramp_A(J, NS);
#pragma unroll
    for (int s = a0; s < a1; ++s) issue(s, s % NS);
    wait_vm<P*(a1 - (J + 1))>();
    bar();
    prod_ramp<NS, P, J + 1, R>(issue);
  }
}
template <int NS, int NK, int P, class Issue>
__device__ __forceinline__ void producer(Issue issue) {
  constexpr int R = ramp_len(NS);
  static_assert(R - 1 + NS <= NK, "ramp too long");
  prod_ramp<NS, P, 0, R>(issue);
#pragma unroll
  for (int s = ramp_A(R - 1, NS); s < R - 1 + NS; ++s) issue(s, s % NS);
  int wr = (R - 1) % NS;
#pragma nounroll
  for (int i = R - 1; i < NK - NS; ++i) {
    wait_vm<P*(NS - 2)>();
    bar();
    issue(i + NS, wr);
    wr = (wr + 1 == NS) ? 0 : wr + 1;
  }
  prod_tail<NS, P, 0>();
}
template <int NK, int PR, int PC, class IR, class ICC, class Mid>
__device__ __forceinline__ void producer_seam(IR issueR, ICC issueC, Mid mid) {
  constexpr int P = PR + PC;
  issueR(0, 0); issueR(1, 1);
  wait_vm<PR>(); bar();
  issueR(2, 2); issueC(2, 2); issueR(3, 3); issueC(3, 3);
  wait_vm<2 * P>(); bar();
  mid();
  constexpr int J0 = 2;
#pragma nounroll
  for (int j = J0; j <= NK - 3; ++j) {
    issueR(j + 2, (j + 2) & 3); issueC(j + 2, (j + 2) & 3);
    wait_vm<2 * P>(); bar();
  }
  wait_vm<P>(); bar();
  wait_vm<0>(); bar();
}
template <int NS, int NK, class Load0, class Body>
__device__ __forceinline__ void consumer(Load0 load0, Body body) {
  static_assert((NK & 1) == 0, "even number of stages");
  bar();
  load0();
  int rd = 1;
#pragma nounroll
  for (int i = 0; i < NK - 2; i += 2) {
    wait_lgkm0();
    bar();
    SB0();
    body(IC<0>{}, IC<1>{}, rd);
    SB0();
    rd = (rd + 1 == NS) ? 0 : rd + 1;
    wait_lgkm0();
    bar();
    SB0();
    body(IC<1>{}, IC<1>{}, rd);
    SB0();
    rd = (rd + 1 == NS) ? 0 : rd + 1;
  }
  wait_lgkm0();
  bar();
  SB0();
  body(IC<0>{}, IC<1>{}, rd);
  SB0();
  body(IC<1>{}, IC<0>{}, 0);
  SB0();
}
template <int NK, class Load0, class Half>
__device__ __forceinline__ void consumer_half(Load0 load0, Half half) {
  bar();
  load0();
#pragma nounroll
  for (int s = 0; s < NK - 1; ++s) {
    SB0();
    half(IC<0>{}, IC<1>{}, s & 3);
    SB0();
    wait_lgkm0();
    bar();
    SB0();
    half(IC<1>{}, IC<1>{}, (s + 1) & 3);
    SB0();
  }
  half(IC<0>{}, IC<1>{}, (NK - 1) & 3);
  SB0();
  half(IC<1>{}, IC<0>{}, 0);
  SB0();
}
typedef __attribute__((address_space(1))) unsigned gu32;
__device__ __forceinline__ void st8u_wt(void* p, u32x2 v) {
  asm volatile("global_store_dwordx2 %0, %1, off sc1\n\ts_nop 1" ::"v"(p), "v"(v) : "memory");
}
__device__ __forceinline__ void st8_wt(void* p, f32x2 v) {
  asm volatile("global_store_dwordx2 %0, %1, off sc1\n\ts_nop 1" ::"v"(p), "v"(v) : "memory");
}
__device__ __forceinline__ void wait_count(char* ws, int idx, unsigned need) {
  gu32* cnt = (gu32*)(ws + WS_CNT + idx * 256);
#pragma nounroll
  for (unsigned spins = 0; spins < (1u << 22); ++spins) {
    if ((unsigned)__builtin_amdgcn_readfirstlane(__hip_atomic_load(cnt, __ATOMIC_RELAXED, __HIP_MEMORY_SCOPE_AGENT)) >= need) break;
    __builtin_amdgcn_s_sleep(1);
  }
  asm volatile("" ::: "memory");
}
__device__ __forceinline__ void add_count(char* ws, int idx) {
  __hip_atomic_fetch_add((gu32*)(ws + WS_CNT + idx * 256), 1u, __ATOMIC_RELAXED, __HIP_MEMORY_SCOPE_AGENT);
}
__device__ __forceinline__ int swap23(int r) { return (r & ~12) | ((r & 4) << 1) | ((r & 8) >> 1); }

__global__ __launch_bounds__(256) void k1_ln_mod(const unsigned short* __restrict__ x,
                                                 const unsigned short* __restrict__ c,
                                                 const unsigned short* __restrict__ wmod,
                                                 const unsigned short* __restrict__ wre,
                                                 const unsigned short* __restrict__ wim,
                                                 const unsigned short* __restrict__ w2,
                                                 const unsigned short* __restrict__ dre,
                                                 const unsigned short* __restrict__ dim,
                                                 const unsigned short* __restrict__ w1,
                                                 const unsigned short* __restrict__ outp,
                                                 char* __restrict__ ws) {
  __shared__ __attribute__((aligned(16))) char smem[16 * 1296 + 256];
  const int tid = threadIdx.x, lane = tid & 63;
  const int bid = blockIdx.x;
  if (bid < 128) {
    const int xcd = bid & 7, i = bid >> 3;
    const int rg = xcd * 4 + (i >> 2), sub = i & 3;
    const int row0 = rg * 64 + sub * 16;
    const int r = tid >> 4, j = tid & 15;
    const u32x4* src = (const u32x4*)(x + (size_t)(row0 + r) * D_);
    u32x4 v[5];
#pragma unroll
    for (int q = 0; q < 5; ++q) v[q] = src[j + 16 * q];
    float f[40];
#pragma unroll
    for (int q = 0; q < 5; ++q)
#pragma unroll
      for (int e = 0; e < 4; ++e) { f[q * 8 + 2 * e] = lo16(v[q][e]); f[q * 8 + 2 * e + 1] = hi16(v[q][e]); }
    float s = 0.f;
#pragma unroll
    for (int e = 0; e < 40; ++e) s += f[e];
#pragma unroll
    for (int o = 1; o < 16; o <<= 1) s += __shfl_xor(s, o, 64);
    const float mean = s * (1.0f / D_);
    float s2 = 0.f;
#pragma unroll
    for (int e = 0; e < 40; ++e) { float d = f[e] - mean; s2 += d * d; }
#pragma unroll
    for (int o = 1; o < 16; o <<= 1) s2 += __shfl_xor(s2, o, 64);
    const float mb = bfr(mean);
    const float vb = bfr(s2 * (1.0f / D_));
    const float rden = 1.0f / bfr(sqrtf(bfr(vb + 1e-5f)));
#pragma unroll
    for (int q = 0; q < 5; ++q) {
      u32x4 o;
#pragma unroll
      for (int e = 0; e < 4; ++e) {
        float a = bfr(f[q * 8 + 2 * e] - mb) * rden;
        float b = bfr(f[q * 8 + 2 * e + 1] - mb) * rden;
        o[e] = pk2(a, b);
      }
      *(u32x4*)(smem + r * 1296 + (j + 16 * q) * 16) = o;
    }
    wait_lgkm0();
    bar();
    const int grp = tid >> 4, li = tid & 15, qq = li >> 2, pp = li & 3;
    char* ht = ws + WS_HT + (size_t)(row0 >> 4) * 2560;
#pragma unroll
    for (int it = 0; it < 5; ++it) {
      const int pr = it * 16 + grp;
      const int cblk = pr >> 1, half = pr & 1;
      const char* a0 = smem + (8 * half + qq) * 1296 + (16 * cblk + 4 * pp) * 2;
      s16x4 t0 = lds_tr(a0);
      s16x4 t1 = lds_tr(a0 + 4 * 1296);
      s16x8 o = __builtin_shufflevector(t0, t1, 0, 1, 2, 3, 4, 5, 6, 7);
      const int d = 16 * cblk + li;
      const int cb = d / 80, rr = d - cb * 80;
      *(u32x4*)(ht + (size_t)cb * (128 * 2560) + rr * 32 + half * 16) = __builtin_bit_cast(u32x4, o);
    }
    if (bid == 0 && tid < 96) *(u32x4*)(ws + WS_E + 2560 + tid * 16) = (u32x4){0u, 0u, 0u, 0u};
    if (bid == 1 && tid < 48) *(unsigned*)(ws + WS_CNT + tid * 256) = 0u;
  } else if (bid < 368) {
    const int g = bid - 128;
    const int cgp = (g & 7) * 30 + (g >> 3);
    const int l2 = tid & 1, rg = tid >> 1;
    u32x4 v[5];
    unsigned short cv16[5];
#pragma unroll
    for (int q = 0; q < 5; ++q) {
      const int k = q * 128 + rg;
      v[q] = *(const u32x4*)(wmod + (size_t)k * MODN + cgp * 16 + l2 * 8);
      cv16[q] = c[k];
    }
    float acc[8];
#pragma unroll
    for (int e = 0; e < 8; ++e) acc[e] = 0.f;
#pragma unroll
    for (int q = 0; q < 5; ++q) {
      const float cv = bf2f(cv16[q]);
      const float sv = bfr(cv / (1.0f + __expf(-cv)));
#pragma unroll
      for (int e = 0; e < 4; ++e) {
        acc[2 * e] += sv * lo16(v[q][e]);
        acc[2 * e + 1] += sv * hi16(v[q][e]);
      }
    }
#pragma unroll
    for (int o = 2; o < 64; o <<= 1)
#pragma unroll
      for (int e = 0; e < 8; ++e) acc[e] += __shfl_xor(acc[e], o, 64);
    float* red = (float*)smem;
    if (lane < 2) {
#pragma unroll
      for (int e = 0; e < 8; ++e) red[(tid >> 6) * 16 + lane * 8 + e] = acc[e];
    }
    wait_lgkm0();
    bar();
    if (tid < 16) {
      float m = bfr((red[tid] + red[16 + tid]) + (red[32 + tid] + red[48 + tid]));
      const int n = cgp * 16 + tid;
      const int part = n / D_, d = n - part * D_;
      if (part == 1 || part == 4) m = bfr(1.0f + m);
      ((float*)(ws + WS_MOD))[n] = m;
      if (part == 4) ((unsigned short*)(ws + WS_E))[d] = (unsigned short)(__float_as_uint(m) >> 16);
      if (part == 3) ((unsigned short*)(ws + WS_E))[D_ + d] = (unsigned short)(__float_as_uint(m) >> 16);
    }
  } else if (bid < 464) {
    const int k0 = (bid - 368) * 16;
    u32x4 v[5];
#pragma unroll
    for (int q = 0; q < 5; ++q) v[q] = LD1((const u32x4*)((const char*)w2 + (size_t)k0 * (D_ * 2) + (tid + 256 * q) * 16));
#pragma unroll
    for (int q = 0; q < 5; ++q) {
      const int id = tid + 256 * q;
      const int r = id / 80, ch = id - r * 80;
      const int cb = ch / 10, cc = ch - cb * 10;
      *(u32x4*)(ws + WS_W2T + ((size_t)cb * F_ + k0 + swap23(r)) * 160 + cc * 16) = v[q];
    }
  } else if (bid < 560) {
    const int c0 = (bid - 464) * 1280;
    u32x4 v[5];
#pragma unroll
    for (int q = 0; q < 5; ++q) v[q] = LD1((const u32x4*)((const char*)w1 + (size_t)(c0 + tid + 256 * q) * 16));
#pragma unroll
    for (int q = 0; q < 5; ++q) {
      const int id = c0 + tid + 256 * q;
      const int k = id / 192, ch = id - k * 192;
      const int cb = ch / 12, cc = ch - cb * 12;
      const int r = swap23(k);
      *(u32x4*)(ws + WS_W1T + ((size_t)cb * D_ + r) * 192 + (cc ^ (((r >> 2) & 1) << 1)) * 16) = v[q];
    }
  } else if (bid < 640) {
    const int m = bid >= 600, c0 = (bid - 560 - 40 * m) * 1280;
    u32x4 v[5];
#pragma unroll
    for (int q = 0; q < 5; ++q) v[q] = LD1((const u32x4*)((const char*)(m ? dim : dre) + (size_t)(c0 + tid + 256 * q) * 16));
#pragma unroll
    for (int q = 0; q < 5; ++q) {
      const int id = c0 + tid + 256 * q;
      const int e = id / 80, ch = id - e * 80;
      const int cb = e / 80, row = e - cb * 80;
      *(u32x4*)(ws + WS_WDT + ((size_t)(cb * 20 + m * 10 + (ch >> 3)) * 80 + row) * 128 + ((ch & 7) ^ ((row >> 1) & 7)) * 16) = v[q];
    }
  } else if (bid >= 640 + 256) {
    const int t = ((bid - 640 - 256) >> 3) * 256 + tid;
    const char* a;
    if (t < 384) a = ws + (size_t)t * 65536;
    else if (t < 512) a = (const char*)wre + (size_t)(t - 384) * 65536;
    else if (t < 640) a = (const char*)wim + (size_t)(t - 512) * 65536;
    else if (t < 680) a = (const char*)x + (size_t)(t - 640) * 65536;
    else if (t < 720) a = (const char*)outp + (size_t)(t - 680) * 65536;
    else a = ws;
    unsigned v;
    asm volatile("global_load_dword %0, %1, off" : "=v"(v) : "v"(a) : "memory");
    wait_vm<0>();
    asm volatile("" ::"v"(v));
  } else {
    const int pw = (bid - 640) * 4 + __builtin_amdgcn_readfirstlane(tid >> 6);
    const char* src = (const char*)(pw < 512 ? wre : wim) + (size_t)(pw & 511) * 16384 + lane * 16;
    u32x4 v[16];
#pragma unroll
    for (int i = 0; i < 16; ++i) v[i] = *(const u32x4*)(src + i * 1024);
    float rs[4];
#pragma unroll
    for (int r = 0; r < 4; ++r) {
      float a0 = 0.f, a1 = 0.f;
#pragma unroll
      for (int q = 0; q < 4; ++q)
#pragma unroll
        for (int e = 0; e < 4; ++e) { a0 += lo16(v[4 * r + q][e]); a1 += hi16(v[4 * r + q][e]); }
      rs[r] = a0 + a1;
    }
#pragma unroll
    for (int o = 1; o < 64; o <<= 1)
#pragma unroll
      for (int r = 0; r < 4; ++r) rs[r] += __shfl_xor(rs[r], o, 64);
    if (lane == 0) *(f32x4*)(ws + WS_RSUM + pw * 16) = (f32x4){rs[0], rs[1], rs[2], rs[3]};
  }
}

__device__ __forceinline__ void dma_res_tile(const char* base  , unsigned lds, int w, int lane) {
#pragma unroll
  for (int j = 0; j < 3; ++j) {
    int L = 160 * w + 64 * j + lane;
    if (L > 639) L = 639;
    const int r = L / 10, ch = L - r * 10;
    if (j < 2) dma16(r * (D_ * 2) + ch * 16, base, lds + (160 * w + 64 * j) * 16);
    else dma16h(r * (D_ * 2) + ch * 16, base, lds + (160 * w + 128) * 16);
  }
}

struct SchedP2 { static constexpr char s[] = "MRMRMRMMRMRMRMMRMRMRMMMMMMMMM"; };
struct SchedP3 { static constexpr char s[] = "RRMRMRRMRMRRMRMRRMRMMM"; };
struct SchedP4 { static constexpr char s[] = "MRMRMRMMRMRMRMMRMRMMRMRMRMMRMRMRMMRMRMMMMMMMMM"; };
constexpr int cnt_ch(const char* s, char c) { int n = 0; for (; *s; ++s) n += (*s == c); return n; }
static_assert(cnt_ch(SchedP2::s, 'M') == 20 && cnt_ch(SchedP2::s, 'R') == 9, "");
static_assert(cnt_ch(SchedP3::s, 'M') == 10 && cnt_ch(SchedP3::s, 'R') == 12, "");
static_assert(cnt_ch(SchedP4::s, 'M') == 30 && cnt_ch(SchedP4::s, 'R') == 16, "");
constexpr int L_RES = 147456, L_MOD = 159744, L_DUM = L_MOD + 2048, L_TOTAL = L_DUM + 1024;
constexpr int P2_NS = 5, P2_STAGE = 26624;
constexpr int P3_STAGE = 36864;
constexpr int P4_STAGE = 28672, P4_E = 4 * P4_STAGE, P4_ST = P4_E + 4096, P4_RS = P4_ST + 8192;

__global__ __launch_bounds__(512, 2) void mega(const unsigned short* __restrict__ wre,
                                               const unsigned short* __restrict__ wim,
                                               const unsigned short* __restrict__ x,
                                               char* __restrict__ ws,
                                               unsigned short* __restrict__ out) {
  extern __shared__ __attribute__((aligned(16))) char smem[];
  const int tid = threadIdx.x, lane = tid & 63;
  const int wv = __builtin_amdgcn_readfirstlane(tid >> 6);
  const int w = wv & 3;
  const bool prod = wv < 4;
  const int bid = blockIdx.x;
  const int xcd = bid & 7, bi = bid >> 3;
  const int cb = bi & 7, rsel = bi >> 3;
  const int g = lane >> 4, l15 = lane & 15;
  const unsigned lds0 = lds_off(smem);
  const float* mod = (const float*)(smem + L_MOD);
  const int cb3 = (xcd & 1) * 4 + (bi & 3);
  const int t3 = ((xcd >> 1) * 8 + (bi >> 2)) * 64;
  const int cb4 = (xcd & 1) * 8 + (bi & 7), grp4 = (xcd >> 1) * 4 + (bi >> 3);
  const int grp3 = t3 >> 7;

  {
    const int isim = rsel >> 1;
    const int grp2 = xcd * 2 + (rsel & 1);
    const int t0 = grp2 * 128;
    if (prod) {
      const char* rbase = (const char*)(isim ? wim : wre) + (size_t)t0 * (S_ * 2);
      const char* cbase = ws + WS_HT + (size_t)cb * 32 * 10240;
      unsigned roff[4];
#pragma unroll
      for (int j = 0; j < 4; ++j) {
        const int row = 32 * w + 8 * j + (lane >> 3);
        roff[j] = row * (S_ * 2) + 16 * ((lane & 7) ^ ((row >> 1) & 7));
      }
      auto run = [&](auto ncc) {
        constexpr int NC = decltype(ncc)::value;
        const int c0 = (NC == 3) ? w * 3072 : 6144 + (w - 2) * 2048;
        producer<P2_NS, 32, 4 + NC>([&](int ks, int buf) {
          const unsigned lb = lds0 + buf * P2_STAGE;
#pragma unroll
          for (int j = 0; j < 4; ++j) dma16(roff[j], rbase + ks * 128, lb + (32 * w + 8 * j) * 128);
#pragma unroll
          for (int j = 0; j < NC; ++j) dma16(lane * 16, cbase + ks * 10240 + c0 + j * 1024, lb + 16384 + c0 + j * 1024);
        });
      };
      if (w < 2) run(IC<3>{}); else run(IC<2>{});
      bar();
      const char* wdt = ws + WS_WDT + (size_t)cb3 * 204800;
#pragma unroll
      for (int j = 0; j < 5; ++j) dma16(lane * 16, wdt + w * 5120 + j * 1024, lds0 + 16384 + w * 5120 + j * 1024);
      bar();
#pragma unroll
      for (int j = 0; j < 5; ++j) dma16(lane * 16, wdt + 20480 + w * 5120 + j * 1024, lds0 + P3_STAGE + 16384 + w * 5120 + j * 1024);
    } else {
      {
        const char* pc;
        unsigned len;
        asm volatile("s_getpc_b64 %0\n.Lmega_pf_here:\n\ts_mov_b32 %1, .Lmega_pf_end-.Lmega_pf_here" : "=s"(pc), "=s"(len));
        const unsigned off = (bi * 4 + w) * 1024;
        if (off + 1024 <= len) dma16(lane * 16, (const char*)((uintptr_t)pc & ~(uintptr_t)15) + off, lds0 + L_DUM);
      }
      if (w < 2) {
        int L = 64 * w + lane; if (L > 99) L = 99;
        const int vec = L / 20, part = vec + (vec >= 3);
        dma16(part * 2560 + (vec < 2 ? cb : cb3) * 320 + (L - vec * 20) * 16, ws + WS_MOD, lds0 + L_MOD + w * 1024);
      }
      dma_res_tile((const char*)x + (size_t)t3 * (D_ * 2) + cb3 * 160, lds0 + L_RES, w, lane);
      const int wm = w & 1, wk = w >> 1;
      const int fsw = ((4 * wk + g) ^ (l15 >> 1)) * 16;
      const int ra = (64 * wm + l15) * 128 + fsw;
      const int ca_ = 16384 + (2 * wk + (g >> 1)) * 2560 + l15 * 32 + (g & 1) * 16;
      bf16x8 rf[2][4], cf[2][5];
      f32x4 acc[5][4];
#pragma unroll
      for (int j = 0; j < 5; ++j)
#pragma unroll
        for (int i = 0; i < 4; ++i) acc[j][i] = (f32x4){0.f, 0.f, 0.f, 0.f};
      const float sgn = isim ? -1.0f : 1.0f;
      const float* rsum = (const float*)(ws + WS_RSUM) + isim * 2048 + t0 + 64 * wm + 32 * wk + l15;
      const float rsr0 = rsum[0], rsr1 = rsum[16];
      auto ldf = [&](auto par, auto ic, int buf) {
        constexpr int PP = decltype(par)::value, n = decltype(ic)::value;
        const char* lb = smem + buf * P2_STAGE;
        if constexpr (n == 0) rf[PP][0] = lds_frag(lb + ra);
        else if constexpr (n < 6) cf[PP][n - 1] = lds_frag(lb + ca_ + (n - 1) * 512);
        else rf[PP][n - 5] = lds_frag(lb + ra + (n - 5) * 2048);
      };
      auto body = [&](auto par, auto dl, int rd) {
        constexpr int PP = decltype(par)::value, DL = decltype(dl)::value;
        auto fm = [&](auto mc) {
          constexpr int m = decltype(mc)::value, i = m / 5, j = m % 5;
          acc[j][i] = MFMA16(cf[PP][j], rf[PP][i], acc[j][i]);
        };
        auto fr = [&](auto rc) { ldf(IC<PP ^ 1>{}, rc, rd); };
        run_sched<SchedP2, 0, 0, 0, DL>(fm, fr);
      };
      auto load0 = [&] { static_for<9>([&](auto ic) { ldf(IC<0>{}, ic, 0); }); };
      consumer<P2_NS, 32>(load0, body);
      auto finish = [&](auto keepc) {
        constexpr int KP = decltype(keepc)::value;
        bar();
        char* dst = smem + w * P3_STAGE + lane * 16;
#pragma unroll
        for (int j = 0; j < 5; ++j) {
          *(f32x4*)(dst + (2 * j) * 1024) = acc[j][2 * (KP ^ 1)];
          *(f32x4*)(dst + (2 * j + 1) * 1024) = acc[j][2 * (KP ^ 1) + 1];
        }
        wait_vm<0>();
        wait_lgkm0();
        bar();
        const char* srcp = smem + (w ^ 2) * P3_STAGE + lane * 16;
        char* pq = ws + WS_PQ + ((size_t)(grp2 * 2 + wm) * 80 + isim * 40 + cb * 5) * 2048 + (32 * KP + l15) * 32 + g * 8;
        const float rs0 = rsr0 * sgn, rs1 = rsr1 * sgn;
        f32x4 p0[5], p1[5], sa[5], c1[5];
#pragma unroll
        for (int j = 0; j < 5; ++j) {
          p0[j] = *(const f32x4*)(srcp + (2 * j) * 1024);
          p1[j] = *(const f32x4*)(srcp + (2 * j + 1) * 1024);
          sa[j] = *(const f32x4*)(mod + 16 * j + 4 * g);
          c1[j] = *(const f32x4*)(mod + 80 + 16 * j + 4 * g);
        }
        u32x2 o0[5], o1[5];
#pragma unroll
        for (int j = 0; j < 5; ++j) {
          const f32x4 cs = c1[j] * sgn;
          const f32x4 v0 = cs * (acc[j][2 * KP] + p0[j]) + sa[j] * rs0;
          const f32x4 v1 = cs * (acc[j][2 * KP + 1] + p1[j]) + sa[j] * rs1;
          o0[j] = (u32x2){pk2(v0[0], v0[1]), pk2(v0[2], v0[3])};
          o1[j] = (u32x2){pk2(v1[0], v1[1]), pk2(v1[2], v1[3])};
        }
#pragma unroll
        for (int j = 0; j < 5; ++j) {
          st8u_wt(pq + j * 2048, o0[j]);
          st8u_wt(pq + j * 2048 + 512, o1[j]);
        }
      };
      if (wk == 0) finish(IC<0>{}); else finish(IC<1>{});
    }
    if (!prod) wait_vm<0>();
    bar();
    if (wv == 4) {
      if (lane == 0) add_count(ws, grp2);
      wait_count(ws, grp3, 16);
    }
    bar();
  }

  {
    const char* wdt = ws + WS_WDT + (size_t)cb3 * 204800;
    if (prod) {
      const char* rbase = ws + WS_PQ + (size_t)(t3 >> 6) * (80 * 2048) + w * 4096;
      producer_seam<10, 4, 5>([&](int ks, int buf) {
#pragma unroll
        for (int j = 0; j < 4; ++j) dma16s(lane * 16, rbase + ks * 16384 + j * 1024, lds0 + buf * P3_STAGE + w * 4096 + j * 1024);
      }, [&](int ks, int buf) {
#pragma unroll
        for (int j = 0; j < 5; ++j)
          dma16(lane * 16, wdt + ks * 20480 + w * 5120 + j * 1024, lds0 + buf * P3_STAGE + 16384 + w * 5120 + j * 1024);
      }, [] {});
      bar();
      const char* w1t = ws + WS_W1T + (size_t)cb4 * 122880;
      dma16(w * 1024 + lane * 16, ws + WS_E, lds0 + P4_E + w * 1024);
#pragma unroll
      for (int s = 0; s < 2; ++s)
#pragma unroll
        for (int j = 0; j < 3; ++j)
          dma16(lane * 16, w1t + s * 12288 + w * 3072 + j * 1024, lds0 + s * P4_STAGE + 16384 + w * 3072 + j * 1024);
    } else {
      const int sw0 = (g ^ (l15 >> 1)) * 16, sw1 = ((4 + g) ^ (l15 >> 1)) * 16;
      const int ra = (g >> 1) * 2048 + (16 * w + l15) * 32 + (g & 1) * 16, ca_ = 16384 + l15 * 128;
      bf16x8 rf[2][2], cf[2][2][5];
      f32x4 acc[5];
#pragma unroll
      for (int j = 0; j < 5; ++j) acc[j] = (f32x4){0.f, 0.f, 0.f, 0.f};
      auto ldf = [&](auto hc, auto ic, int buf) {
        constexpr int H = decltype(hc)::value, n = decltype(ic)::value, kk = n / 6, m = n % 6;
        const char* lb = smem + buf * P3_STAGE;
        if constexpr (m == 0) rf[H][kk] = lds_frag(lb + ra + H * 8192 + kk * 4096);
        else cf[H][kk][m - 1] = lds_frag(lb + (kk ? sw1 : sw0) + ca_ + H * 10240 + (m - 1) * 2048);
      };
      auto half = [&](auto par, auto dl, int buf) {
        constexpr int PP = decltype(par)::value, DL = decltype(dl)::value;
        auto fm = [&](auto mc) {
          constexpr int m = decltype(mc)::value, kk = m / 5, j = m % 5;
          acc[j] = MFMA16(cf[PP][kk][j], rf[PP][kk], acc[j]);
        };
        auto fr = [&](auto rc) { ldf(IC<PP ^ 1>{}, rc, buf); };
        run_sched<SchedP3, 0, 0, 0, DL>(fm, fr);
      };
      char* res = smem + L_RES + (16 * w + l15) * 160 + g * 8;
      u32x2 xr[5];
      f32x4 ga[5], c1[5];
#pragma unroll
      for (int j = 0; j < 5; ++j) {
        xr[j] = *(const u32x2*)(res + j * 32);
        ga[j] = *(const f32x4*)(mod + 160 + 16 * j + 4 * g);
        c1[j] = *(const f32x4*)(mod + 240 + 16 * j + 4 * g);
      }
      auto load0 = [&] { static_for<12>([&](auto ic) { ldf(IC<0>{}, ic, 0); }); };
      consumer_half<10>(load0, half);
      bar();
      char* xc = ws + WS_XC + ((size_t)(t3 >> 6) * 40 + cb3 * 5) * 2048 + (16 * w + l15) * 32 + g * 8;
      float xf[20];
      float s = 0.f;
#pragma unroll
      for (int j = 0; j < 5; ++j) {
        const f32x4 y = acc[j];
        const u32x2 o = {pk2(lo16(xr[j][0]) + ga[j][0] * y[0], hi16(xr[j][0]) + ga[j][1] * y[1]),
                         pk2(lo16(xr[j][1]) + ga[j][2] * y[2], hi16(xr[j][1]) + ga[j][3] * y[3])};
        xr[j] = o;
        xf[4 * j] = lo16(o[0]); xf[4 * j + 1] = hi16(o[0]); xf[4 * j + 2] = lo16(o[1]); xf[4 * j + 3] = hi16(o[1]);
        st8u_wt(xc + j * 2048, (u32x2){pk2(xf[4 * j] * c1[j][0], xf[4 * j + 1] * c1[j][1]), pk2(xf[4 * j + 2] * c1[j][2], xf[4 * j + 3] * c1[j][3])});
        s += (xf[4 * j] + xf[4 * j + 1]) + (xf[4 * j + 2] + xf[4 * j + 3]);
      }
      s += __shfl_xor(s, 16, 64);
      s += __shfl_xor(s, 32, 64);
      const float ml = s * (1.0f / 80.0f);
      float m2 = 0.f;
#pragma unroll
      for (int e = 0; e < 20; ++e) { float d = xf[e] - ml; m2 += d * d; }
      m2 += __shfl_xor(m2, 16, 64);
      m2 += __shfl_xor(m2, 32, 64);
      if (lane < 16) st8_wt(ws + WS_ST + ((size_t)(t3 >> 6) * 8 + cb3) * 512 + (16 * w + l15) * 8, (f32x2){s, m2});
#pragma unroll
      for (int j = 0; j < 5; ++j) *(u32x2*)(res + j * 32) = xr[j];
      wait_vm<0>();
    }
    bar();
    if (wv == 4) {
      if (lane == 0) add_count(ws, 16 + grp3);
      wait_count(ws, 16 + grp4, 16);
    }
    bar();
  }

  {
    const int t0 = grp4 * 128;
    const char* w1t = ws + WS_W1T + (size_t)cb4 * 122880;
    if (prod) {
      const char* rbase = ws + WS_XC + (size_t)(grp4 * 2 + (w >> 1)) * (40 * 2048) + (w & 1) * 4096;
      dma16s(w * 2048 + lane * 16, ws + WS_ST + (size_t)t0 * 64, lds0 + P4_ST + w * 2048);
      dma16s(w * 2048 + 1024 + lane * 16, ws + WS_ST + (size_t)t0 * 64, lds0 + P4_ST + w * 2048 + 1024);
      producer_seam<10, 4, 3>([&](int ks, int buf) {
#pragma unroll
        for (int j = 0; j < 4; ++j) dma16s(lane * 16, rbase + ks * 8192 + j * 1024, lds0 + buf * P4_STAGE + w * 4096 + j * 1024);
      }, [&](int ks, int buf) {
#pragma unroll
        for (int j = 0; j < 3; ++j)
          dma16(lane * 16, w1t + ks * 12288 + w * 3072 + j * 1024, lds0 + buf * P4_STAGE + 16384 + w * 3072 + j * 1024);
      }, [&] {
        if (tid < 128) {
          const char* sp = smem + P4_ST + (tid >> 6) * 4096 + (tid & 63) * 8;
          f32x2 pp[8];
#pragma unroll
          for (int e = 0; e < 8; ++e) pp[e] = *(const f32x2*)(sp + e * 512);
          const float mean = (((pp[0][0] + pp[1][0]) + (pp[2][0] + pp[3][0])) + ((pp[4][0] + pp[5][0]) + (pp[6][0] + pp[7][0]))) * (1.0f / D_);
          float m2 = ((pp[0][1] + pp[1][1]) + (pp[2][1] + pp[3][1])) + ((pp[4][1] + pp[5][1]) + (pp[6][1] + pp[7][1]));
          float dd = 0.f;
#pragma unroll
          for (int e = 0; e < 8; ++e) { const float d = pp[e][0] * (1.0f / 80.0f) - mean; dd += d * d; }
          m2 += 80.0f * dd;
          const float rden = 1.0f / bfr(sqrtf(bfr(bfr(m2 * (1.0f / D_)) + 1e-5f)));
          *(f32x2*)(smem + P4_RS + tid * 8) = (f32x2){bfr(mean), rden};
        }
        wait_lgkm0();
      });
      bar();
      const char* w2t = ws + WS_W2T + (size_t)cb3 * F_ * 160;
#pragma unroll
      for (int s = 0; s < 2; ++s)
#pragma unroll
        for (int j = 0; j < 5; ++j)
          dma16(lane * 16, w2t + s * 20480 + w * 5120 + j * 1024, lds0 + s * P3_STAGE + 16384 + w * 5120 + j * 1024);
    } else {
      const int q = l15 >> 2, p4 = lane & 3;
      const int wm = w & 1, wn = w >> 1;
      const int rpos = 16 * (g >> 1) + 4 * (g & 1) + q;
      const int xs = (rpos >> 2) & 1;
      int cadr[3];
#pragma unroll
      for (int j = 0; j < 3; ++j) {
        const int jj = 3 * wn + j;
        cadr[j] = 16384 + rpos * 192 + (2 * (jj ^ xs) + (p4 >> 1)) * 16 + (p4 & 1) * 8;
      }
      const int ra = wm * 8192 + (g >> 1) * 2048 + l15 * 32 + (g & 1) * 16;
      int ea = P4_E + (l15 < 2 ? l15 : 2) * 1280 + g * 16;
      bf16x8 rf[2][2][5], cf[2][2][3];
      f32x4 acc[3][5];
#pragma unroll
      for (int j = 0; j < 3; ++j)
#pragma unroll
        for (int i = 0; i < 5; ++i) acc[j][i] = (f32x4){0.f, 0.f, 0.f, 0.f};
      auto ldf = [&](auto par, auto ic, int buf) {
        constexpr int PP = decltype(par)::value, n = decltype(ic)::value, kk = n / 8, m = n % 8;
        const char* lb = smem + buf * P4_STAGE;
        if constexpr (m == 0) rf[PP][kk][0] = lds_frag(lb + ra + kk * 4096);
        else if constexpr (m < 4) cf[PP][kk][m - 1] = cat_frag(lds_tr(lb + cadr[m - 1] + kk * 6144), lds_tr(lb + cadr[m - 1] + kk * 6144 + 1536));
        else if constexpr (m < 7) rf[PP][kk][m - 3] = lds_frag(lb + ra + kk * 4096 + (m - 3) * 512);
        else {
          rf[PP][kk][4] = lds_frag(smem + ea + kk * 64);
          if constexpr (kk == 1) ea += 128;
        }
      };
      auto body = [&](auto par, auto dl, int rd) {
        constexpr int PP = decltype(par)::value, DL = decltype(dl)::value;
        auto fm = [&](auto mc) {
          constexpr int m = decltype(mc)::value, kk = m / 15, i = (m % 15) / 3, j = m % 3;
          acc[j][i] = MFMA16(cf[PP][kk][j], rf[PP][kk][i], acc[j][i]);
        };
        auto fr = [&](auto rc) { ldf(IC<PP ^ 1>{}, rc, rd); };
        run_sched<SchedP4, 0, 0, 0, DL>(fm, fr);
      };
      auto load0 = [&] { static_for<16>([&](auto ic) { ldf(IC<0>{}, ic, 0); }); };
      consumer<4, 10>(load0, body);
      f32x2 rsv[4];
#pragma unroll
      for (int i = 0; i < 4; ++i) rsv[i] = *(const f32x2*)(smem + P4_RS + (64 * wm + 16 * i + l15) * 8);
      wait_lgkm0();
      bar();
      f32x4 u[3], v[3];
#pragma unroll
      for (int j = 0; j < 3; ++j)
#pragma unroll
        for (int e = 0; e < 4; ++e) {
          const int a = __float_as_int(acc[j][4][e]);
          u[j][e] = __int_as_float(__builtin_amdgcn_update_dpp(0, a, 0x150, 0xf, 0xf, false));
          v[j][e] = __int_as_float(__builtin_amdgcn_update_dpp(0, a, 0x151, 0xf, 0xf, false));
        }
      char* h1 = ws + WS_H1 + ((size_t)(grp4 * 2 + wm) * 96 + cb4 * 6 + 3 * wn) * 2048 + l15 * 32 + g * 8;
#pragma unroll
      for (int i = 0; i < 4; ++i) {
        const f32x2 rs = rsv[i];
#pragma unroll
        for (int j = 0; j < 3; ++j) {
          const f32x4 hv = (acc[j][i] - u[j] * rs[0]) * rs[1] + v[j];
          st8u_wt(h1 + j * 2048 + i * 512,
                  (u32x2){pk2(fmaxf(hv[0], 0.f), fmaxf(hv[1], 0.f)), pk2(fmaxf(hv[2], 0.f), fmaxf(hv[3], 0.f))});
        }
      }
      wait_vm<0>();
    }
    bar();
    if (wv == 4) {
      if (lane == 0) add_count(ws, 32 + grp4);
      wait_count(ws, 32 + grp3, 16);
    }
    bar();
  }

  {
    const char* w2t = ws + WS_W2T + (size_t)cb3 * F_ * 160;
    if (prod) {
      const char* rbase = ws + WS_H1 + (size_t)(t3 >> 6) * (96 * 2048) + w * 4096;
      producer_seam<12, 4, 5>([&](int ks, int buf) {
#pragma unroll
        for (int j = 0; j < 4; ++j) dma16s(lane * 16, rbase + ks * 16384 + j * 1024, lds0 + buf * P3_STAGE + w * 4096 + j * 1024);
      }, [&](int ks, int buf) {
#pragma unroll
        for (int j = 0; j < 5; ++j)
          dma16(lane * 16, w2t + ks * 20480 + w * 5120 + j * 1024, lds0 + buf * P3_STAGE + 16384 + w * 5120 + j * 1024);
      }, [] {});
    } else {
      const int q = l15 >> 2, p4 = lane & 3;
      const int ra = (g >> 1) * 2048 + (16 * w + l15) * 32 + (g & 1) * 16;
      const int ca_ = 16384 + (16 * (g >> 1) + 4 * (g & 1) + q) * 160 + p4 * 8;
      bf16x8 rf[2][2], cf[2][2][5];
      f32x4 acc[5];
#pragma unroll
      for (int j = 0; j < 5; ++j) acc[j] = (f32x4){0.f, 0.f, 0.f, 0.f};
      auto ldf = [&](auto hc, auto ic, int buf) {
        constexpr int H = decltype(hc)::value, n = decltype(ic)::value, kk = n / 6, m = n % 6;
        const char* lb = smem + buf * P3_STAGE;
        if constexpr (m == 0) rf[H][kk] = lds_frag(lb + ra + H * 8192 + kk * 4096);
        else cf[H][kk][m - 1] = cat_frag(lds_tr(lb + ca_ + H * 10240 + kk * 5120 + (m - 1) * 32),
                                         lds_tr(lb + ca_ + H * 10240 + kk * 5120 + (m - 1) * 32 + 1280));
      };
      auto half = [&](auto par, auto dl, int buf) {
        constexpr int PP = decltype(par)::value, DL = decltype(dl)::value;
        auto fm = [&](auto mc) {
          constexpr int m = decltype(mc)::value, kk = m / 5, j = m % 5;
          acc[j] = MFMA16(cf[PP][kk][j], rf[PP][kk], acc[j]);
        };
        auto fr = [&](auto rc) { ldf(IC<PP ^ 1>{}, rc, buf); };
        run_sched<SchedP3, 0, 0, 0, DL>(fm, fr);
      };
      char* res = smem + L_RES + (16 * w + l15) * 160 + g * 8;
      u32x2 xr[5];
      f32x4 gb[5];
#pragma unroll
      for (int j = 0; j < 5; ++j) {
        xr[j] = *(const u32x2*)(res + j * 32);
        gb[j] = *(const f32x4*)(mod + 320 + 16 * j + 4 * g);
      }
      auto load0 = [&] { static_for<12>([&](auto ic) { ldf(IC<0>{}, ic, 0); }); };
      consumer_half<12>(load0, half);
#pragma unroll
      for (int j = 0; j < 5; ++j) {
        const f32x4 y = acc[j];
        *(u32x2*)(res + j * 32) = (u32x2){pk2(lo16(xr[j][0]) + gb[j][0] * y[0], hi16(xr[j][0]) + gb[j][1] * y[1]),
                                          pk2(lo16(xr[j][1]) + gb[j][2] * y[2], hi16(xr[j][1]) + gb[j][3] * y[3])};
      }
      wait_vm<0>();
    }
    wait_lgkm0();
    bar();
    for (int id = tid; id < 640; id += 512) {
      const int row = id / 10, c = id - row * 10;
      *(u32x4*)((char*)out + (size_t)(t3 + row) * (D_ * 2) + cb3 * 160 + c * 16) = *(const u32x4*)(smem + L_RES + row * 160 + c * 16);
    }
  }
  asm volatile(".Lmega_pf_end:");
}

extern "C" void kernel_launch(void* const* d_in, const int* in_sizes, int n_in,
                              void* d_out, int out_size, void* d_ws, size_t ws_size,
                              hipStream_t stream) {
  const unsigned short* x    = (const unsigned short*)d_in[0];
  const unsigned short* wsre = (const unsigned short*)d_in[1];
  const unsigned short* wsim = (const unsigned short*)d_in[2];
  const unsigned short* wdre = (const unsigned short*)d_in[3];
  const unsigned short* wdim = (const unsigned short*)d_in[4];
  const unsigned short* w1   = (const unsigned short*)d_in[6];
  const unsigned short* w2   = (const unsigned short*)d_in[7];
  const unsigned short* c    = (const unsigned short*)d_in[8];
  const unsigned short* wmod = (const unsigned short*)d_in[9];
  char* ws = (char*)d_ws;
  unsigned short* out = (unsigned short*)d_out;
  (void)hipFuncSetAttribute((const void*)mega, hipFuncAttributeMaxDynamicSharedMemorySize, L_TOTAL);
  k1_ln_mod<<<640 + 256 + 24, 256, 0, stream>>>(x, c, wmod, wsre, wsim, w2, wdre, wdim, w1, out, ws);
  mega<<<256, 512, L_TOTAL, stream>>>(wsre, wsim, x, ws, out);
}
```

```cpp
#include <hip/hip_runtime.h>
#include <hip/hip_bf16.h>
#include <stdint.h>

typedef __attribute__((ext_vector_type(8))) __bf16 bf16x8;
typedef __attribute__((ext_vector_type(2))) __bf16 bf16x2;
typedef __attribute__((ext_vector_type(4))) float f32x4;
typedef __attribute__((ext_vector_type(2))) float f32x2;
typedef __attribute__((ext_vector_type(4))) short s16x4;
typedef __attribute__((ext_vector_type(8))) short s16x8;
typedef __attribute__((ext_vector_type(4))) unsigned u32x4;
typedef __attribute__((ext_vector_type(2))) unsigned u32x2;
typedef __attribute__((address_space(3))) s16x4* lds_s16x4_ptr;

constexpr int S_ = 2048, D_ = 640, F_ = 1536, MODN = 3840;

constexpr size_t WS_MOD = 0;
constexpr size_t WS_E   = 16384;
constexpr size_t WS_CNT = 20480;
constexpr size_t WS_ST  = 32768;
constexpr size_t WS_RSUM = WS_ST + (size_t)S_ * 64;
constexpr size_t WS_HT  = WS_RSUM + 16384;
constexpr size_t WS_PQ  = WS_HT + (size_t)D_ * S_ * 2;
constexpr size_t WS_XC  = WS_PQ + (size_t)S_ * 1280 * 2;
constexpr size_t WS_H1  = WS_XC + (size_t)S_ * D_ * 2;
constexpr size_t WS_W2T = WS_H1 + (size_t)S_ * F_ * 2;
constexpr size_t WS_W1T = WS_W2T + (size_t)F_ * D_ * 2;
constexpr size_t WS_WDT = WS_W1T + (size_t)F_ * D_ * 2;

template <int V> struct IC { static constexpr int value = V; };
template <int N, int I = 0, class F> __device__ __forceinline__ void static_for(F f) {
  if constexpr (I < N) { f(IC<I>{}); static_for<N, I + 1>(f); }
}

__device__ __forceinline__ float bf2f(unsigned short u) { return __uint_as_float(((unsigned)u) << 16); }
__device__ __forceinline__ float bfr(float f) { return (float)(__bf16)f; }
__device__ __forceinline__ unsigned pk2(float a, float b) {
  bf16x2 v = __builtin_convertvector((f32x2){a, b}, bf16x2);
  return __builtin_bit_cast(unsigned, v);
}
__device__ __forceinline__ float lo16(unsigned u) { return __uint_as_float(u << 16); }
__device__ __forceinline__ float hi16(unsigned u) { return __uint_as_float(u & 0xffff0000u); }

__device__ __forceinline__ void dma16(unsigned voff, const char* sbase, unsigned lds) {
  asm volatile("s_mov_b32 m0, %2\n\ts_nop 0\n\tglobal_load_lds_dwordx4 %0, %1"
               ::"v"(voff), "s"(sbase), "s"(lds) : "memory");
}
__device__ __forceinline__ void dma16s(unsigned voff, const char* sbase, unsigned lds) {
  asm volatile("s_mov_b32 m0, %2\n\ts_nop 0\n\tglobal_load_lds_dwordx4 %0, %1 sc1"
               ::"v"(voff), "s"(sbase), "s"(lds) : "memory");
}
#define LD1(p) __builtin_nontemporal_load(p)
__device__ __forceinline__ void dma16h(unsigned voff, const char* sbase, unsigned lds) {
  asm volatile("s_mov_b32 m0, %2\n\ts_mov_b32 exec_hi, 0\n\tglobal_load_lds_dwordx4 %0, %1\n\ts_mov_b32 exec_hi, -1"
               ::"v"(voff), "s"(sbase), "s"(lds) : "memory");
}
__device__ __forceinline__ unsigned lds_off(const void* p) {
  return (unsigned)(uintptr_t)(__attribute__((address_space(3))) const char*)p;
}
#define SB0() __builtin_amdgcn_sched_barrier(0)
__device__ __forceinline__ void bar() {
  asm volatile("" ::: "memory");
  __builtin_amdgcn_s_barrier();
  asm volatile("" ::: "memory");
}
__device__ __forceinline__ bf16x8 lds_frag(const char* p) { return *(const bf16x8*)p; }
__device__ __forceinline__ s16x4 lds_tr(const char* p) {
  return __builtin_amdgcn_ds_read_tr16_b64_v4i16((lds_s16x4_ptr)p);
}
__device__ __forceinline__ bf16x8 cat_frag(s16x4 a, s16x4 b) {
  s16x8 v = __builtin_shufflevector(a, b, 0, 1, 2, 3, 4, 5, 6, 7);
  return __builtin_bit_cast(bf16x8, v);
}
#define MFMA16(a, b, c) __builtin_amdgcn_mfma_f32_16x16x32_bf16((a), (b), (c), 0, 0, 0)

template <class Sc, int POS, int NM, int NR, int DL, class FM, class FR>
__device__ __forceinline__ void run_sched(FM& fm, FR& fr) {
  if constexpr (Sc::s[POS] != 0) {
    constexpr char c = Sc::s[POS];
    if constexpr (c == 'M') {
      fm(IC<NM>{}); SB0();
      run_sched<Sc, POS + 1, NM + 1, NR, DL>(fm, fr);
    } else {
      if constexpr (DL) { fr(IC<NR>{}); SB0(); }
      run_sched<Sc, POS + 1, NM, NR + 1, DL>(fm, fr);
    }
  }
}
template <int N> __device__ __forceinline__ void wait_vm() { asm volatile("s_waitcnt vmcnt(%0)" ::"n"(N) : "memory"); }
__device__ __forceinline__ void wait_lgkm0() { asm volatile("s_waitcnt lgkmcnt(0)" ::: "memory"); }

template <int NS, int P, int TI>
__device__ __forceinline__ void prod_tail() {
  if constexpr (TI < NS - 1) {
    wait_vm<P*(NS - 2 - TI)>();
    bar();
    prod_tail<NS, P, TI + 1>();
  }
}
constexpr int ramp_A(int j, int NS) {
  int a = 2;
  for (int i = 1; i <= j; ++i) { int lim = (i == 1) ? NS : (i - 1) + NS; a = (a + 2 < lim) ? a + 2 : lim; }
  return a;
}
constexpr int ramp_len(int NS) { int j = 1; while (ramp_A(j, NS) != ((j == 1) ? NS : (j - 1) + NS) || j < 2) ++j; return j; }
template <int NS, int P, int J, int R, class Issue>
__device__ __forceinline__ void prod_ramp(Issue& issue) {
  if constexpr (J < R) {
    constexpr int a0 = (J == 0) ? 0 : ramp_A(J - 1, NS), a1 = ramp_A(J, NS);
#pragma unroll
    for (int s = a0; s < a1; ++s) issue(s, s % NS);
    wait_vm<P*(a1 - (J + 1))>();
    bar();
    prod_ramp<NS, P, J + 1, R>(issue);
  }
}
template <int NS, int NK, int P, class Issue>
__device__ __forceinline__ void producer(Issue issue) {
  constexpr int R = ramp_len(NS);
  static_assert(R - 1 + NS <= NK, "ramp too long");
  prod_ramp<NS, P, 0, R>(issue);
#pragma unroll
  for (int s = ramp_A(R - 1, NS); s < R - 1 + NS; ++s) issue(s, s % NS);
  int wr = (R - 1) % NS;
#pragma nounroll
  for (int i = R - 1; i < NK - NS; ++i) {
    wait_vm<P*(NS - 2)>();
    bar();
    issue(i + NS, wr);
    wr = (wr + 1 == NS) ? 0 : wr + 1;
  }
  prod_tail<NS, P, 0>();
}
template <int NK, int PR, int PC, class IR, class ICC, class Mid>
__device__ __forceinline__ void producer_seam(IR issueR, ICC issueC, Mid mid) {
  constexpr int P = PR + PC;
  issueR(0, 0); issueR(1, 1);
  wait_vm<PR>(); bar();
  issueR(2, 2); issueC(2, 2); issueR(3, 3); issueC(3, 3);
  wait_vm<2 * P>(); bar();
  mid();
  constexpr int J0 = 2;
#pragma nounroll
  for (int j = J0; j <= NK - 3; ++j) {
    issueR(j + 2, (j + 2) & 3); issueC(j + 2, (j + 2) & 3);
    wait_vm<2 * P>(); bar();
  }
  wait_vm<P>(); bar();
  wait_vm<0>(); bar();
}
template <int NS, int NK, class Load0, class Body>
__device__ __forceinline__ void consumer(Load0 load0, Body body) {
  static_assert((NK & 1) == 0, "even number of stages");
  bar();
  load0();
  int rd = 1;
#pragma nounroll
  for (int i = 0; i < NK - 2; i += 2) {
    wait_lgkm0();
    bar();
    SB0();
    body(IC<0>{}, IC<1>{}, rd);
    SB0();
    rd = (rd + 1 == NS) ? 0 : rd + 1;
    wait_lgkm0();
    bar();
    SB0();
    body(IC<1>{}, IC<1>{}, rd);
    SB0();
    rd = (rd + 1 == NS) ? 0 : rd + 1;
  }
  wait_lgkm0();
  bar();
  SB0();
  body(IC<0>{}, IC<1>{}, rd);
  SB0();
  body(IC<1>{}, IC<0>{}, 0);
  SB0();
}
template <int NK, class Load0, class Half>
__device__ __forceinline__ void consumer_half(Load0 load0, Half half) {
  bar();
  load0();
#pragma nounroll
  for (int s = 0; s < NK - 1; ++s) {
    SB0();
    half(IC<0>{}, IC<1>{}, s & 3);
    SB0();
    wait_lgkm0();
    bar();
    SB0();
    half(IC<1>{}, IC<1>{}, (s + 1) & 3);
    SB0();
  }
  half(IC<0>{}, IC<1>{}, (NK - 1) & 3);
  SB0();
  half(IC<1>{}, IC<0>{}, 0);
  SB0();
}
typedef __attribute__((address_space(1))) unsigned gu32;
__device__ __forceinline__ void st8u_wt(void* p, u32x2 v) {
  asm volatile("global_store_dwordx2 %0, %1, off sc1\n\ts_nop 1" ::"v"(p), "v"(v) : "memory");
}
__device__ __forceinline__ void st8_wt(void* p, f32x2 v) {
  asm volatile("global_store_dwordx2 %0, %1, off sc1\n\ts_nop 1" ::"v"(p), "v"(v) : "memory");
}
__device__ __forceinline__ void wait_count(char* ws, int idx, unsigned need) {
  gu32* cnt = (gu32*)(ws + WS_CNT + idx * 256);
#pragma nounroll
  for (unsigned spins = 0; spins < (1u << 22); ++spins) {
    if ((unsigned)__builtin_amdgcn_readfirstlane(__hip_atomic_load(cnt, __ATOMIC_RELAXED, __HIP_MEMORY_SCOPE_AGENT)) >= need) break;
    __builtin_amdgcn_s_sleep(1);
  }
  asm volatile("" ::: "memory");
}
__device__ __forceinline__ void add_count(char* ws, int idx) {
  __hip_atomic_fetch_add((gu32*)(ws + WS_CNT + idx * 256), 1u, __ATOMIC_RELAXED, __HIP_MEMORY_SCOPE_AGENT);
}
__device__ __forceinline__ int swap23(int r) { return (r & ~12) | ((r & 4) << 1) | ((r & 8) >> 1); }

__global__ __launch_bounds__(256) void k1_ln_mod(const unsigned short* __restrict__ x,
                                                 const unsigned short* __restrict__ c,
                                                 const unsigned short* __restrict__ wmod,
                                                 const unsigned short* __restrict__ wre,
                                                 const unsigned short* __restrict__ wim,
                                                 const unsigned short* __restrict__ w2,
                                                 const unsigned short* __restrict__ dre,
                                                 const unsigned short* __restrict__ dim,
                                                 const unsigned short* __restrict__ w1,
                                                 const unsigned short* __restrict__ outp,
                                                 char* __restrict__ ws) {
  __shared__ __attribute__((aligned(16))) char smem[16 * 1552];
  const int tid = threadIdx.x, lane = tid & 63;
  const int bid = blockIdx.x;
  u32x4 codepf;
  {
    const char* pc;
    unsigned len;
    asm volatile("s_getpc_b64 %0\n.Lk1_pf_here:\n\ts_mov_b32 %1, .Lmega_pf_end-.Lk1_pf_here" : "=s"(pc), "=s"(len));
    unsigned off = (((bid >> 3) * 4 + __builtin_amdgcn_readfirstlane(tid >> 6)) & 63) * 1024;
    if ((int)len < 1024 || off + 1024 > len) off = 0;
    asm volatile("global_load_dwordx4 %0, %1, %2" : "=v"(codepf) : "v"(lane * 16), "s"((const char*)((uintptr_t)pc & ~(uintptr_t)15) + off) : "memory");
  }
  if (bid < 128) {
    const int xcd = bid & 7, i = bid >> 3;
    const int rg = xcd * 4 + (i >> 2), sub = i & 3;
    const int row0 = rg * 64 + sub * 16;
    const int r = tid >> 4, j = tid & 15;
    const u32x4* src = (const u32x4*)(x + (size_t)(row0 + r) * D_);
    u32x4 v[5];
#pragma unroll
    for (int q = 0; q < 5; ++q) v[q] = src[j + 16 * q];
    float f[40];
#pragma unroll
    for (int q = 0; q < 5; ++q)
#pragma unroll
      for (int e = 0; e < 4; ++e) { f[q * 8 + 2 * e] = lo16(v[q][e]); f[q * 8 + 2 * e + 1] = hi16(v[q][e]); }
    float s = 0.f;
#pragma unroll
    for (int e = 0; e < 40; ++e) s += f[e];
#pragma unroll
    for (int o = 1; o < 16; o <<= 1) s += __shfl_xor(s, o, 64);
    const float mean = s * (1.0f / D_);
    float s2 = 0.f;
#pragma unroll
    for (int e = 0; e < 40; ++e) { float d = f[e] - mean; s2 += d * d; }
#pragma unroll
    for (int o = 1; o < 16; o <<= 1) s2 += __shfl_xor(s2, o, 64);
    const float mb = bfr(mean);
    const float vb = bfr(s2 * (1.0f / D_));
    const float rden = 1.0f / bfr(sqrtf(bfr(vb + 1e-5f)));
#pragma unroll
    for (int q = 0; q < 5; ++q) {
      u32x4 o;
#pragma unroll
      for (int e = 0; e < 4; ++e) {
        float a = bfr(f[q * 8 + 2 * e] - mb) * rden;
        float b = bfr(f[q * 8 + 2 * e + 1] - mb) * rden;
        o[e] = pk2(a, b);
      }
      *(u32x4*)(smem + r * 1296 + (j + 16 * q) * 16) = o;
    }
    wait_lgkm0();
    bar();
    const int grp = tid >> 4, li = tid & 15, qq = li >> 2, pp = li & 3;
    char* ht = ws + WS_HT + (size_t)(row0 >> 4) * 2560;
#pragma unroll
    for (int it = 0; it < 5; ++it) {
      const int pr = it * 16 + grp;
      const int cblk = pr >> 1, half = pr & 1;
      const char* a0 = smem + (8 * half + qq) * 1296 + (16 * cblk + 4 * pp) * 2;
      s16x4 t0 = lds_tr(a0);
      s16x4 t1 = lds_tr(a0 + 4 * 1296);
      s16x8 o = __builtin_shufflevector(t0, t1, 0, 1, 2, 3, 4, 5, 6, 7);
      const int d = 16 * cblk + li;
      const int cb = d / 80, rr = d - cb * 80;
      *(u32x4*)(ht + (size_t)cb * (128 * 2560) + rr * 32 + half * 16) = __builtin_bit_cast(u32x4, o);
    }
    if (bid == 0 && tid < 96) *(u32x4*)(ws + WS_E + 2560 + tid * 16) = (u32x4){0u, 0u, 0u, 0u};
    if (bid == 1 && tid < 48) *(unsigned*)(ws + WS_CNT + tid * 256) = 0u;
  } else if (bid < 368) {
    const int g = bid - 128;
    const int cgp = (g & 7) * 30 + (g >> 3);
    const int l2 = tid & 1, rg = tid >> 1;
    u32x4 v[5];
    unsigned short cv16[5];
#pragma unroll
    for (int q = 0; q < 5; ++q) {
      const int k = q * 128 + rg;
      v[q] = *(const u32x4*)(wmod + (size_t)k * MODN + cgp * 16 + l2 * 8);
      cv16[q] = c[k];
    }
    float acc[8];
#pragma unroll
    for (int e = 0; e < 8; ++e) acc[e] = 0.f;
#pragma unroll
    for (int q = 0; q < 5; ++q) {
      const float cv = bf2f(cv16[q]);
      const float sv = bfr(cv / (1.0f + __expf(-cv)));
#pragma unroll
      for (int e = 0; e < 4; ++e) {
        acc[2 * e] += sv * lo16(v[q][e]);
        acc[2 * e + 1] += sv * hi16(v[q][e]);
      }
    }
#pragma unroll
    for (int o = 2; o < 64; o <<= 1)
#pragma unroll
      for (int e = 0; e < 8; ++e) acc[e] += __shfl_xor(acc[e], o, 64);
    float* red = (float*)smem;
    if (lane < 2) {
#pragma unroll
      for (int e = 0; e < 8; ++e) red[(tid >> 6) * 16 + lane * 8 + e] = acc[e];
    }
    wait_lgkm0();
    bar();
    if (tid < 16) {
      float m = bfr((red[tid] + red[16 + tid]) + (red[32 + tid] + red[48 + tid]));
      const int n = cgp * 16 + tid;
      const int part = n / D_, d = n - part * D_;
      if (part == 1 || part == 4) m = bfr(1.0f + m);
      ((float*)(ws + WS_MOD))[n] = m;
      if (part == 4) ((unsigned short*)(ws + WS_E))[d] = (unsigned short)(__float_as_uint(m) >> 16);
      if (part == 3) ((unsigned short*)(ws + WS_E))[D_ + d] = (unsigned short)(__float_as_uint(m) >> 16);
    }
  } else if (bid < 560) {
    const bool isw2 = bid < 464;
    const int b1 = bid - 464;
    if (isw2 || b1 < 80) {
      const int kg = isw2 ? bid - 368 : (b1 >> 1);
      const int nch = isw2 ? 80 : 96;
      const int n0 = isw2 ? 0 : (b1 & 1) * 768;
      const char* src = isw2 ? (const char*)w2 + (size_t)kg * 16 * (D_ * 2) : (const char*)w1 + (size_t)kg * 16 * (F_ * 2) + n0 * 2;
      const int pitch = isw2 ? D_ * 2 : F_ * 2;
      const int r = tid >> 4, j = tid & 15;
      u32x4 v[6];
#pragma unroll
      for (int q = 0; q < 6; ++q)
        if (q < 5 || !isw2) v[q] = LD1((const u32x4*)(src + (size_t)r * pitch + (j + 16 * q) * 16));
#pragma unroll
      for (int q = 0; q < 6; ++q)
        if (q < 5 || !isw2) *(u32x4*)(smem + r * 1552 + (j + 16 * q) * 16) = v[q];
      wait_lgkm0();
      bar();
      const int grp = tid >> 4, li = tid & 15, qq = li >> 2, pp = li & 3;
#pragma unroll
      for (int it = 0; it < 6; ++it) {
        if (it < 5 || !isw2) {
          const int pr = it * 16 + grp;
          const int cblk = pr >> 1, half = pr & 1;
          const char* a0 = smem + (8 * half + qq) * 1552 + (16 * cblk + 4 * pp) * 2;
          s16x4 t0 = lds_tr(a0);
          s16x4 t1 = lds_tr(a0 + 4 * 1552);
          s16x8 o = __builtin_shufflevector(t0, t1, 0, 1, 2, 3, 4, 5, 6, 7);
          const int n = n0 + 16 * cblk + li;
          const int cbk = n / nch, rr = n - cbk * nch;
          char* dst = isw2 ? ws + WS_W2T + ((size_t)(cbk * 96 + kg) * 80 + rr) * 32 : ws + WS_W1T + ((size_t)(cbk * 40 + kg) * 96 + rr) * 32;
          *(u32x4*)(dst + half * 16) = __builtin_bit_cast(u32x4, o);
        }
      }
    }
  } else if (bid < 640) {
    const int m = bid >= 600, c0 = (bid - 560 - 40 * m) * 1280;
    u32x4 v[5];
#pragma unroll
    for (int q = 0; q < 5; ++q) v[q] = LD1((const u32x4*)((const char*)(m ? dim : dre) + (size_t)(c0 + tid + 256 * q) * 16));
#pragma unroll
    for (int q = 0; q < 5; ++q) {
      const int id = c0 + tid + 256 * q;
      const int e = id / 80, ch = id - e * 80;
      const int cb = e / 80, row = e - cb * 80;
      *(u32x4*)(ws + WS_WDT + ((size_t)(cb * 20 + m * 10 + (ch >> 3)) * 80 + row) * 128 + ((ch & 7) ^ ((row >> 1) & 7)) * 16) = v[q];
    }
  } else if (bid >= 640 + 256) {
    const int t = ((bid - 640 - 256) >> 3) * 256 + tid;
    const char* a;
    if (t < 384) a = ws + (size_t)t * 65536;
    else if (t < 512) a = (const char*)wre + (size_t)(t - 384) * 65536;
    else if (t < 640) a = (const char*)wim + (size_t)(t - 512) * 65536;
    else if (t < 680) a = (const char*)x + (size_t)(t - 640) * 65536;
    else if (t < 720) a = (const char*)outp + (size_t)(t - 680) * 65536;
    else a = ws;
    unsigned v;
    asm volatile("global_load_dword %0, %1, off" : "=v"(v) : "v"(a) : "memory");
    wait_vm<0>();
    asm volatile("" ::"v"(v));
  } else {
    const int pw = (bid - 640) * 4 + __builtin_amdgcn_readfirstlane(tid >> 6);
    const char* src = (const char*)(pw < 512 ? wre : wim) + (size_t)(pw & 511) * 16384 + lane * 16;
    u32x4 v[16];
#pragma unroll
    for (int i = 0; i < 16; ++i) v[i] = *(const u32x4*)(src + i * 1024);
    float rs[4];
#pragma unroll
    for (int r = 0; r < 4; ++r) {
      float a0 = 0.f, a1 = 0.f;
#pragma unroll
      for (int q = 0; q < 4; ++q)
#pragma unroll
        for (int e = 0; e < 4; ++e) { a0 += lo16(v[4 * r + q][e]); a1 += hi16(v[4 * r + q][e]); }
      rs[r] = a0 + a1;
    }
#pragma unroll
    for (int o = 1; o < 64; o <<= 1)
#pragma unroll
      for (int r = 0; r < 4; ++r) rs[r] += __shfl_xor(rs[r], o, 64);
    if (lane == 0) *(f32x4*)(ws + WS_RSUM + pw * 16) = (f32x4){rs[0], rs[1], rs[2], rs[3]};
  }
  wait_vm<0>();
  asm volatile("" ::"v"(codepf));
}

__device__ __forceinline__ void dma_res_tile(const char* base  , unsigned lds, int w, int lane) {
#pragma unroll
  for (int j = 0; j < 3; ++j) {
    int L = 160 * w + 64 * j + lane;
    if (L > 639) L = 639;
    const int r = L / 10, ch = L - r * 10;
    if (j < 2) dma16(r * (D_ * 2) + ch * 16, base, lds + (160 * w + 64 * j) * 16);
    else dma16h(r * (D_ * 2) + ch * 16, base, lds + (160 * w + 128) * 16);
  }
}

struct SchedP2 { static constexpr char s[] = "MRMRMRMRMRMRMRMRMRMRMRMRMRMRMMMMMM"; };
struct SchedP3 { static constexpr char s[] = "RRMRMRRMRMRRMRMRRMRMMM"; };
struct SchedP4 { static constexpr char s[] = "MRMRMRMMRMRMRMMRMRMMRMRMRMMRMRMRMMRMRMMMMMMMMM"; };
constexpr int cnt_ch(const char* s, char c) { int n = 0; for (; *s; ++s) n += (*s == c); return n; }
static_assert(cnt_ch(SchedP2::s, 'M') == 20 && cnt_ch(SchedP2::s, 'R') == 14, "");
static_assert(cnt_ch(SchedP3::s, 'M') == 10 && cnt_ch(SchedP3::s, 'R') == 12, "");
static_assert(cnt_ch(SchedP4::s, 'M') == 30 && cnt_ch(SchedP4::s, 'R') == 16, "");
constexpr int L_RES = 147456, L_MOD = 159744, L_DUM = L_MOD + 2048, L_TOTAL = L_DUM + 1024;
constexpr int P2_NS = 5, P2_STAGE = 26624;
constexpr int P3_STAGE = 36864;
constexpr int P4_STAGE = 28672, P4_E = 4 * P4_STAGE, P4_ST = P4_E + 4096, P4_RS = P4_ST + 8192;

__global__ __launch_bounds__(512, 2) void mega(const unsigned short* __restrict__ wre,
                                               const unsigned short* __restrict__ wim,
                                               const unsigned short* __restrict__ x,
                                               char* __restrict__ ws,
                                               unsigned short* __restrict__ out) {
  extern __shared__ __attribute__((aligned(16))) char smem[];
  const int tid = threadIdx.x, lane = tid & 63;
  const int wv = __builtin_amdgcn_readfirstlane(tid >> 6);
  const int w = wv & 3;
  const bool prod = wv < 4;
  const int bid = blockIdx.x;
  const int xcd = bid & 7, bi = bid >> 3;
  const int cb = bi & 7, rsel = bi >> 3;
  const int g = lane >> 4, l15 = lane & 15;
  const unsigned lds0 = lds_off(smem);
  const float* mod = (const float*)(smem + L_MOD);
  const int cb3 = (xcd & 1) * 4 + (bi & 3);
  const int t3 = ((xcd >> 1) * 8 + (bi >> 2)) * 64;
  const int cb4 = (xcd & 1) * 8 + (bi & 7), grp4 = (xcd >> 1) * 4 + (bi >> 3);
  const int grp3 = t3 >> 7;

  {
    const int isim = rsel >> 1;
    const int grp2 = xcd * 2 + (rsel & 1);
    const int t0 = grp2 * 128;
    if (prod) {
      const char* rbase = (const char*)(isim ? wim : wre) + (size_t)t0 * (S_ * 2);
      const char* cbase = ws + WS_HT + (size_t)cb * 32 * 10240;
      unsigned roff[4];
#pragma unroll
      for (int j = 0; j < 4; ++j) {
        const int row = 32 * w + 8 * j + (lane >> 3);
        roff[j] = row * (S_ * 2) + 16 * ((lane & 7) ^ ((row >> 1) & 7));
      }
      auto run = [&](auto ncc) {
        constexpr int NC = decltype(ncc)::value;
        const int c0 = (NC == 3) ? w * 3072 : 6144 + (w - 2) * 2048;
        producer<P2_NS, 32, 4 + NC>([&](int ks, int buf) {
          const unsigned lb = lds0 + buf * P2_STAGE;
#pragma unroll
          for (int j = 0; j < 4; ++j) dma16(roff[j], rbase + ks * 128, lb + (32 * w + 8 * j) * 128);
#pragma unroll
          for (int j = 0; j < NC; ++j) dma16(lane * 16, cbase + ks * 10240 + c0 + j * 1024, lb + 16384 + c0 + j * 1024);
        });
      };
      if (w < 2) run(IC<3>{}); else run(IC<2>{});
      bar();
      const char* wdt = ws + WS_WDT + (size_t)cb3 * 204800;
#pragma unroll
      for (int j = 0; j < 5; ++j) dma16(lane * 16, wdt + w * 5120 + j * 1024, lds0 + 16384 + w * 5120 + j * 1024);
#pragma unroll
      for (int j = 0; j < 5; ++j) dma16(lane * 16, wdt + 20480 + w * 5120 + j * 1024, lds0 + P3_STAGE + 16384 + w * 5120 + j * 1024);
    } else {
      {
        const char* pc;
        unsigned len;
        asm volatile("s_getpc_b64 %0\n.Lmega_pf_here:\n\ts_mov_b32 %1, .Lmega_pf_end-.Lmega_pf_here" : "=s"(pc), "=s"(len));
        const unsigned off = (bi * 4 + w) * 1024;
        if (off + 1024 <= len) dma16(lane * 16, (const char*)((uintptr_t)pc & ~(uintptr_t)15) + off, lds0 + L_DUM);
      }
      if (w < 2) {
        int L = 64 * w + lane; if (L > 99) L = 99;
        const int vec = L / 20, part = vec + (vec >= 3);
        dma16(part * 2560 + (vec < 2 ? cb : cb3) * 320 + (L - vec * 20) * 16, ws + WS_MOD, lds0 + L_MOD + w * 1024);
      }
      dma_res_tile((const char*)x + (size_t)t3 * (D_ * 2) + cb3 * 160, lds0 + L_RES, w, lane);
      const int sw0 = (g ^ (l15 >> 1)) * 16, sw1 = ((4 + g) ^ (l15 >> 1)) * 16;
      const int ra = (32 * w + l15) * 128;
      const int ca_ = 16384 + (g >> 1) * 2560 + l15 * 32 + (g & 1) * 16;
      bf16x8 rf[2][2][2], cf[2][2][5];
      f32x4 acc[5][2];
#pragma unroll
      for (int j = 0; j < 5; ++j) { acc[j][0] = (f32x4){0.f, 0.f, 0.f, 0.f}; acc[j][1] = (f32x4){0.f, 0.f, 0.f, 0.f}; }
      const float sgn = isim ? -1.0f : 1.0f;
      const float* rsum = (const float*)(ws + WS_RSUM) + isim * 2048 + t0 + 32 * w + l15;
      const float rsr0 = rsum[0], rsr1 = rsum[16];
      auto ldf = [&](auto par, auto ic, int buf) {
        constexpr int PP = decltype(par)::value, n = decltype(ic)::value, kk = n / 7, m = n % 7;
        const char* lb = smem + buf * P2_STAGE;
        if constexpr (m == 0) rf[PP][kk][0] = lds_frag(lb + ra + (kk ? sw1 : sw0));
        else if constexpr (m < 6) cf[PP][kk][m - 1] = lds_frag(lb + ca_ + kk * 5120 + (m - 1) * 512);
        else rf[PP][kk][1] = lds_frag(lb + ra + (kk ? sw1 : sw0) + 2048);
      };
      auto body = [&](auto par, auto dl, int rd) {
        constexpr int PP = decltype(par)::value, DL = decltype(dl)::value;
        auto fm = [&](auto mc) {
          constexpr int m = decltype(mc)::value, kk = m / 10, i = (m % 10) / 5, j = m % 5;
          acc[j][i] = MFMA16(cf[PP][kk][j], rf[PP][kk][i], acc[j][i]);
        };
        auto fr = [&](auto rc) { ldf(IC<PP ^ 1>{}, rc, rd); };
        run_sched<SchedP2, 0, 0, 0, DL>(fm, fr);
      };
      auto load0 = [&] { static_for<14>([&](auto ic) { ldf(IC<0>{}, ic, 0); }); wait_vm<0>(); };
      consumer<P2_NS, 32>(load0, body);
      f32x4 sa[5], c1[5];
#pragma unroll
      for (int j = 0; j < 5; ++j) {
        sa[j] = *(const f32x4*)(mod + 16 * j + 4 * g);
        c1[j] = *(const f32x4*)(mod + 80 + 16 * j + 4 * g);
      }
      bar();
      char* pq = ws + WS_PQ + ((size_t)(grp2 * 2 + (w >> 1)) * 80 + isim * 40 + cb * 5) * 2048 + (32 * (w & 1) + l15) * 32 + g * 8;
      const float rs0 = rsr0 * sgn, rs1 = rsr1 * sgn;
#pragma unroll
      for (int j = 0; j < 5; ++j) {
        const f32x4 cs = c1[j] * sgn;
        const f32x4 v0 = cs * acc[j][0] + sa[j] * rs0;
        const f32x4 v1 = cs * acc[j][1] + sa[j] * rs1;
        st8u_wt(pq + j * 2048, (u32x2){pk2(v0[0], v0[1]), pk2(v0[2], v0[3])});
        st8u_wt(pq + j * 2048 + 512, (u32x2){pk2(v1[0], v1[1]), pk2(v1[2], v1[3])});
      }
    }
    if (!prod) wait_vm<0>();
    bar();
    if (wv == 4) {
      if (lane == 0) add_count(ws, grp2);
      wait_count(ws, grp3, 16);
    }
    bar();
  }

  {
    const char* wdt = ws + WS_WDT + (size_t)cb3 * 204800;
    if (prod) {
      const char* rbase = ws + WS_PQ + (size_t)(t3 >> 6) * (80 * 2048) + w * 4096;
      producer_seam<10, 4, 5>([&](int ks, int buf) {
#pragma unroll
        for (int j = 0; j < 4; ++j) dma16s(lane * 16, rbase + ks * 16384 + j * 1024, lds0 + buf * P3_STAGE + w * 4096 + j * 1024);
      }, [&](int ks, int buf) {
#pragma unroll
        for (int j = 0; j < 5; ++j)
          dma16(lane * 16, wdt + ks * 20480 + w * 5120 + j * 1024, lds0 + buf * P3_STAGE + 16384 + w * 5120 + j * 1024);
      }, [] {});
      bar();
      const char* w1t = ws + WS_W1T + (size_t)cb4 * 122880;
      dma16(w * 1024 + lane * 16, ws + WS_E, lds0 + P4_E + w * 1024);
#pragma unroll
      for (int s = 0; s < 2; ++s)
#pragma unroll
        for (int j = 0; j < 3; ++j)
          dma16(lane * 16, w1t + s * 12288 + w * 3072 + j * 1024, lds0 + s * P4_STAGE + 16384 + w * 3072 + j * 1024);
    } else {
      const int sw0 = (g ^ (l15 >> 1)) * 16, sw1 = ((4 + g) ^ (l15 >> 1)) * 16;
      const int ra = (g >> 1) * 2048 + (16 * w + l15) * 32 + (g & 1) * 16, ca_ = 16384 + l15 * 128;
      bf16x8 rf[2][2], cf[2][2][5];
      f32x4 acc[5];
#pragma unroll
      for (int j = 0; j < 5; ++j) acc[j] = (f32x4){0.f, 0.f, 0.f, 0.f};
      auto ldf = [&](auto hc, auto ic, int buf) {
        constexpr int H = decltype(hc)::value, n = decltype(ic)::value, kk = n / 6, m = n % 6;
        const char* lb = smem + buf * P3_STAGE;
        if constexpr (m == 0) rf[H][kk] = lds_frag(lb + ra + H * 8192 + kk * 4096);
        else cf[H][kk][m - 1] = lds_frag(lb + (kk ? sw1 : sw0) + ca_ + H * 10240 + (m - 1) * 2048);
      };
      auto half = [&](auto par, auto dl, int buf) {
        constexpr int PP = decltype(par)::value, DL = decltype(dl)::value;
        auto fm = [&](auto mc) {
          constexpr int m = decltype(mc)::value, kk = m / 5, j = m % 5;
          acc[j] = MFMA16(cf[PP][kk][j], rf[PP][kk], acc[j]);
        };
        auto fr = [&](auto rc) { ldf(IC<PP ^ 1>{}, rc, buf); };
        run_sched<SchedP3, 0, 0, 0, DL>(fm, fr);
      };
      char* res = smem + L_RES + (16 * w + l15) * 160 + g * 8;
      u32x2 xr[5];
      f32x4 ga[5], c1[5];
#pragma unroll
      for (int j = 0; j < 5; ++j) {
        xr[j] = *(const u32x2*)(res + j * 32);
        ga[j] = *(const f32x4*)(mod + 160 + 16 * j + 4 * g);
        c1[j] = *(const f32x4*)(mod + 240 + 16 * j + 4 * g);
      }
      auto load0 = [&] { static_for<12>([&](auto ic) { ldf(IC<0>{}, ic, 0); }); };
      consumer_half<10>(load0, half);
      bar();
      char* xc = ws + WS_XC + ((size_t)(t3 >> 6) * 40 + cb3 * 5) * 2048 + (16 * w + l15) * 32 + g * 8;
      float xf[20];
      float s = 0.f;
#pragma unroll
      for (int j = 0; j < 5; ++j) {
        const f32x4 y = acc[j];
        const u32x2 o = {pk2(lo16(xr[j][0]) + ga[j][0] * y[0], hi16(xr[j][0]) + ga[j][1] * y[1]),
                         pk2(lo16(xr[j][1]) + ga[j][2] * y[2], hi16(xr[j][1]) + ga[j][3] * y[3])};
        xr[j] = o;
        xf[4 * j] = lo16(o[0]); xf[4 * j + 1] = hi16(o[0]); xf[4 * j + 2] = lo16(o[1]); xf[4 * j + 3] = hi16(o[1]);
        st8u_wt(xc + j * 2048, (u32x2){pk2(xf[4 * j] * c1[j][0], xf[4 * j + 1] * c1[j][1]), pk2(xf[4 * j + 2] * c1[j][2], xf[4 * j + 3] * c1[j][3])});
        s += (xf[4 * j] + xf[4 * j + 1]) + (xf[4 * j + 2] + xf[4 * j + 3]);
      }
      s += __shfl_xor(s, 16, 64);
      s += __shfl_xor(s, 32, 64);
      const float ml = s * (1.0f / 80.0f);
      float m2 = 0.f;
#pragma unroll
      for (int e = 0; e < 20; ++e) { float d = xf[e] - ml; m2 += d * d; }
      m2 += __shfl_xor(m2, 16, 64);
      m2 += __shfl_xor(m2, 32, 64);
      if (lane < 16) st8_wt(ws + WS_ST + ((size_t)(t3 >> 6) * 8 + cb3) * 512 + (16 * w + l15) * 8, (f32x2){s, m2});
#pragma unroll
      for (int j = 0; j < 5; ++j) *(u32x2*)(res + j * 32) = xr[j];
      wait_vm<0>();
    }
    bar();
    if (wv == 4) {
      if (lane == 0) add_count(ws, 16 + grp3);
      wait_count(ws, 16 + grp4, 16);
    }
    bar();
  }

  {
    const int t0 = grp4 * 128;
    const char* w1t = ws + WS_W1T + (size_t)cb4 * 122880;
    if (prod) {
      const char* rbase = ws + WS_XC + (size_t)(grp4 * 2 + (w >> 1)) * (40 * 2048) + (w & 1) * 4096;
      dma16s(w * 2048 + lane * 16, ws + WS_ST + (size_t)t0 * 64, lds0 + P4_ST + w * 2048);
      dma16s(w * 2048 + 1024 + lane * 16, ws + WS_ST + (size_t)t0 * 64, lds0 + P4_ST + w * 2048 + 1024);
      producer_seam<10, 4, 3>([&](int ks, int buf) {
#pragma unroll
        for (int j = 0; j < 4; ++j) dma16s(lane * 16, rbase + ks * 8192 + j * 1024, lds0 + buf * P4_STAGE + w * 4096 + j * 1024);
      }, [&](int ks, int buf) {
#pragma unroll
        for (int j = 0; j < 3; ++j)
          dma16(lane * 16, w1t + ks * 12288 + w * 3072 + j * 1024, lds0 + buf * P4_STAGE + 16384 + w * 3072 + j * 1024);
      }, [&] {
        if (tid < 128) {
          const char* sp = smem + P4_ST + (tid >> 6) * 4096 + (tid & 63) * 8;
          f32x2 pp[8];
#pragma unroll
          for (int e = 0; e < 8; ++e) pp[e] = *(const f32x2*)(sp + e * 512);
          const float mean = (((pp[0][0] + pp[1][0]) + (pp[2][0] + pp[3][0])) + ((pp[4][0] + pp[5][0]) + (pp[6][0] + pp[7][0]))) * (1.0f / D_);
          float m2 = ((pp[0][1] + pp[1][1]) + (pp[2][1] + pp[3][1])) + ((pp[4][1] + pp[5][1]) + (pp[6][1] + pp[7][1]));
          float dd = 0.f;
#pragma unroll
          for (int e = 0; e < 8; ++e) { const float d = pp[e][0] * (1.0f / 80.0f) - mean; dd += d * d; }
          m2 += 80.0f * dd;
          const float rden = 1.0f / bfr(sqrtf(bfr(bfr(m2 * (1.0f / D_)) + 1e-5f)));
          *(f32x2*)(smem + P4_RS + tid * 8) = (f32x2){bfr(mean), rden};
        }
        wait_lgkm0();
      });
      bar();
      const char* w2t = ws + WS_W2T + (size_t)cb3 * F_ * 160;
#pragma unroll
      for (int s = 0; s < 2; ++s)
#pragma unroll
        for (int j = 0; j < 5; ++j)
          dma16(lane * 16, w2t + s * 20480 + w * 5120 + j * 1024, lds0 + s * P3_STAGE + 16384 + w * 5120 + j * 1024);
    } else {
      const int wm = w & 1, wn = w >> 1;
      const int ca_ = 16384 + (g >> 1) * 3072 + (48 * wn + l15) * 32 + (g & 1) * 16;
      const int ra = wm * 8192 + (g >> 1) * 2048 + l15 * 32 + (g & 1) * 16;
      int ea = P4_E + (l15 < 2 ? l15 : 2) * 1280 + g * 16;
      bf16x8 rf[2][2][5], cf[2][2][3];
      f32x4 acc[3][5];
#pragma unroll
      for (int j = 0; j < 3; ++j)
#pragma unroll
        for (int i = 0; i < 5; ++i) acc[j][i] = (f32x4){0.f, 0.f, 0.f, 0.f};
      auto ldf = [&](auto par, auto ic, int buf) {
        constexpr int PP = decltype(par)::value, n = decltype(ic)::value, kk = n / 8, m = n % 8;
        const char* lb = smem + buf * P4_STAGE;
        if constexpr (m == 0) rf[PP][kk][0] = lds_frag(lb + ra + kk * 4096);
        else if constexpr (m < 4) cf[PP][kk][m - 1] = lds_frag(lb + ca_ + kk * 6144 + (m - 1) * 512);
        else if constexpr (m < 7) rf[PP][kk][m - 3] = lds_frag(lb + ra + kk * 4096 + (m - 3) * 512);
        else {
          rf[PP][kk][4] = lds_frag(smem + ea + kk * 64);
          if constexpr (kk == 1) ea += 128;
        }
      };
      auto body = [&](auto par, auto dl, int rd) {
        constexpr int PP = decltype(par)::value, DL = decltype(dl)::value;
        auto fm = [&](auto mc) {
          constexpr int m = decltype(mc)::value, kk = m / 15, i = (m % 15) / 3, j = m % 3;
          acc[j][i] = MFMA16(cf[PP][kk][j], rf[PP][kk][i], acc[j][i]);
        };
        auto fr = [&](auto rc) { ldf(IC<PP ^ 1>{}, rc, rd); };
        run_sched<SchedP4, 0, 0, 0, DL>(fm, fr);
      };
      auto load0 = [&] { static_for<16>([&](auto ic) { ldf(IC<0>{}, ic, 0); }); };
      consumer<4, 10>(load0, body);
      f32x2 rsv[4];
#pragma unroll
      for (int i = 0; i < 4; ++i) rsv[i] = *(const f32x2*)(smem + P4_RS + (64 * wm + 16 * i + l15) * 8);
      wait_lgkm0();
      bar();
      f32x4 u[3], v[3];
#pragma unroll
      for (int j = 0; j < 3; ++j)
#pragma unroll
        for (int e = 0; e < 4; ++e) {
          const int a = __float_as_int(acc[j][4][e]);
          u[j][e] = __int_as_float(__builtin_amdgcn_update_dpp(0, a, 0x150, 0xf, 0xf, false));
          v[j][e] = __int_as_float(__builtin_amdgcn_update_dpp(0, a, 0x151, 0xf, 0xf, false));
        }
      char* h1 = ws + WS_H1 + ((size_t)(grp4 * 2 + wm) * 96 + cb4 * 6 + 3 * wn) * 2048 + l15 * 32 + g * 8;
#pragma unroll
      for (int i = 0; i < 4; ++i) {
        const f32x2 rs = rsv[i];
#pragma unroll
        for (int j = 0; j < 3; ++j) {
          const f32x4 hv = (acc[j][i] - u[j] * rs[0]) * rs[1] + v[j];
          st8u_wt(h1 + j * 2048 + i * 512,
                  (u32x2){pk2(fmaxf(hv[0], 0.f), fmaxf(hv[1], 0.f)), pk2(fmaxf(hv[2], 0.f), fmaxf(hv[3], 0.f))});
        }
      }
      wait_vm<0>();
    }
    bar();
    if (wv == 4) {
      if (lane == 0) add_count(ws, 32 + grp4);
      wait_count(ws, 32 + grp3, 16);
    }
    bar();
  }

  {
    const char* w2t = ws + WS_W2T + (size_t)cb3 * F_ * 160;
    if (prod) {
      const char* rbase = ws + WS_H1 + (size_t)(t3 >> 6) * (96 * 2048) + w * 4096;
      producer_seam<12, 4, 5>([&](int ks, int buf) {
#pragma unroll
        for (int j = 0; j < 4; ++j) dma16s(lane * 16, rbase + ks * 16384 + j * 1024, lds0 + buf * P3_STAGE + w * 4096 + j * 1024);
      }, [&](int ks, int buf) {
#pragma unroll
        for (int j = 0; j < 5; ++j)
          dma16(lane * 16, w2t + ks * 20480 + w * 5120 + j * 1024, lds0 + buf * P3_STAGE + 16384 + w * 5120 + j * 1024);
      }, [] {});
    } else {
      const int ra = (g >> 1) * 2048 + (16 * w + l15) * 32 + (g & 1) * 16;
      const int ca_ = 16384 + (g >> 1) * 2560 + l15 * 32 + (g & 1) * 16;
      bf16x8 rf[2][2], cf[2][2][5];
      f32x4 acc[5];
#pragma unroll
      for (int j = 0; j < 5; ++j) acc[j] = (f32x4){0.f, 0.f, 0.f, 0.f};
      auto ldf = [&](auto hc, auto ic, int buf) {
        constexpr int H = decltype(hc)::value, n = decltype(ic)::value, kk = n / 6, m = n % 6;
        const char* lb = smem + buf * P3_STAGE;
        if constexpr (m == 0) rf[H][kk] = lds_frag(lb + ra + H * 8192 + kk * 4096);
        else cf[H][kk][m - 1] = lds_frag(lb + ca_ + H * 10240 + kk * 5120 + (m - 1) * 512);
      };
      auto half = [&](auto par, auto dl, int buf) {
        constexpr int PP = decltype(par)::value, DL = decltype(dl)::value;
        auto fm = [&](auto mc) {
          constexpr int m = decltype(mc)::value, kk = m / 5, j = m % 5;
          acc[j] = MFMA16(cf[PP][kk][j], rf[PP][kk], acc[j]);
        };
        auto fr = [&](auto rc) { ldf(IC<PP ^ 1>{}, rc, buf); };
        run_sched<SchedP3, 0, 0, 0, DL>(fm, fr);
      };
      char* res = smem + L_RES + (16 * w + l15) * 160 + g * 8;
      u32x2 xr[5];
      f32x4 gb[5];
#pragma unroll
      for (int j = 0; j < 5; ++j) {
        xr[j] = *(const u32x2*)(res + j * 32);
        gb[j] = *(const f32x4*)(mod + 320 + 16 * j + 4 * g);
      }
      auto load0 = [&] { static_for<12>([&](auto ic) { ldf(IC<0>{}, ic, 0); }); };
      consumer_half<12>(load0, half);
#pragma unroll
      for (int j = 0; j < 5; ++j) {
        const f32x4 y = acc[j];
        *(u32x2*)(res + j * 32) = (u32x2){pk2(lo16(xr[j][0]) + gb[j][0] * y[0], hi16(xr[j][0]) + gb[j][1] * y[1]),
                                          pk2(lo16(xr[j][1]) + gb[j][2] * y[2], hi16(xr[j][1]) + gb[j][3] * y[3])};
      }
      wait_vm<0>();
    }
    wait_lgkm0();
    bar();
    for (int id = tid; id < 640; id += 512) {
      const int row = id / 10, c = id - row * 10;
      *(u32x4*)((char*)out + (size_t)(t3 + row) * (D_ * 2) + cb3 * 160 + c * 16) = *(const u32x4*)(smem + L_RES + row * 160 + c * 16);
    }
  }
  asm volatile(".Lmega_pf_end:");
}

extern "C" void kernel_launch(void* const* d_in, const int* in_sizes, int n_in,
                              void* d_out, int out_size, void* d_ws, size_t ws_size,
                              hipStream_t stream) {
  const unsigned short* x    = (const unsigned short*)d_in[0];
  const unsigned short* wsre = (const unsigned short*)d_in[1];
  const unsigned short* wsim = (const unsigned short*)d_in[2];
  const unsigned short* wdre = (const unsigned short*)d_in[3];
  const unsigned short* wdim = (const unsigned short*)d_in[4];
  const unsigned short* w1   = (const unsigned short*)d_in[6];
  const unsigned short* w2   = (const unsigned short*)d_in[7];
  const unsigned short* c    = (const unsigned short*)d_in[8];
  const unsigned short* wmod = (const unsigned short*)d_in[9];
  char* ws = (char*)d_ws;
  unsigned short* out = (unsigned short*)d_out;
  (void)hipFuncSetAttribute((const void*)mega, hipFuncAttributeMaxDynamicSharedMemorySize, L_TOTAL);
  k1_ln_mod<<<640 + 256 + 24, 256, 0, stream>>>(x, c, wmod, wsre, wsim, w2, wdre, wdim, w1, out, ws);
  mega<<<256, 512, L_TOTAL, stream>>>(wsre, wsim, x, ws, out);
}
```

```cpp
#include <hip/hip_runtime.h>
#include <hip/hip_bf16.h>
#include <stdint.h>

typedef __attribute__((ext_vector_type(8))) __bf16 bf16x8;
typedef __attribute__((ext_vector_type(2))) __bf16 bf16x2;
typedef __attribute__((ext_vector_type(4))) float f32x4;
typedef __attribute__((ext_vector_type(2))) float f32x2;
typedef __attribute__((ext_vector_type(4))) short s16x4;
typedef __attribute__((ext_vector_type(8))) short s16x8;
typedef __attribute__((ext_vector_type(4))) unsigned u32x4;
typedef __attribute__((ext_vector_type(2))) unsigned u32x2;
typedef __attribute__((address_space(3))) s16x4* lds_s16x4_ptr;

constexpr int S_ = 2048, D_ = 640, F_ = 1536, MODN = 3840;

constexpr size_t WS_MOD = 0;
constexpr size_t WS_E   = 16384;
constexpr size_t WS_CNT = 20480;
constexpr size_t WS_ST  = 32768;
constexpr size_t WS_RSUM = WS_ST + (size_t)S_ * 64;
constexpr size_t WS_HT  = WS_RSUM + 16384;
constexpr size_t WS_PQ  = WS_HT + (size_t)D_ * S_ * 2;
constexpr size_t WS_XC  = WS_PQ + (size_t)S_ * 1280 * 2;
constexpr size_t WS_H1  = WS_XC + (size_t)S_ * D_ * 2;
constexpr size_t WS_W2T = WS_H1 + (size_t)S_ * F_ * 2;
constexpr size_t WS_W1T = WS_W2T + (size_t)F_ * D_ * 2;
constexpr size_t WS_WDT = WS_W1T + (size_t)F_ * D_ * 2;

template <int V> struct IC { static constexpr int value = V; };
template <int N, int I = 0, class F> __device__ __forceinline__ void static_for(F f) {
  if constexpr (I < N) { f(IC<I>{}); static_for<N, I + 1>(f); }
}

__device__ __forceinline__ float bf2f(unsigned short u) { return __uint_as_float(((unsigned)u) << 16); }
__device__ __forceinline__ float bfr(float f) { return (float)(__bf16)f; }
__device__ __forceinline__ unsigned pk2(float a, float b) {
  bf16x2 v = __builtin_convertvector((f32x2){a, b}, bf16x2);
  return __builtin_bit_cast(unsigned, v);
}
__device__ __forceinline__ float lo16(unsigned u) { return __uint_as_float(u << 16); }
__device__ __forceinline__ float hi16(unsigned u) { return __uint_as_float(u & 0xffff0000u); }

__device__ __forceinline__ void dma16(unsigned voff, const char* sbase, unsigned lds) {
  asm volatile("s_mov_b32 m0, %2\n\ts_nop 0\n\tglobal_load_lds_dwordx4 %0, %1"
               ::"v"(voff), "s"(sbase), "s"(lds) : "memory");
}
__device__ __forceinline__ void dma16s(unsigned voff, const char* sbase, unsigned lds) {
  asm volatile("s_mov_b32 m0, %2\n\ts_nop 0\n\tglobal_load_lds_dwordx4 %0, %1 sc1"
               ::"v"(voff), "s"(sbase), "s"(lds) : "memory");
}
#define LD1(p) __builtin_nontemporal_load(p)
__device__ __forceinline__ void dma16h(unsigned voff, const char* sbase, unsigned lds) {
  asm volatile("s_mov_b32 m0, %2\n\ts_mov_b32 exec_hi, 0\n\tglobal_load_lds_dwordx4 %0, %1\n\ts_mov_b32 exec_hi, -1"
               ::"v"(voff), "s"(sbase), "s"(lds) : "memory");
}
__device__ __forceinline__ unsigned lds_off(const void* p) {
  return (unsigned)(uintptr_t)(__attribute__((address_space(3))) const char*)p;
}
#define SB0() __builtin_amdgcn_sched_barrier(0)
__device__ __forceinline__ void bar() {
  asm volatile("" ::: "memory");
  __builtin_amdgcn_s_barrier();
  asm volatile("" ::: "memory");
}
__device__ __forceinline__ bf16x8 lds_frag(const char* p) { return *(const bf16x8*)p; }
__device__ __forceinline__ s16x4 lds_tr(const char* p) {
  return __builtin_amdgcn_ds_read_tr16_b64_v4i16((lds_s16x4_ptr)p);
}
#define MFMA16(a, b, c) __builtin_amdgcn_mfma_f32_16x16x32_bf16((a), (b), (c), 0, 0, 0)

template <class Sc, int POS, int NM, int NR, int DL, class FM, class FR>
__device__ __forceinline__ void run_sched(FM& fm, FR& fr) {
  if constexpr (Sc::s[POS] != 0) {
    constexpr char c = Sc::s[POS];
    if constexpr (c == 'M') {
      fm(IC<NM>{}); SB0();
      run_sched<Sc, POS + 1, NM + 1, NR, DL>(fm, fr);
    } else {
      if constexpr (DL) { fr(IC<NR>{}); SB0(); }
      run_sched<Sc, POS + 1, NM, NR + 1, DL>(fm, fr);
    }
  }
}
template <int N> __device__ __forceinline__ void wait_vm() { asm volatile("s_waitcnt vmcnt(%0)" ::"n"(N) : "memory"); }
__device__ __forceinline__ void wait_lgkm0() { asm volatile("s_waitcnt lgkmcnt(0)" ::: "memory"); }

template <int NS, int P, int TI>
__device__ __forceinline__ void prod_tail() {
  if constexpr (TI < NS - 1) {
    wait_vm<P*(NS - 2 - TI)>();
    bar();
    prod_tail<NS, P, TI + 1>();
  }
}
constexpr int ramp_A(int j, int NS) {
  int a = 2;
  for (int i = 1; i <= j; ++i) { int lim = (i == 1) ? NS : (i - 1) + NS; a = (a + 2 < lim) ? a + 2 : lim; }
  return a;
}
constexpr int ramp_len(int NS) { int j = 1; while (ramp_A(j, NS) != ((j == 1) ? NS : (j - 1) + NS) || j < 2) ++j; return j; }
template <int NS, int P, int J, int R, class Issue>
__device__ __forceinline__ void prod_ramp(Issue& issue) {
  if constexpr (J < R) {
    constexpr int a0 = (J == 0) ? 0 : ramp_A(J - 1, NS), a1 = ramp_A(J, NS);
#pragma unroll
    for (int s = a0; s < a1; ++s) issue(s, s % NS);
    wait_vm<P*(a1 - (J + 1))>();
    bar();
    prod_ramp<NS, P, J + 1, R>(issue);
  }
}
template <int NS, int NK, int P, class Issue>
__device__ __forceinline__ void producer(Issue issue) {
  constexpr int R = ramp_len(NS);
  static_assert(R - 1 + NS <= NK, "ramp too long");
  prod_ramp<NS, P, 0, R>(issue);
#pragma unroll
  for (int s = ramp_A(R - 1, NS); s < R - 1 + NS; ++s) issue(s, s % NS);
  int wr = (R - 1) % NS;
#pragma nounroll
  for (int i = R - 1; i < NK - NS; ++i) {
    wait_vm<P*(NS - 2)>();
    bar();
    issue(i + NS, wr);
    wr = (wr + 1 == NS) ? 0 : wr + 1;
  }
  prod_tail<NS, P, 0>();
}
template <int NK, int PR, int PC, class IR, class ICC, class Mid>
__device__ __forceinline__ void producer_seam(IR issueR, ICC issueC, Mid mid) {
  constexpr int P = PR + PC;
  issueR(0, 0); issueR(1, 1);
  wait_vm<PR>(); bar();
  issueR(2, 2); issueC(2, 2); issueR(3, 3); issueC(3, 3);
  wait_vm<2 * P>(); bar();
  mid();
  constexpr int J0 = 2;
#pragma nounroll
  for (int j = J0; j <= NK - 3; ++j) {
    issueR(j + 2, (j + 2) & 3); issueC(j + 2, (j + 2) & 3);
    wait_vm<2 * P>(); bar();
  }
  wait_vm<P>(); bar();
  wait_vm<0>(); bar();
}
template <int NS, int NK, class Load0, class Body>
__device__ __forceinline__ void consumer(Load0 load0, Body body) {
  static_assert((NK & 1) == 0, "even number of stages");
  bar();
  load0();
  int rd = 1;
#pragma nounroll
  for (int i = 0; i < NK - 2; i += 2) {
    wait_lgkm0();
    bar();
    SB0();
    body(IC<0>{}, IC<1>{}, rd);
    SB0();
    rd = (rd + 1 == NS) ? 0 : rd + 1;
    wait_lgkm0();
    bar();
    SB0();
    body(IC<1>{}, IC<1>{}, rd);
    SB0();
    rd = (rd + 1 == NS) ? 0 : rd + 1;
  }
  wait_lgkm0();
  bar();
  SB0();
  body(IC<0>{}, IC<1>{}, rd);
  SB0();
  body(IC<1>{}, IC<0>{}, 0);
  SB0();
}
template <int NK, class Load0, class Half>
__device__ __forceinline__ void consumer_half(Load0 load0, Half half) {
  bar();
  load0();
#pragma nounroll
  for (int s = 0; s < NK - 1; ++s) {
    SB0();
    half(IC<0>{}, IC<1>{}, s & 3);
    SB0();
    wait_lgkm0();
    bar();
    SB0();
    half(IC<1>{}, IC<1>{}, (s + 1) & 3);
    SB0();
  }
  half(IC<0>{}, IC<1>{}, (NK - 1) & 3);
  SB0();
  half(IC<1>{}, IC<0>{}, 0);
  SB0();
}
typedef __attribute__((address_space(1))) unsigned gu32;
__device__ __forceinline__ void st8u_wt(void* p, u32x2 v) {
  asm volatile("global_store_dwordx2 %0, %1, off sc1\n\ts_nop 1" ::"v"(p), "v"(v) : "memory");
}
__device__ __forceinline__ void st8_wt(void* p, f32x2 v) {
  asm volatile("global_store_dwordx2 %0, %1, off sc1\n\ts_nop 1" ::"v"(p), "v"(v) : "memory");
}
__device__ __forceinline__ void wait_count(char* ws, int idx, unsigned need) {
  gu32* cnt = (gu32*)(ws + WS_CNT + idx * 256);
#pragma nounroll
  for (unsigned spins = 0; spins < (1u << 22); ++spins) {
    if ((unsigned)__builtin_amdgcn_readfirstlane(__hip_atomic_load(cnt, __ATOMIC_RELAXED, __HIP_MEMORY_SCOPE_AGENT)) >= need) break;
    __builtin_amdgcn_s_sleep(1);
  }
  asm volatile("" ::: "memory");
}
__device__ __forceinline__ void add_count(char* ws, int idx) {
  __hip_atomic_fetch_add((gu32*)(ws + WS_CNT + idx * 256), 1u, __ATOMIC_RELAXED, __HIP_MEMORY_SCOPE_AGENT);
}

__global__ __launch_bounds__(256) void k1_ln_mod(const unsigned short* __restrict__ x,
                                                 const unsigned short* __restrict__ c,
                                                 const unsigned short* __restrict__ wmod,
                                                 const unsigned short* __restrict__ wre,
                                                 const unsigned short* __restrict__ wim,
                                                 const unsigned short* __restrict__ w2,
                                                 const unsigned short* __restrict__ dre,
                                                 const unsigned short* __restrict__ dim,
                                                 const unsigned short* __restrict__ w1,
                                                 const unsigned short* __restrict__ outp,
                                                 char* __restrict__ ws) {
  __shared__ __attribute__((aligned(16))) char smem[16 * 1552];
  const int tid = threadIdx.x, lane = tid & 63;
  const int bid = blockIdx.x;
  if (bid < 128) {
    const int xcd = bid & 7, i = bid >> 3;
    const int rg = xcd * 4 + (i >> 2), sub = i & 3;
    const int row0 = rg * 64 + sub * 16;
    const int r = tid >> 4, j = tid & 15;
    const u32x4* src = (const u32x4*)(x + (size_t)(row0 + r) * D_);
    u32x4 v[5];
#pragma unroll
    for (int q = 0; q < 5; ++q) v[q] = src[j + 16 * q];
    float f[40];
#pragma unroll
    for (int q = 0; q < 5; ++q)
#pragma unroll
      for (int e = 0; e < 4; ++e) { f[q * 8 + 2 * e] = lo16(v[q][e]); f[q * 8 + 2 * e + 1] = hi16(v[q][e]); }
    float s = 0.f;
#pragma unroll
    for (int e = 0; e < 40; ++e) s += f[e];
#pragma unroll
    for (int o = 1; o < 16; o <<= 1) s += __shfl_xor(s, o, 64);
    const float mean = s * (1.0f / D_);
    float s2 = 0.f;
#pragma unroll
    for (int e = 0; e < 40; ++e) { float d = f[e] - mean; s2 += d * d; }
#pragma unroll
    for (int o = 1; o < 16; o <<= 1) s2 += __shfl_xor(s2, o, 64);
    const float mb = bfr(mean);
    const float vb = bfr(s2 * (1.0f / D_));
    const float rden = 1.0f / bfr(sqrtf(bfr(vb + 1e-5f)));
#pragma unroll
    for (int q = 0; q < 5; ++q) {
      u32x4 o;
#pragma unroll
      for (int e = 0; e < 4; ++e) {
        float a = bfr(f[q * 8 + 2 * e] - mb) * rden;
        float b = bfr(f[q * 8 + 2 * e + 1] - mb) * rden;
        o[e] = pk2(a, b);
      }
      *(u32x4*)(smem + r * 1296 + (j + 16 * q) * 16) = o;
    }
    wait_lgkm0();
    bar();
    const int grp = tid >> 4, li = tid & 15, qq = li >> 2, pp = li & 3;
    char* ht = ws + WS_HT + (size_t)(row0 >> 4) * 2560;
#pragma unroll
    for (int it = 0; it < 5; ++it) {
      const int pr = it * 16 + grp;
      const int cblk = pr >> 1, half = pr & 1;
      const char* a0 = smem + (8 * half + qq) * 1296 + (16 * cblk + 4 * pp) * 2;
      s16x4 t0 = lds_tr(a0);
      s16x4 t1 = lds_tr(a0 + 4 * 1296);
      s16x8 o = __builtin_shufflevector(t0, t1, 0, 1, 2, 3, 4, 5, 6, 7);
      const int d = 16 * cblk + li;
      const int cb = d / 80, rr = d - cb * 80;
      *(u32x4*)(ht + (size_t)cb * (128 * 2560) + rr * 32 + half * 16) = __builtin_bit_cast(u32x4, o);
    }
    if (bid == 0 && tid < 96) *(u32x4*)(ws + WS_E + 2560 + tid * 16) = (u32x4){0u, 0u, 0u, 0u};
    if (bid == 1 && tid < 48) *(unsigned*)(ws + WS_CNT + tid * 256) = 0u;
  } else if (bid < 368) {
    const int g = bid - 128;
    const int cgp = (g & 7) * 30 + (g >> 3);
    const int l2 = tid & 1, rg = tid >> 1;
    u32x4 v[5];
    unsigned short cv16[5];
#pragma unroll
    for (int q = 0; q < 5; ++q) {
      const int k = q * 128 + rg;
      v[q] = *(const u32x4*)(wmod + (size_t)k * MODN + cgp * 16 + l2 * 8);
      cv16[q] = c[k];
    }
    float acc[8];
#pragma unroll
    for (int e = 0; e < 8; ++e) acc[e] = 0.f;
#pragma unroll
    for (int q = 0; q < 5; ++q) {
      const float cv = bf2f(cv16[q]);
      const float sv = bfr(cv / (1.0f + __expf(-cv)));
#pragma unroll
      for (int e = 0; e < 4; ++e) {
        acc[2 * e] += sv * lo16(v[q][e]);
        acc[2 * e + 1] += sv * hi16(v[q][e]);
      }
    }
#pragma unroll
    for (int o = 2; o < 64; o <<= 1)
#pragma unroll
      for (int e = 0; e < 8; ++e) acc[e] += __shfl_xor(acc[e], o, 64);
    float* red = (float*)smem;
    if (lane < 2) {
#pragma unroll
      for (int e = 0; e < 8; ++e) red[(tid >> 6) * 16 + lane * 8 + e] = acc[e];
    }
    wait_lgkm0();
    bar();
    if (tid < 16) {
      float m = bfr((red[tid] + red[16 + tid]) + (red[32 + tid] + red[48 + tid]));
      const int n = cgp * 16 + tid;
      const int part = n / D_, d = n - part * D_;
      if (part == 1 || part == 4) m = bfr(1.0f + m);
      ((float*)(ws + WS_MOD))[n] = m;
      if (part == 4) ((unsigned short*)(ws + WS_E))[d] = (unsigned short)(__float_as_uint(m) >> 16);
      if (part == 3) ((unsigned short*)(ws + WS_E))[D_ + d] = (unsigned short)(__float_as_uint(m) >> 16);
    }
  } else if (bid < 560) {
    const bool isw2 = bid < 464;
    const int b1 = bid - 464;
    if (isw2 || b1 < 80) {
      const int kg = isw2 ? bid - 368 : (b1 >> 1);
      const int nch = isw2 ? 80 : 96;
      const int n0 = isw2 ? 0 : (b1 & 1) * 768;
      const char* src = isw2 ? (const char*)w2 + (size_t)kg * 16 * (D_ * 2) : (const char*)w1 + (size_t)kg * 16 * (F_ * 2) + n0 * 2;
      const int pitch = isw2 ? D_ * 2 : F_ * 2;
      const int r = tid >> 4, j = tid & 15;
      u32x4 v[6];
#pragma unroll
      for (int q = 0; q < 6; ++q)
        if (q < 5 || !isw2) v[q] = LD1((const u32x4*)(src + (size_t)r * pitch + (j + 16 * q) * 16));
#pragma unroll
      for (int q = 0; q < 6; ++q)
        if (q < 5 || !isw2) *(u32x4*)(smem + r * 1552 + (j + 16 * q) * 16) = v[q];
      wait_lgkm0();
      bar();
      const int grp = tid >> 4, li = tid & 15, qq = li >> 2, pp = li & 3;
#pragma unroll
      for (int it = 0; it < 6; ++it) {
        if (it < 5 || !isw2) {
          const int pr = it * 16 + grp;
          const int cblk = pr >> 1, half = pr & 1;
          const char* a0 = smem + (8 * half + qq) * 1552 + (16 * cblk + 4 * pp) * 2;
          s16x4 t0 = lds_tr(a0);
          s16x4 t1 = lds_tr(a0 + 4 * 1552);
          s16x8 o = __builtin_shufflevector(t0, t1, 0, 1, 2, 3, 4, 5, 6, 7);
          const int n = n0 + 16 * cblk + li;
          const int cbk = n / nch, rr = n - cbk * nch;
          char* dst = isw2 ? ws + WS_W2T + ((size_t)(cbk * 96 + kg) * 80 + rr) * 32 : ws + WS_W1T + ((size_t)(cbk * 40 + kg) * 96 + rr) * 32;
          *(u32x4*)(dst + half * 16) = __builtin_bit_cast(u32x4, o);
        }
      }
    }
  } else if (bid < 640) {
    const int m = bid >= 600, c0 = (bid - 560 - 40 * m) * 1280;
    u32x4 v[5];
#pragma unroll
    for (int q = 0; q < 5; ++q) v[q] = LD1((const u32x4*)((const char*)(m ? dim : dre) + (size_t)(c0 + tid + 256 * q) * 16));
#pragma unroll
    for (int q = 0; q < 5; ++q) {
      const int id = c0 + tid + 256 * q;
      const int e = id / 80, ch = id - e * 80;
      const int cb = e / 80, row = e - cb * 80;
      *(u32x4*)(ws + WS_WDT + ((size_t)(cb * 20 + m * 10 + (ch >> 3)) * 80 + row) * 128 + ((ch & 7) ^ ((row >> 1) & 7)) * 16) = v[q];
    }
  } else if (bid >= 640 + 256) {
    const int t = ((bid - 640 - 256) >> 3) * 256 + tid;
    const char* a;
    if (t < 384) a = ws + (size_t)t * 65536;
    else if (t < 512) a = (const char*)wre + (size_t)(t - 384) * 65536;
    else if (t < 640) a = (const char*)wim + (size_t)(t - 512) * 65536;
    else if (t < 680) a = (const char*)x + (size_t)(t - 640) * 65536;
    else if (t < 720) a = (const char*)outp + (size_t)(t - 680) * 65536;
    else a = ws;
    unsigned v;
    asm volatile("global_load_dword %0, %1, off" : "=v"(v) : "v"(a) : "memory");
    wait_vm<0>();
    asm volatile("" ::"v"(v));
  } else {
    const int pw = (bid - 640) * 4 + __builtin_amdgcn_readfirstlane(tid >> 6);
    const char* src = (const char*)(pw < 512 ? wre : wim) + (size_t)(pw & 511) * 16384 + lane * 16;
    u32x4 v[16];
#pragma unroll
    for (int i = 0; i < 16; ++i) v[i] = *(const u32x4*)(src + i * 1024);
    float rs[4];
#pragma unroll
    for (int r = 0; r < 4; ++r) {
      float a0 = 0.f, a1 = 0.f;
#pragma unroll
      for (int q = 0; q < 4; ++q)
#pragma unroll
        for (int e = 0; e < 4; ++e) { a0 += lo16(v[4 * r + q][e]); a1 += hi16(v[4 * r + q][e]); }
      rs[r] = a0 + a1;
    }
#pragma unroll
    for (int o = 1; o < 64; o <<= 1)
#pragma unroll
      for (int r = 0; r < 4; ++r) rs[r] += __shfl_xor(rs[r], o, 64);
    if (lane == 0) *(f32x4*)(ws + WS_RSUM + pw * 16) = (f32x4){rs[0], rs[1], rs[2], rs[3]};
  }
}

__device__ __forceinline__ void dma_res_tile(const char* base  , unsigned lds, int w, int lane) {
#pragma unroll
  for (int j = 0; j < 3; ++j) {
    int L = 160 * w + 64 * j + lane;
    if (L > 639) L = 639;
    const int r = L / 10, ch = L - r * 10;
    if (j < 2) dma16(r * (D_ * 2) + ch * 16, base, lds + (160 * w + 64 * j) * 16);
    else dma16h(r * (D_ * 2) + ch * 16, base, lds + (160 * w + 128) * 16);
  }
}

struct SchedP2 { static constexpr char s[] = "MRMRMRMRMRMRMRMRMRMRMRMRMRMRMMMMMM"; };
struct SchedP3 { static constexpr char s[] = "RRMRMRRMRMRRMRMRRMRMMM"; };
struct SchedP4 { static constexpr char s[] = "MRMRMRMMRMRMRMMRMRMMRMRMRMMRMRMRMMRMRMMMMMMMMM"; };
constexpr int cnt_ch(const char* s, char c) { int n = 0; for (; *s; ++s) n += (*s == c); return n; }
static_assert(cnt_ch(SchedP2::s, 'M') == 20 && cnt_ch(SchedP2::s, 'R') == 14, "");
static_assert(cnt_ch(SchedP3::s, 'M') == 10 && cnt_ch(SchedP3::s, 'R') == 12, "");
static_assert(cnt_ch(SchedP4::s, 'M') == 30 && cnt_ch(SchedP4::s, 'R') == 16, "");
constexpr int L_RES = 147456, L_MOD = 159744, L_DUM = L_MOD + 2048, L_TOTAL = L_DUM + 1024;
constexpr int P2_NS = 5, P2_STAGE = 26624;
constexpr int P3_STAGE = 36864;
constexpr int P4_STAGE = 28672, P4_E = 4 * P4_STAGE, P4_ST = P4_E + 4096, P4_RS = P4_ST + 8192;

__global__ __launch_bounds__(512, 2) void mega(const unsigned short* __restrict__ wre,
                                               const unsigned short* __restrict__ wim,
                                               const unsigned short* __restrict__ x,
                                               char* __restrict__ ws,
                                               unsigned short* __restrict__ out) {
  extern __shared__ __attribute__((aligned(16))) char smem[];
  const int tid = threadIdx.x, lane = tid & 63;
  const int wv = __builtin_amdgcn_readfirstlane(tid >> 6);
  const int w = wv & 3;
  const bool prod = wv < 4;
  const int bid = blockIdx.x;
  const int xcd = bid & 7, bi = bid >> 3;
  const int cb = bi & 7, rsel = bi >> 3;
  const int g = lane >> 4, l15 = lane & 15;
  const unsigned lds0 = lds_off(smem);
  const float* mod = (const float*)(smem + L_MOD);
  const int cb3 = (xcd & 1) * 4 + (bi & 3);
  const int t3 = ((xcd >> 1) * 8 + (bi >> 2)) * 64;
  const int cb4 = (xcd & 1) * 8 + (bi & 7), grp4 = (xcd >> 1) * 4 + (bi >> 3);
  const int grp3 = t3 >> 7;

  {
    const int isim = rsel >> 1;
    const int grp2 = xcd * 2 + (rsel & 1);
    const int t0 = grp2 * 128;
    if (prod) {
      const char* rbase = (const char*)(isim ? wim : wre) + (size_t)t0 * (S_ * 2);
      const char* cbase = ws + WS_HT + (size_t)cb * 32 * 10240;
      unsigned roff[4];
#pragma unroll
      for (int j = 0; j < 4; ++j) {
        const int row = 32 * w + 8 * j + (lane >> 3);
        roff[j] = row * (S_ * 2) + 16 * ((lane & 7) ^ ((row >> 1) & 7));
      }
      auto run = [&](auto ncc) {
        constexpr int NC = decltype(ncc)::value;
        const int c0 = (NC == 3) ? w * 3072 : 6144 + (w - 2) * 2048;
        producer<P2_NS, 32, 4 + NC>([&](int ks, int buf) {
          const unsigned lb = lds0 + buf * P2_STAGE;
#pragma unroll
          for (int j = 0; j < 4; ++j) dma16(roff[j], rbase + ks * 128, lb + (32 * w + 8 * j) * 128);
#pragma unroll
          for (int j = 0; j < NC; ++j) dma16(lane * 16, cbase + ks * 10240 + c0 + j * 1024, lb + 16384 + c0 + j * 1024);
        });
      };
      if (w < 2) run(IC<3>{}); else run(IC<2>{});
      bar();
      const char* wdt = ws + WS_WDT + (size_t)cb3 * 204800;
#pragma unroll
      for (int j = 0; j < 5; ++j) dma16(lane * 16, wdt + w * 5120 + j * 1024, lds0 + 16384 + w * 5120 + j * 1024);
#pragma unroll
      for (int j = 0; j < 5; ++j) dma16(lane * 16, wdt + 20480 + w * 5120 + j * 1024, lds0 + P3_STAGE + 16384 + w * 5120 + j * 1024);
    } else {
      {
        const char* pc;
        unsigned len;
        asm volatile("s_getpc_b64 %0\n.Lmega_pf_here:\n\ts_mov_b32 %1, .Lmega_pf_end-.Lmega_pf_here" : "=s"(pc), "=s"(len));
        const unsigned off = (bi * 4 + w) * 1024;
        if (off + 1024 <= len) dma16(lane * 16, (const char*)((uintptr_t)pc & ~(uintptr_t)15) + off, lds0 + L_DUM);
      }
      if (w < 2) {
        int L = 64 * w + lane; if (L > 99) L = 99;
        const int vec = L / 20, part = vec + (vec >= 3);
        dma16(part * 2560 + (vec < 2 ? cb : cb3) * 320 + (L - vec * 20) * 16, ws + WS_MOD, lds0 + L_MOD + w * 1024);
      }
      dma_res_tile((const char*)x + (size_t)t3 * (D_ * 2) + cb3 * 160, lds0 + L_RES, w, lane);
      const int sw0 = (g ^ (l15 >> 1)) * 16, sw1 = ((4 + g) ^ (l15 >> 1)) * 16;
      const int ra = (32 * w + l15) * 128;
      const int ca_ = 16384 + (g >> 1) * 2560 + l15 * 32 + (g & 1) * 16;
      bf16x8 rf[2][2][2], cf[2][2][5];
      f32x4 acc[5][2];
#pragma unroll
      for (int j = 0; j < 5; ++j) { acc[j][0] = (f32x4){0.f, 0.f, 0.f, 0.f}; acc[j][1] = (f32x4){0.f, 0.f, 0.f, 0.f}; }
      const float sgn = isim ? -1.0f : 1.0f;
      const float* rsum = (const float*)(ws + WS_RSUM) + isim * 2048 + t0 + 32 * w + l15;
      const float rsr0 = rsum[0], rsr1 = rsum[16];
      auto ldf = [&](auto par, auto ic, int buf) {
        constexpr int PP = decltype(par)::value, n = decltype(ic)::value, kk = n / 7, m = n % 7;
        const char* lb = smem + buf * P2_STAGE;
        if constexpr (m == 0) rf[PP][kk][0] = lds_frag(lb + ra + (kk ? sw1 : sw0));
        else if constexpr (m < 6) cf[PP][kk][m - 1] = lds_frag(lb + ca_ + kk * 5120 + (m - 1) * 512);
        else rf[PP][kk][1] = lds_frag(lb + ra + (kk ? sw1 : sw0) + 2048);
      };
      auto body = [&](auto par, auto dl, int rd) {
        constexpr int PP = decltype(par)::value, DL = decltype(dl)::value;
        auto fm = [&](auto mc) {
          constexpr int m = decltype(mc)::value, kk = m / 10, i = (m % 10) / 5, j = m % 5;
          acc[j][i] = MFMA16(cf[PP][kk][j], rf[PP][kk][i], acc[j][i]);
        };
        auto fr = [&](auto rc) { ldf(IC<PP ^ 1>{}, rc, rd); };
        run_sched<SchedP2, 0, 0, 0, DL>(fm, fr);
      };
      auto load0 = [&] { static_for<14>([&](auto ic) { ldf(IC<0>{}, ic, 0); }); wait_vm<0>(); };
      consumer<P2_NS, 32>(load0, body);
      f32x4 sa[5], c1[5];
#pragma unroll
      for (int j = 0; j < 5; ++j) {
        sa[j] = *(const f32x4*)(mod + 16 * j + 4 * g);
        c1[j] = *(const f32x4*)(mod + 80 + 16 * j + 4 * g);
      }
      bar();
      char* pq = ws + WS_PQ + ((size_t)(grp2 * 2 + (w >> 1)) * 80 + isim * 40 + cb * 5) * 2048 + (32 * (w & 1) + l15) * 32 + g * 8;
      const float rs0 = rsr0 * sgn, rs1 = rsr1 * sgn;
#pragma unroll
      for (int j = 0; j < 5; ++j) {
        const f32x4 cs = c1[j] * sgn;
        const f32x4 v0 = cs * acc[j][0] + sa[j] * rs0;
        const f32x4 v1 = cs * acc[j][1] + sa[j] * rs1;
        st8u_wt(pq + j * 2048, (u32x2){pk2(v0[0], v0[1]), pk2(v0[2], v0[3])});
        st8u_wt(pq + j * 2048 + 512, (u32x2){pk2(v1[0], v1[1]), pk2(v1[2], v1[3])});
      }
    }
    if (!prod) wait_vm<0>();
    bar();
    if (wv == 4) {
      if (lane == 0) add_count(ws, grp2);
      wait_count(ws, grp3, 16);
    }
    bar();
  }

  {
    const char* wdt = ws + WS_WDT + (size_t)cb3 * 204800;
    if (prod) {
      const char* rbase = ws + WS_PQ + (size_t)(t3 >> 6) * (80 * 2048) + w * 4096;
      producer_seam<10, 4, 5>([&](int ks, int buf) {
#pragma unroll
        for (int j = 0; j < 4; ++j) dma16s(lane * 16, rbase + ks * 16384 + j * 1024, lds0 + buf * P3_STAGE + w * 4096 + j * 1024);
      }, [&](int ks, int buf) {
#pragma unroll
        for (int j = 0; j < 5; ++j)
          dma16(lane * 16, wdt + ks * 20480 + w * 5120 + j * 1024, lds0 + buf * P3_STAGE + 16384 + w * 5120 + j * 1024);
      }, [] {});
      bar();
      const char* w1t = ws + WS_W1T + (size_t)cb4 * 122880;
      dma16(w * 1024 + lane * 16, ws + WS_E, lds0 + P4_E + w * 1024);
#pragma unroll
      for (int s = 0; s < 2; ++s)
#pragma unroll
        for (int j = 0; j < 3; ++j)
          dma16(lane * 16, w1t + s * 12288 + w * 3072 + j * 1024, lds0 + s * P4_STAGE + 16384 + w * 3072 + j * 1024);
    } else {
      const int sw0 = (g ^ (l15 >> 1)) * 16, sw1 = ((4 + g) ^ (l15 >> 1)) * 16;
      const int ra = (g >> 1) * 2048 + (16 * w + l15) * 32 + (g & 1) * 16, ca_ = 16384 + l15 * 128;
      bf16x8 rf[2][2], cf[2][2][5];
      f32x4 acc[5];
#pragma unroll
      for (int j = 0; j < 5; ++j) acc[j] = (f32x4){0.f, 0.f, 0.f, 0.f};
      auto ldf = [&](auto hc, auto ic, int buf) {
        constexpr int H = decltype(hc)::value, n = decltype(ic)::value, kk = n / 6, m = n % 6;
        const char* lb = smem + buf * P3_STAGE;
        if constexpr (m == 0) rf[H][kk] = lds_frag(lb + ra + H * 8192 + kk * 4096);
        else cf[H][kk][m - 1] = lds_frag(lb + (kk ? sw1 : sw0) + ca_ + H * 10240 + (m - 1) * 2048);
      };
      auto half = [&](auto par, auto dl, int buf) {
        constexpr int PP = decltype(par)::value, DL = decltype(dl)::value;
        auto fm = [&](auto mc) {
          constexpr int m = decltype(mc)::value, kk = m / 5, j = m % 5;
          acc[j] = MFMA16(cf[PP][kk][j], rf[PP][kk], acc[j]);
        };
        auto fr = [&](auto rc) { ldf(IC<PP ^ 1>{}, rc, buf); };
        run_sched<SchedP3, 0, 0, 0, DL>(fm, fr);
      };
      char* res = smem + L_RES + (16 * w + l15) * 160 + g * 8;
      u32x2 xr[5];
      f32x4 ga[5], c1[5];
#pragma unroll
      for (int j = 0; j < 5; ++j) {
        xr[j] = *(const u32x2*)(res + j * 32);
        ga[j] = *(const f32x4*)(mod + 160 + 16 * j + 4 * g);
        c1[j] = *(const f32x4*)(mod + 240 + 16 * j + 4 * g);
      }
      auto load0 = [&] { static_for<12>([&](auto ic) { ldf(IC<0>{}, ic, 0); }); };
      consumer_half<10>(load0, half);
      bar();
      char* xc = ws + WS_XC + ((size_t)(t3 >> 6) * 40 + cb3 * 5) * 2048 + (16 * w + l15) * 32 + g * 8;
      float xf[20];
      float s = 0.f;
#pragma unroll
      for (int j = 0; j < 5; ++j) {
        const f32x4 y = acc[j];
        const u32x2 o = {pk2(lo16(xr[j][0]) + ga[j][0] * y[0], hi16(xr[j][0]) + ga[j][1] * y[1]),
                         pk2(lo16(xr[j][1]) + ga[j][2] * y[2], hi16(xr[j][1]) + ga[j][3] * y[3])};
        xr[j] = o;
        xf[4 * j] = lo16(o[0]); xf[4 * j + 1] = hi16(o[0]); xf[4 * j + 2] = lo16(o[1]); xf[4 * j + 3] = hi16(o[1]);
        st8u_wt(xc + j * 2048, (u32x2){pk2(xf[4 * j] * c1[j][0], xf[4 * j + 1] * c1[j][1]), pk2(xf[4 * j + 2] * c1[j][2], xf[4 * j + 3] * c1[j][3])});
        s += (xf[4 * j] + xf[4 * j + 1]) + (xf[4 * j + 2] + xf[4 * j + 3]);
      }
      s += __shfl_xor(s, 16, 64);
      s += __shfl_xor(s, 32, 64);
      const float ml = s * (1.0f / 80.0f);
      float m2 = 0.f;
#pragma unroll
      for (int e = 0; e < 20; ++e) { float d = xf[e] - ml; m2 += d * d; }
      m2 += __shfl_xor(m2, 16, 64);
      m2 += __shfl_xor(m2, 32, 64);
      if (lane < 16) st8_wt(ws + WS_ST + ((size_t)(t3 >> 6) * 8 + cb3) * 512 + (16 * w + l15) * 8, (f32x2){s, m2});
#pragma unroll
      for (int j = 0; j < 5; ++j) *(u32x2*)(res + j * 32) = xr[j];
      wait_vm<0>();
    }
    bar();
    if (wv == 4) {
      if (lane == 0) add_count(ws, 16 + grp3);
      wait_count(ws, 16 + grp4, 16);
    }
    bar();
  }

  {
    const int t0 = grp4 * 128;
    const char* w1t = ws + WS_W1T + (size_t)cb4 * 122880;
    if (prod) {
      const char* rbase = ws + WS_XC + (size_t)(grp4 * 2 + (w >> 1)) * (40 * 2048) + (w & 1) * 4096;
      dma16s(w * 2048 + lane * 16, ws + WS_ST + (size_t)t0 * 64, lds0 + P4_ST + w * 2048);
      dma16s(w * 2048 + 1024 + lane * 16, ws + WS_ST + (size_t)t0 * 64, lds0 + P4_ST + w * 2048 + 1024);
      producer_seam<10, 4, 3>([&](int ks, int buf) {
#pragma unroll
        for (int j = 0; j < 4; ++j) dma16s(lane * 16, rbase + ks * 8192 + j * 1024, lds0 + buf * P4_STAGE + w * 4096 + j * 1024);
      }, [&](int ks, int buf) {
#pragma unroll
        for (int j = 0; j < 3; ++j)
          dma16(lane * 16, w1t + ks * 12288 + w * 3072 + j * 1024, lds0 + buf * P4_STAGE + 16384 + w * 3072 + j * 1024);
      }, [&] {
        if (tid < 128) {
          const char* sp = smem + P4_ST + (tid >> 6) * 4096 + (tid & 63) * 8;
          f32x2 pp[8];
#pragma unroll
          for (int e = 0; e < 8; ++e) pp[e] = *(const f32x2*)(sp + e * 512);
          const float mean = (((pp[0][0] + pp[1][0]) + (pp[2][0] + pp[3][0])) + ((pp[4][0] + pp[5][0]) + (pp[6][0] + pp[7][0]))) * (1.0f / D_);
          float m2 = ((pp[0][1] + pp[1][1]) + (pp[2][1] + pp[3][1])) + ((pp[4][1] + pp[5][1]) + (pp[6][1] + pp[7][1]));
          float dd = 0.f;
#pragma unroll
          for (int e = 0; e < 8; ++e) { const float d = pp[e][0] * (1.0f / 80.0f) - mean; dd += d * d; }
          m2 += 80.0f * dd;
          const float rden = 1.0f / bfr(sqrtf(bfr(bfr(m2 * (1.0f / D_)) + 1e-5f)));
          *(f32x2*)(smem + P4_RS + tid * 8) = (f32x2){bfr(mean), rden};
        }
        wait_lgkm0();
      });
      bar();
      const char* w2t = ws + WS_W2T + (size_t)cb3 * F_ * 160;
#pragma unroll
      for (int s = 0; s < 2; ++s)
#pragma unroll
        for (int j = 0; j < 5; ++j)
          dma16(lane * 16, w2t + s * 20480 + w * 5120 + j * 1024, lds0 + s * P3_STAGE + 16384 + w * 5120 + j * 1024);
    } else {
      const int wm = w & 1, wn = w >> 1;
      const int ca_ = 16384 + (g >> 1) * 3072 + (48 * wn + l15) * 32 + (g & 1) * 16;
      const int ra = wm * 8192 + (g >> 1) * 2048 + l15 * 32 + (g & 1) * 16;
      int ea = P4_E + (l15 < 2 ? l15 : 2) * 1280 + g * 16;
      bf16x8 rf[2][2][5], cf[2][2][3];
      f32x4 acc[3][5];
#pragma unroll
      for (int j = 0; j < 3; ++j)
#pragma unroll
        for (int i = 0; i < 5; ++i) acc[j][i] = (f32x4){0.f, 0.f, 0.f, 0.f};
      auto ldf = [&](auto par, auto ic, int buf) {
        constexpr int PP = decltype(par)::value, n = decltype(ic)::value, kk = n / 8, m = n % 8;
        const char* lb = smem + buf * P4_STAGE;
        if constexpr (m == 0) rf[PP][kk][0] = lds_frag(lb + ra + kk * 4096);
        else if constexpr (m < 4) cf[PP][kk][m - 1] = lds_frag(lb + ca_ + kk * 6144 + (m - 1) * 512);
        else if constexpr (m < 7) rf[PP][kk][m - 3] = lds_frag(lb + ra + kk * 4096 + (m - 3) * 512);
        else {
          rf[PP][kk][4] = lds_frag(smem + ea + kk * 64);
          if constexpr (kk == 1) ea += 128;
        }
      };
      auto body = [&](auto par, auto dl, int rd) {
        constexpr int PP = decltype(par)::value, DL = decltype(dl)::value;
        auto fm = [&](auto mc) {
          constexpr int m = decltype(mc)::value, kk = m / 15, i = (m % 15) / 3, j = m % 3;
          acc[j][i] = MFMA16(cf[PP][kk][j], rf[PP][kk][i], acc[j][i]);
        };
        auto fr = [&](auto rc) { ldf(IC<PP ^ 1>{}, rc, rd); };
        run_sched<SchedP4, 0, 0, 0, DL>(fm, fr);
      };
      auto load0 = [&] { static_for<16>([&](auto ic) { ldf(IC<0>{}, ic, 0); }); };
      consumer<4, 10>(load0, body);
      f32x2 rsv[4];
#pragma unroll
      for (int i = 0; i < 4; ++i) rsv[i] = *(const f32x2*)(smem + P4_RS + (64 * wm + 16 * i + l15) * 8);
      wait_lgkm0();
      bar();
      f32x4 u[3], v[3];
#pragma unroll
      for (int j = 0; j < 3; ++j)
#pragma unroll
        for (int e = 0; e < 4; ++e) {
          const int a = __float_as_int(acc[j][4][e]);
          u[j][e] = __int_as_float(__builtin_amdgcn_update_dpp(0, a, 0x150, 0xf, 0xf, false));
          v[j][e] = __int_as_float(__builtin_amdgcn_update_dpp(0, a, 0x151, 0xf, 0xf, false));
        }
      char* h1 = ws + WS_H1 + ((size_t)(grp4 * 2 + wm) * 96 + cb4 * 6 + 3 * wn) * 2048 + l15 * 32 + g * 8;
#pragma unroll
      for (int i = 0; i < 4; ++i) {
        const f32x2 rs = rsv[i];
#pragma unroll
        for (int j = 0; j < 3; ++j) {
          const f32x4 hv = (acc[j][i] - u[j] * rs[0]) * rs[1] + v[j];
          st8u_wt(h1 + j * 2048 + i * 512,
                  (u32x2){pk2(fmaxf(hv[0], 0.f), fmaxf(hv[1], 0.f)), pk2(fmaxf(hv[2], 0.f), fmaxf(hv[3], 0.f))});
        }
      }
      wait_vm<0>();
    }
    bar();
    if (wv == 4) {
      if (lane == 0) add_count(ws, 32 + grp4);
      wait_count(ws, 32 + grp3, 16);
    }
    bar();
  }

  {
    const char* w2t = ws + WS_W2T + (size_t)cb3 * F_ * 160;
    if (prod) {
      const char* rbase = ws + WS_H1 + (size_t)(t3 >> 6) * (96 * 2048) + w * 4096;
      producer_seam<12, 4, 5>([&](int ks, int buf) {
#pragma unroll
        for (int j = 0; j < 4; ++j) dma16s(lane * 16, rbase + ks * 16384 + j * 1024, lds0 + buf * P3_STAGE + w * 4096 + j * 1024);
      }, [&](int ks, int buf) {
#pragma unroll
        for (int j = 0; j < 5; ++j)
          dma16(lane * 16, w2t + ks * 20480 + w * 5120 + j * 1024, lds0 + buf * P3_STAGE + 16384 + w * 5120 + j * 1024);
      }, [] {});
    } else {
      const int ra = (g >> 1) * 2048 + (16 * w + l15) * 32 + (g & 1) * 16;
      const int ca_ = 16384 + (g >> 1) * 2560 + l15 * 32 + (g & 1) * 16;
      bf16x8 rf[2][2], cf[2][2][5];
      f32x4 acc[5];
#pragma unroll
      for (int j = 0; j < 5; ++j) acc[j] = (f32x4){0.f, 0.f, 0.f, 0.f};
      auto ldf = [&](auto hc, auto ic, int buf) {
        constexpr int H = decltype(hc)::value, n = decltype(ic)::value, kk = n / 6, m = n % 6;
        const char* lb = smem + buf * P3_STAGE;
        if constexpr (m == 0) rf[H][kk] = lds_frag(lb + ra + H * 8192 + kk * 4096);
        else cf[H][kk][m - 1] = lds_frag(lb + ca_ + H * 10240 + kk * 5120 + (m - 1) * 512);
      };
      auto half = [&](auto par, auto dl, int buf) {
        constexpr int PP = decltype(par)::value, DL = decltype(dl)::value;
        auto fm = [&](auto mc) {
          constexpr int m = decltype(mc)::value, kk = m / 5, j = m % 5;
          acc[j] = MFMA16(cf[PP][kk][j], rf[PP][kk], acc[j]);
        };
        auto fr = [&](auto rc) { ldf(IC<PP ^ 1>{}, rc, buf); };
        run_sched<SchedP3, 0, 0, 0, DL>(fm, fr);
      };
      char* res = smem + L_RES + (16 * w + l15) * 160 + g * 8;
      u32x2 xr[5];
      f32x4 gb[5];
#pragma unroll
      for (int j = 0; j < 5; ++j) {
        xr[j] = *(const u32x2*)(res + j * 32);
        gb[j] = *(const f32x4*)(mod + 320 + 16 * j + 4 * g);
      }
      auto load0 = [&] { static_for<12>([&](auto ic) { ldf(IC<0>{}, ic, 0); }); };
      consumer_half<12>(load0, half);
#pragma unroll
      for (int j = 0; j < 5; ++j) {
        const f32x4 y = acc[j];
        *(u32x2*)(res + j * 32) = (u32x2){pk2(lo16(xr[j][0]) + gb[j][0] * y[0], hi16(xr[j][0]) + gb[j][1] * y[1]),
                                          pk2(lo16(xr[j][1]) + gb[j][2] * y[2], hi16(xr[j][1]) + gb[j][3] * y[3])};
      }
      wait_vm<0>();
    }
    wait_lgkm0();
    bar();
    for (int id = tid; id < 640; id += 512) {
      const int row = id / 10, c = id - row * 10;
      *(u32x4*)((char*)out + (size_t)(t3 + row) * (D_ * 2) + cb3 * 160 + c * 16) = *(const u32x4*)(smem + L_RES + row * 160 + c * 16);
    }
  }
  asm volatile(".Lmega_pf_end:");
}

extern "C" void kernel_launch(void* const* d_in, const int* in_sizes, int n_in,
                              void* d_out, int out_size, void* d_ws, size_t ws_size,
                              hipStream_t stream) {
  const unsigned short* x    = (const unsigned short*)d_in[0];
  const unsigned short* wsre = (const unsigned short*)d_in[1];
  const unsigned short* wsim = (const unsigned short*)d_in[2];
  const unsigned short* wdre = (const unsigned short*)d_in[3];
  const unsigned short* wdim = (const unsigned short*)d_in[4];
  const unsigned short* w1   = (const unsigned short*)d_in[6];
  const unsigned short* w2   = (const unsigned short*)d_in[7];
  const unsigned short* c    = (const unsigned short*)d_in[8];
  const unsigned short* wmod = (const unsigned short*)d_in[9];
  char* ws = (char*)d_ws;
  unsigned short* out = (unsigned short*)d_out;
  (void)hipFuncSetAttribute((const void*)mega, hipFuncAttributeMaxDynamicSharedMemorySize, L_TOTAL);
  k1_ln_mod<<<640 + 256 + 24, 256, 0, stream>>>(x, c, wmod, wsre, wsim, w2, wdre, wdim, w1, out, ws);
  mega<<<256, 512, L_TOTAL, stream>>>(wsre, wsim, x, ws, out);
}
```
